```python
import torch

def _softmax(x, axis=-1):
    return torch.softmax(x, dim=axis)
def _gelu(x):
    return torch.nn.functional.gelu(x, approximate="tanh")
def _silu(x):
    return torch.nn.functional.silu(x)
def _sigmoid(x):
    return torch.sigmoid(x)
def _layernorm(x, axis=-1, eps=1e-5):
    m = x.mean(dim=axis, keepdim=True)
    v = x.var(dim=axis, keepdim=True, unbiased=False)
    return (x - m) / torch.sqrt(v + eps)
def _scatter_add(tgt, idx, src, axis=0):
    return tgt.clone().index_add_(axis, idx, src)
def _segment_sum(src, seg, n):
    out = torch.zeros((n,) + src.shape[1:], dtype=src.dtype)
    return out.index_add_(0, seg, src)
def _pad(x, pw):
    flat = tuple(v for pair in reversed(pw) for v in pair)
    return torch.nn.functional.pad(x, flat)

kernel_name = 'gated_mix__mha_block__sliding_local_dilated__prompt'

def setup_inputs():

    _w = (lambda a: (lambda t: t.to(torch.bfloat16) if t.is_floating_point() else t)(torch.as_tensor(a)))
    def _A_setup():
        B, S, D, H = 1, 2048, 448, 16
        return {
            "x": _w((lambda *s: torch.randn(*s, dtype=torch.bfloat16) * 0.05)(B, S, D)),
            "wqkv": _w((lambda *s: torch.randn(*s, dtype=torch.bfloat16) * 1.6)(D, 3 * D)),
            "wo": _w((lambda *s: torch.randn(*s, dtype=torch.bfloat16) * 0.1)(D, D)),
        }
    _A_in = _A_setup()

    def _B_setup():
        B, S, D = 1, 2048, 448
        return {
            "x": _w((lambda *s: torch.randn(*s, dtype=torch.bfloat16) * 0.05)(B, S, D)),
            "wqkv": _w((lambda *s: torch.randn(*s, dtype=torch.bfloat16) * 1.6)(D, 3 * D)),
            "wo": _w((lambda *s: torch.randn(*s, dtype=torch.bfloat16) * 0.05)(D, D)),
        }
    _B_in = _B_setup()

    return {
        "A_x": _A_in["x"],
        "A_wqkv": _A_in["wqkv"],
        "A_wo": _A_in["wo"],
        "B_x": _B_in["x"],
        "B_wqkv": _B_in["wqkv"],
        "B_wo": _B_in["wo"],
        "gate_w": _w((lambda *s: torch.randn(*s, dtype=torch.bfloat16) * 0.05)(448) * 16.0),
    }

def reference(A_x, A_wqkv, A_wo, B_x, B_wqkv, B_wo, gate_w):
    def _A_ref(x, wqkv, wo):
        B, S, D = x.shape
        H = 16
        Dh = D // H
        qkv = (lambda x, *d: x.permute(*d))((x @ wqkv).reshape(B, S, 3, H, Dh), 2, 0, 3, 1, 4)
        q, k, v = qkv[0], qkv[1], qkv[2]
        scores = torch.einsum("bhqd,bhkd->bhqk", q, k) * (Dh ** -0.5)
        p = _softmax(scores, axis=-1)
        o = torch.einsum("bhqk,bhkd->bhqd", p, v)
        return (lambda x, *d: x.permute(*d))(o, 0, 2, 1, 3).reshape(B, S, D) @ wo

    def _B_ref(x, wqkv, wo):
        B, S, D = x.shape
        H = 16; Dh = D // H
        W, dil = 8, 2
        qkv = (lambda x, *d: x.permute(*d))((x @ wqkv).reshape(B, S, 3, H, Dh), 2, 0, 3, 1, 4)
        q, k, v = qkv[0], qkv[1], qkv[2]
        scale = Dh ** -0.5
        pos = torch.arange(S)
        off = torch.arange(W) * dil
        idx = pos[:, None] - off[None, :]
        mask = idx >= 0
        idx_c = torch.where(mask, idx, 0).reshape(-1)
        kg = k[:, :, idx_c, :].reshape(B, H, S, W, Dh)
        vg = v[:, :, idx_c, :].reshape(B, H, S, W, Dh)
        logits = torch.einsum("bhsd,bhswd->bhsw", q, kg) * scale
        logits = torch.where(mask[None, None, :, :], logits, -1e9)
        attn = _softmax(logits, axis=-1)
        o = torch.einsum("bhsw,bhswd->bhsd", attn, vg)
        return (lambda x, *d: x.permute(*d))(o, 0, 2, 1, 3).reshape(B, S, D) @ wo

    x = A_x
    g = _sigmoid((x * gate_w).sum(-1, keepdims=True))
    a = _A_ref(x, A_wqkv, A_wo)
    b = _B_ref(x, B_wqkv, B_wo)
    return g * a + (1 - g) * b
```

```cpp
#include <hip/hip_runtime.h>
#include <hip/hip_bf16.h>
#include <stdint.h>

typedef short bf16x8 __attribute__((ext_vector_type(8)));
typedef short s16x4 __attribute__((ext_vector_type(4)));
typedef float f32x16 __attribute__((ext_vector_type(16)));
typedef float f32x2 __attribute__((ext_vector_type(2)));
typedef float f32x4 __attribute__((ext_vector_type(4)));
typedef unsigned u32x4 __attribute__((ext_vector_type(4)));
typedef unsigned u32x2 __attribute__((ext_vector_type(2)));
typedef __bf16 bf2_t __attribute__((ext_vector_type(2)));
#define LAS __attribute__((address_space(3)))
#define GAS __attribute__((address_space(1)))
#define DI __device__ __forceinline__
#define MFMA(a, b, c) __builtin_amdgcn_mfma_f32_32x32x16_bf16((a), (b), (c), 0, 0, 0)

constexpr int SEQ = 2048, DM = 448, NH = 16, DH = 28, NQKV = 1344;
constexpr float QSCALE = 0.18898223650461362f * 1.4426950408889634f;

constexpr size_t WS_KVA = 0;
constexpr size_t WS_KVB = WS_KVA + 16u * 64 * 4096;
constexpr size_t WS_QA = WS_KVB + 16u * 64 * 4096;
constexpr size_t WS_QB = WS_QA + 16u * 64 * 2048;
constexpr size_t WS_OA = WS_QB + 16u * 64 * 2048;
constexpr size_t WS_OB = WS_OA + 16u * 64 * 2048;
constexpr size_t WS_G = WS_OB + 16u * 64 * 2048;

DI unsigned cvtpk(float lo, float hi) {
    f32x2 v = {lo, hi};
    return __builtin_bit_cast(unsigned, __builtin_convertvector(v, bf2_t));
}
DI void glds16(unsigned voff, const void* sbase, unsigned lds_dst) {
    unsigned sv;
    asm volatile("s_mov_b32 %0, m0\n\ts_mov_b32 m0, %3\n\ts_nop 0\n\tglobal_load_lds_dwordx4 %1, %2\n\ts_mov_b32 m0, %0"
                 : "=&s"(sv) : "v"(voff), "s"(sbase), "s"(lds_dst) : "memory");
}
#define WAIT_BAR(N) asm volatile("s_waitcnt vmcnt(" #N ") lgkmcnt(0)\n\ts_barrier" ::: "memory")
DI bf16x8 ldsr128(unsigned a) { return *(const LAS bf16x8*)(uintptr_t)a; }
typedef short v4i16_t __attribute__((ext_vector_type(4)));
DI s16x4 ldstr(unsigned a) { return __builtin_bit_cast(s16x4, __builtin_amdgcn_ds_read_tr16_b64_v4i16((LAS v4i16_t*)(uintptr_t)a)); }
DI bf16x8 wfrag(unsigned a0, unsigned a1) { s16x4 lo = ldstr(a0), hi = ldstr(a1); return __builtin_shufflevector(lo, hi, 0, 1, 2, 3, 4, 5, 6, 7); }
DI float swap_max(float m) {
    auto rr = __builtin_amdgcn_permlane32_swap(__float_as_uint(m), __float_as_uint(m), false, false);
    return __builtin_fmaxf(__uint_as_float(rr[0]), __uint_as_float(rr[1]));
}
DI float from_hi(float v) {
    auto rr = __builtin_amdgcn_permlane32_swap(__float_as_uint(v), __float_as_uint(v), false, false);
    return __uint_as_float(rr[1]);
}

constexpr unsigned L_X = 0;
constexpr unsigned L_W1 = 96 * 1024;
constexpr unsigned L_GT = 152 * 1024;
constexpr unsigned L_X6 = L_W1;
constexpr unsigned L_W26 = L_W1 + 16 * 1024;
constexpr unsigned LDS_BYTES = 160 * 1024;

DI void pack16(const f32x16& a, u32x4& s0, u32x4& s1) {
    s0 = (u32x4){cvtpk(a[0], a[1]), cvtpk(a[2], a[3]), cvtpk(a[4], a[5]), cvtpk(a[6], a[7])};
    s1 = (u32x4){cvtpk(a[8], a[9]), cvtpk(a[10], a[11]), cvtpk(a[12], a[13]), cvtpk(a[14], a[15])};
}

__global__ __launch_bounds__(512, 2) void k_qkv(const char* __restrict__ x, const char* __restrict__ wa, const char* __restrict__ wb,
                                                const unsigned short* __restrict__ gw, char* __restrict__ ws) {
    extern __shared__ __attribute__((aligned(16))) char smem[];
    const unsigned lds0 = (unsigned)(uintptr_t)(LAS char*)smem;
    const int tid = threadIdx.x, lane = tid & 63, w = __builtin_amdgcn_readfirstlane(tid >> 6), t = w & 3, g = w >> 2;
    const int bid = blockIdx.x, xg = bid & 7, idx = bid >> 3;
    const int h = (xg >> 1) * 4 + (idx & 3), j = (xg & 1) * 8 + (idx >> 2);
    const int r32 = lane & 31, hi = lane >> 5;
    const int xrow = 16 * w + (lane >> 3), fx = (xrow >> 1) & 7;
    const unsigned vx0 = xrow * 896 + (((lane & 7) ^ fx) << 4), vx1 = (xrow + 8) * 896 + (((lane & 7) ^ fx ^ 4) << 4);
    const unsigned vw = (lane >> 2) * 2688 + (lane & 3) * 16;
    const char* xb = x + (size_t)j * 128 * 896;
    const int cw = (h * 28 - 4 * (h & 1)) * 2;
    const char* w1b = wa + (1 + g) * 896 + cw + (size_t)(16 * t) * 2688;
    const int pi = w >> 1;
    const char* w2b = (pi == 0 ? wa : wb + (pi - 1) * 896) + cw + (size_t)(32 * (w & 1)) * 2688;
#define DMA_X(c, dst) do { glds16(vx0, xb + (c) * 128, lds0 + (dst) + w * 2048); glds16(vx1, xb + (c) * 128, lds0 + (dst) + w * 2048 + 1024); } while (0)
#define DMA_W1(c) glds16(vw, w1b + (size_t)(c) * 64 * 2688, lds0 + L_W1 + (c) * 8192 + w * 1024)
#define DMA_W2(c, dst) do { glds16(vw, w2b + (size_t)(c) * 64 * 2688, lds0 + (dst) + w * 2048); glds16(vw, w2b + (size_t)((c) * 64 + 16) * 2688, lds0 + (dst) + w * 2048 + 1024); } while (0)
    unsigned gv;
    asm volatile("global_load_ushort %0, %1, %2" : "=v"(gv) : "v"((tid < DM ? tid : DM - 1) * 2), "s"(gw) : "memory");
#pragma unroll
    for (int c = 0; c < 6; ++c) { DMA_X(c, L_X + c * 16384); DMA_W1(c); }
    DMA_W1(6);
    const int fr = (r32 >> 1) & 7;
    unsigned xa[4];
#pragma unroll
    for (int ks = 0; ks < 4; ++ks) xa[ks] = lds0 + (4 * t + (r32 >> 3)) * 1024 + (r32 & 7) * 128 + (((2 * ks + hi) ^ fr) << 4);
    const int q4 = (lane & 15) >> 2, p4 = lane & 3, ch = (lane >> 4) & 1;
    const unsigned wlane = (8 * hi + q4) * 64 + ch * 32 + p4 * 8 + (h & 1) * 8;
    bf16x8 xf[28];
    f32x16 acc0 = {}, acc1 = {};
#define STEP1(c, XOFF) do { \
        const unsigned wa_ = lds0 + L_W1 + (c) * 8192 + g * 4096 + wlane; \
        _Pragma("unroll") for (int ks = 0; ks < 4; ++ks) xf[4 * (c) + ks] = ldsr128(xa[ks] + (XOFF)); \
        if (g == 0) { _Pragma("unroll") for (int ks = 0; ks < 4; ++ks) acc0 = MFMA(wfrag(wa_ + ks * 1024, wa_ + ks * 1024 + 256), xf[4 * (c) + ks], acc0); } \
        else        { _Pragma("unroll") for (int ks = 0; ks < 4; ++ks) acc0 = MFMA(xf[4 * (c) + ks], wfrag(wa_ + ks * 1024, wa_ + ks * 1024 + 256), acc0); } \
    } while (0)
    WAIT_BAR(16);
    asm volatile("" : "+v"(gv));
    if (tid < DM) { u32x2 e = {gv, 0u}; *(LAS u32x2*)(uintptr_t)(lds0 + L_GT + tid * 8) = e; }
    STEP1(0, L_X);
    WAIT_BAR(13); DMA_W2(0, L_X); STEP1(1, L_X + 16384);
    WAIT_BAR(12); DMA_X(6, L_X6); DMA_W2(1, L_X + 16384); STEP1(2, L_X + 2 * 16384);
    WAIT_BAR(13); DMA_W2(2, L_X + 2 * 16384); STEP1(3, L_X + 3 * 16384);
    WAIT_BAR(12); DMA_W2(3, L_X + 3 * 16384); DMA_W2(6, L_W26); STEP1(4, L_X + 4 * 16384);
    WAIT_BAR(13); DMA_W2(4, L_X + 4 * 16384); STEP1(5, L_X + 5 * 16384);
    WAIT_BAR(10); DMA_W2(5, L_X + 5 * 16384); STEP1(6, L_X6);
    const int tile = 4 * j + t;
    {
        u32x4 s0, s1; pack16(acc0, s0, s1);
        if (g == 0) { if (hi) { s1[2] = 0x00003F80u; s1[3] = 0u; } }
        else if (r32 == 28) { s0 = (u32x4){0x3F803F80u, 0x3F803F80u, 0x3F803F80u, 0x3F803F80u}; s1 = s0; }
        char* dst = ws + WS_KVA + ((size_t)(h * 64 + tile) * 4096) + g * 2048 + lane * 16;
        *(GAS u32x4*)dst = s0; *(GAS u32x4*)(dst + 1024) = s1;
    }
    acc0 = (f32x16){};
    const bool spec = (g == 0) && ch == 1 && p4 == 3;
    const unsigned gstride = spec ? 32u : 256u;
#define STEP2(c, WOFF) do { \
        const unsigned wb_ = lds0 + (WOFF) + g * 8192 + wlane; \
        unsigned a_ = spec ? (lds0 + L_GT + 512 * (c) + 64 * hi + 8 * q4) : wb_; \
        if (g == 0) { _Pragma("unroll") for (int ks = 0; ks < 4; ++ks) { \
                const unsigned a1_ = a_ + gstride; \
                acc0 = MFMA(wfrag(a_, a1_), xf[4 * (c) + ks], acc0); a_ += 4 * gstride; \
                acc1 = MFMA(wfrag(wb_ + 4096 + ks * 1024, wb_ + 4096 + ks * 1024 + 256), xf[4 * (c) + ks], acc1); } } \
        else { _Pragma("unroll") for (int ks = 0; ks < 4; ++ks) { \
                acc0 = MFMA(wfrag(wb_ + ks * 1024, wb_ + ks * 1024 + 256), xf[4 * (c) + ks], acc0); \
                acc1 = MFMA(xf[4 * (c) + ks], wfrag(wb_ + 4096 + ks * 1024, wb_ + 4096 + ks * 1024 + 256), acc1); } } \
    } while (0)
    STEP2(0, L_X);
    WAIT_BAR(6);
    STEP2(1, L_X + 16384); STEP2(2, L_X + 2 * 16384); STEP2(3, L_X + 3 * 16384);
    WAIT_BAR(0);
    STEP2(4, L_X + 4 * 16384); STEP2(5, L_X + 5 * 16384); STEP2(6, L_W26);
    if (g == 0) {
        if (h == 0 && hi) *(GAS float*)(ws + WS_G + (size_t)(tile * 32 + r32) * 4) = acc0[12];
#pragma unroll
        for (int i = 0; i < 16; ++i) { acc0[i] *= QSCALE; acc1[i] *= QSCALE; }
        u32x4 s0, s1; pack16(acc0, s0, s1);
        if (hi) { s1[2] = 0u; s1[3] = 0u; }
        char* dst = ws + WS_QA + ((size_t)(h * 64 + tile) * 2048) + lane * 16;
        *(GAS u32x4*)dst = s0; *(GAS u32x4*)(dst + 1024) = s1;
        pack16(acc1, s0, s1);
        if (hi) { s1[2] = 0u; s1[3] = 0u; }
        dst = ws + WS_QB + ((size_t)(h * 64 + tile) * 2048) + lane * 16;
        *(GAS u32x4*)dst = s0; *(GAS u32x4*)(dst + 1024) = s1;
    } else {
        u32x4 s0, s1; pack16(acc0, s0, s1);
        if (hi) { s1[2] = 0x00003F80u; s1[3] = 0u; }
        char* dst = ws + WS_KVB + ((size_t)(h * 64 + tile) * 4096) + lane * 16;
        *(GAS u32x4*)dst = s0; *(GAS u32x4*)(dst + 1024) = s1;
        pack16(acc1, s0, s1);
        if (r32 == 28) { s0 = (u32x4){0x3F803F80u, 0x3F803F80u, 0x3F803F80u, 0x3F803F80u}; s1 = s0; }
        *(GAS u32x4*)(dst + 2048) = s0; *(GAS u32x4*)(dst + 3072) = s1;
    }
}

constexpr unsigned L_RING = 0;
constexpr unsigned L_CMB = 128 * 1024;

DI bf16x8 gld128(const char* p) { return *(const GAS bf16x8*)p; }
DI bf16x8 asbf(u32x4 v) { return __builtin_bit_cast(bf16x8, v); }
DI float ex2(float v) { return __builtin_amdgcn_exp2f(v); }

DI void store_o(const f32x16& o, float sc, char* dst, int hi) {
    f32x16 v;
#pragma unroll
    for (int i = 0; i < 16; ++i) v[i] = o[i] * sc;
    u32x4 s0, s1; pack16(v, s0, s1);
    if (hi) { s1[2] = 0u; s1[3] = 0u; }
    *(GAS u32x4*)dst = s0; *(GAS u32x4*)(dst + 1024) = s1;
}

__global__ __launch_bounds__(512, 2) void k_attn(char* __restrict__ ws) {
    extern __shared__ __attribute__((aligned(16))) char smem[];
    const unsigned lds0 = (unsigned)(uintptr_t)(LAS char*)smem;
    const int tid = threadIdx.x, lane = tid & 63, w = __builtin_amdgcn_readfirstlane(tid >> 6), t = w & 3, g = w >> 2;
    const int bid = blockIdx.x, xg = bid & 7, idx = bid >> 3;
    const int h = (xg >> 1) * 4 + (idx & 3), j = (xg & 1) * 8 + (idx >> 2);
    const int r32 = lane & 31, hi = lane >> 5;
    const int tile = 4 * j + t;
    const char* kvb = ws + WS_KVA + (size_t)h * 262144 + w * 4096;
    const unsigned vl = lane * 16;
#define DMA_KV(gr) do { _Pragma("unroll") for (int i_ = 0; i_ < 4; ++i_) glds16(vl, kvb + (size_t)(gr) * 32768 + i_ * 1024, lds0 + L_RING + ((gr) & 3) * 32768 + w * 4096 + i_ * 1024); } while (0)
    DMA_KV(0); DMA_KV(1); DMA_KV(2);
    const char* qp = ws + WS_QA + (size_t)(h * 64 + tile) * 2048 + vl;
    bf16x8 q0 = gld128(qp); u32x4 q1 = __builtin_bit_cast(u32x4, gld128(qp + 1024));
    const float glog = *(const GAS float*)(ws + WS_G + (size_t)(tile * 32 + r32) * 4);
    const float gate = 1.0f / (1.0f + __expf(-glog));
    if (g == 0) {
        const char* qb = ws + WS_QB + (size_t)(h * 64 + tile) * 2048 + vl;
        const char* kc = ws + WS_KVB + (size_t)(h * 64 + tile) * 4096 + vl;
        const char* kp = tile > 0 ? kc - 4096 : kc;
        const bf16x8 bq0 = gld128(qb), bq1 = gld128(qb + 1024);
        const bf16x8 k0 = gld128(kc), k1 = gld128(kc + 1024), v0 = gld128(kc + 2048), v1 = gld128(kc + 3072);
        const bf16x8 pk0 = gld128(kp), pk1 = gld128(kp + 1024), pv1 = gld128(kp + 3072);
        const unsigned long long M = (0x5555ull << (r32 + 18)) >> (4 * hi);
        const unsigned mlo = tile > 0 ? (unsigned)M : 0u, mhi = (unsigned)(M >> 32);
        f32x16 cb, pb;
#pragma unroll
        for (int r = 0; r < 16; ++r) { const int c = (r & 3) + 8 * (r >> 2);
            cb[r] = ((mhi >> c) & 1u) ? 0.f : -__builtin_inff();
            pb[r] = ((mlo >> c) & 1u) ? 0.f : -__builtin_inff(); }
        f32x16 sc = MFMA(k0, bq0, cb); sc = MFMA(k1, bq1, sc);
        f32x16 sp = MFMA(pk0, bq0, pb); sp = MFMA(pk1, bq1, sp);
        float m = sc[0];
#pragma unroll
        for (int r = 1; r < 16; ++r) m = __builtin_fmaxf(m, sc[r]);
#pragma unroll
        for (int r = 8; r < 16; ++r) m = __builtin_fmaxf(m, sp[r]);
        m = swap_max(m);
#pragma unroll
        for (int r = 0; r < 16; ++r) sc[r] = ex2(sc[r] - m);
#pragma unroll
        for (int r = 8; r < 16; ++r) sp[r] = ex2(sp[r] - m);
        u32x4 c0, c1, p0, p1; pack16(sc, c0, c1); pack16(sp, p0, p1);
        f32x16 o = MFMA(v0, asbf(c0), (f32x16){}); o = MFMA(v1, asbf(c1), o); o = MFMA(pv1, asbf(p1), o);
        const float l = from_hi(o[12]);
        store_o(o, (1.0f - gate) / l, ws + WS_OB + (size_t)(tile * 16 + h) * 2048 + vl, hi);
    }
    f32x16 o = {};
    float mref = 0.f;
    const unsigned ra = lds0 + L_RING + g * 4096 + vl;
#define TILE(addr) do { \
        const bf16x8 k0_ = ldsr128(addr), k1_ = ldsr128((addr) + 1024), v0_ = ldsr128((addr) + 2048), v1_ = ldsr128((addr) + 3072); \
        f32x16 s_ = MFMA(k0_, q0, (f32x16){}); s_ = MFMA(k1_, asbf(q1), s_); \
        _Pragma("unroll") for (int r = 0; r < 16; ++r) s_[r] = ex2(s_[r]); \
        u32x4 p0_, p1_; pack16(s_, p0_, p1_); \
        o = MFMA(v0_, asbf(p0_), o); o = MFMA(v1_, asbf(p1_), o); \
    } while (0)
    WAIT_BAR(8);
    DMA_KV(3);
    {
        const bf16x8 k0_ = ldsr128(ra), k1_ = ldsr128(ra + 1024);
        f32x16 s_ = MFMA(k0_, q0, (f32x16){}); s_ = MFMA(k1_, asbf(q1), s_);
        float m = s_[0];
#pragma unroll
        for (int r = 1; r < 16; ++r) m = __builtin_fmaxf(m, s_[r]);
        m = swap_max(m);
        const unsigned nm = cvtpk(-m, 0.f);
        mref = -__uint_as_float(nm << 16);
        if (hi) q1[2] = nm;
    }
    for (int gr = 0; gr < 8; ++gr) {
        if (gr > 0) {
            if (gr < 6) { WAIT_BAR(8); } else if (gr == 6) { WAIT_BAR(4); } else { WAIT_BAR(0); }
            if (gr + 3 < 8) DMA_KV(gr + 3);
        }
        const unsigned a = ra + (gr & 3) * 32768;
        TILE(a); TILE(a + 8192); TILE(a + 16384); TILE(a + 24576);
    }
    const unsigned cb = lds0 + L_CMB + t * 4352;
    if (g == 1) {
#pragma unroll
        for (int i = 0; i < 4; ++i) *(LAS f32x4*)(uintptr_t)(cb + i * 1024 + vl) = (f32x4){o[4 * i], o[4 * i + 1], o[4 * i + 2], o[4 * i + 3]};
        *(LAS float*)(uintptr_t)(cb + 4096 + lane * 4) = mref;
    }
    WAIT_BAR(0);
    if (g == 0) {
        const float m1 = *(const LAS float*)(uintptr_t)(cb + 4096 + lane * 4);
        const float mm = __builtin_fmaxf(mref, m1), a0 = ex2(mref - mm), a1 = ex2(m1 - mm);
#pragma unroll
        for (int i = 0; i < 4; ++i) { const f32x4 v = *(const LAS f32x4*)(uintptr_t)(cb + i * 1024 + vl);
#pragma unroll
            for (int e = 0; e < 4; ++e) o[4 * i + e] = o[4 * i + e] * a0 + v[e] * a1; }
        const float l = from_hi(o[12]);
        store_o(o, gate / l, ws + WS_OA + (size_t)(tile * 16 + h) * 2048 + vl, hi);
    }
}

constexpr unsigned L_WO = 0;
constexpr unsigned L_OF = 116 * 1024;
constexpr unsigned L_RED = 148 * 1024;

__global__ __launch_bounds__(512, 2) void k_out(const char* __restrict__ woa, const char* __restrict__ wob, const char* __restrict__ ws, char* __restrict__ out) {
    extern __shared__ __attribute__((aligned(16))) char smem[];
    const unsigned lds0 = (unsigned)(uintptr_t)(LAS char*)smem;
    const int tid = threadIdx.x, lane = tid & 63, w = __builtin_amdgcn_readfirstlane(tid >> 6), c = w & 3, g = w >> 2;
    const int bid = blockIdx.x, xg = bid & 7, idx = bid >> 3;
    const int h = (xg >> 1) * 4 + (idx & 3), j = (xg & 1) * 8 + (idx >> 2);
    const int tt = 4 * j + (h >> 2), cg = h & 3;
    const int col0 = cg < 3 ? cg * 128 : 320;
    const int r32 = lane & 31, hi = lane >> 5;
    const unsigned vl = lane * 16;
    const unsigned vwo = (lane >> 4) * 896 + (((lane & 15) ^ ((lane >> 4) << 2)) << 4);
    const int q4 = (lane & 15) >> 2, p4 = lane & 3, ch = (lane >> 4) & 1;
    const unsigned wl = lds0 + L_WO + (4 * hi + q4) * 256 + (((c ^ q4) << 6) + ch * 32 + p4 * 8);
    f32x16 acc = {};
    for (int br = 0; br < 2; ++br) {
        const char* wo = (br ? woa : wob) + col0 * 2;
        const char* of = ws + (br ? WS_OA : WS_OB) + (size_t)tt * 32768;
        if (br) WAIT_BAR(0);
        for (int i = 0; i < 14; ++i) glds16(vwo, wo + (size_t)(w * 14 + i) * 4 * 896, lds0 + L_WO + (w * 14 + i) * 1024);
        for (int i = 0; i < 4; ++i) glds16(vl, of + w * 4096 + i * 1024, lds0 + L_OF + w * 4096 + i * 1024);
        if (br == 0 && tid < 64) *(LAS u32x4*)(uintptr_t)(lds0 + L_WO + 448 * 256 + vl) = (u32x4){0u, 0u, 0u, 0u};
        WAIT_BAR(0);
#pragma unroll
        for (int hh = 0; hh < 8; ++hh) {
            const unsigned wa_ = wl + (g * 8 + hh) * 28 * 256;
            const unsigned oa_ = lds0 + L_OF + (g * 8 + hh) * 2048 + vl;
            acc = MFMA(ldsr128(oa_), wfrag(wa_, wa_ + 8 * 256), acc);
            acc = MFMA(ldsr128(oa_ + 1024), wfrag(wa_ + 16 * 256, wa_ + 24 * 256), acc);
        }
    }
    WAIT_BAR(0);
    const unsigned rb = lds0 + L_OF + c * 4096 + vl;
    if (g == 1) {
#pragma unroll
        for (int i = 0; i < 4; ++i) *(LAS f32x4*)(uintptr_t)(rb + i * 1024) = (f32x4){acc[4 * i], acc[4 * i + 1], acc[4 * i + 2], acc[4 * i + 3]};
    }
    WAIT_BAR(0);
    if (g == 0 && (cg < 3 || c >= 2)) {
#pragma unroll
        for (int i = 0; i < 4; ++i) { const f32x4 v = *(const LAS f32x4*)(uintptr_t)(rb + i * 1024);
#pragma unroll
            for (int e = 0; e < 4; ++e) acc[4 * i + e] += v[e]; }
        char* op = out + (size_t)(tt * 32) * 896 + (col0 + 32 * c + r32) * 2;
#pragma unroll
        for (int r = 0; r < 16; ++r) { const int row = (r & 3) + 8 * (r >> 2) + 4 * hi;
            *(GAS unsigned short*)(op + row * 896) = (unsigned short)(cvtpk(acc[r], 0.f) & 0xffffu); }
    }
}

extern "C" void kernel_launch(void* const* d_in, const int* in_sizes, int n_in,
                              void* d_out, int out_size, void* d_ws, size_t ws_size,
                              hipStream_t stream) {
    (void)in_sizes; (void)n_in; (void)out_size; (void)ws_size;
    (void)hipFuncSetAttribute((const void*)k_qkv, hipFuncAttributeMaxDynamicSharedMemorySize, LDS_BYTES);
    (void)hipFuncSetAttribute((const void*)k_attn, hipFuncAttributeMaxDynamicSharedMemorySize, LDS_BYTES);
    (void)hipFuncSetAttribute((const void*)k_out, hipFuncAttributeMaxDynamicSharedMemorySize, LDS_BYTES);
    k_qkv<<<256, 512, LDS_BYTES, stream>>>((const char*)d_in[0], (const char*)d_in[1], (const char*)d_in[4], (const unsigned short*)d_in[6], (char*)d_ws);
    k_attn<<<256, 512, LDS_BYTES, stream>>>((char*)d_ws);
    k_out<<<256, 512, LDS_BYTES, stream>>>((const char*)d_in[2], (const char*)d_in[5], (const char*)d_ws, (char*)d_out);
}
```

```cpp
#include <hip/hip_runtime.h>
#include <hip/hip_bf16.h>
#include <stdint.h>

typedef short bf16x8 __attribute__((ext_vector_type(8)));
typedef short s16x4 __attribute__((ext_vector_type(4)));
typedef float f32x16 __attribute__((ext_vector_type(16)));
typedef float f32x4 __attribute__((ext_vector_type(4)));
typedef float f32x2 __attribute__((ext_vector_type(2)));
typedef unsigned u32x4 __attribute__((ext_vector_type(4)));
typedef unsigned u32x2 __attribute__((ext_vector_type(2)));
typedef __bf16 bf2_t __attribute__((ext_vector_type(2)));
typedef unsigned long long u64;
#define LAS __attribute__((address_space(3)))
#define GAS __attribute__((address_space(1)))
#define DI __device__ __forceinline__
#define MFMA(a, b, c) __builtin_amdgcn_mfma_f32_32x32x16_bf16((a), (b), (c), 0, 0, 0)
#define SBAR() __builtin_amdgcn_sched_barrier(0)
#define PIN(x) asm volatile("" : "+v"(x))
#define RLX_AGENT __ATOMIC_RELAXED, __HIP_MEMORY_SCOPE_AGENT

constexpr float QSCALE = 0.18898223650461362f * 1.4426950408889634f;

constexpr unsigned WS_EPOCH = 0;
constexpr unsigned WS_FA = 4096;
constexpr unsigned WS_FB = WS_FA + 2048;
constexpr unsigned WS_FOA = WS_FB + 2048;
constexpr unsigned WS_FOB = WS_FOA + 8192;
constexpr unsigned WS_BR = 65536;
constexpr unsigned BR_Q = 0, BR_KV = 2097152, BR_O = 6291456, BR_SZ = 8388608;
constexpr unsigned WS_TOTAL = WS_BR + 2 * BR_SZ;

constexpr unsigned L_XB = 0;
constexpr unsigned L_KW = 98304;
constexpr unsigned L_GT = 155648;
constexpr unsigned L_CNT = 159232;
constexpr unsigned L_GL = 159744;
constexpr unsigned L_Q = 139264;
constexpr unsigned L_RING = 0;
constexpr unsigned L_BOP = 98304;
constexpr unsigned L_QM0 = 0, L_QM1 = 32768, L_QM2 = 98304, L_QM3 = 126976;
constexpr unsigned L_OF = 65536;
constexpr unsigned L_SCR = 155648;
constexpr unsigned LDS_BYTES = 163840;

DI unsigned cvtpk(float lo, float hi) { f32x2 v = {lo, hi}; return __builtin_bit_cast(unsigned, __builtin_convertvector(v, bf2_t)); }
#define GLDS(voff, sbase, lds_s, LC, OFF) asm volatile("s_add_i32 m0, %2, %3\n\ts_nop 0\n\tglobal_load_lds_dwordx4 %0, %1 offset:%4" \
        :: "v"(voff), "s"(sbase), "s"(__builtin_amdgcn_readfirstlane(lds_s)), "n"((int)(LC)), "n"((int)(OFF)) : "memory", "scc")
#define GLDSC(voff, sbase, lds_s, LC, OFF) asm volatile("s_add_i32 m0, %2, %3\n\ts_nop 0\n\tglobal_load_lds_dwordx4 %0, %1 offset:%4 sc1" \
        :: "v"(voff), "s"(sbase), "s"(__builtin_amdgcn_readfirstlane(lds_s)), "n"((int)(LC)), "n"((int)(OFF)) : "memory", "scc")
#define WAIT_BAR(N) asm volatile("s_waitcnt vmcnt(" #N ") lgkmcnt(0)\n\ts_barrier" ::: "memory")
#define BAR_ONLY() asm volatile("s_waitcnt lgkmcnt(0)\n\ts_barrier" ::: "memory")
DI bf16x8 ldsr128(unsigned a) { return *(const LAS bf16x8*)(uintptr_t)a; }
typedef short v4i16_t __attribute__((ext_vector_type(4)));
DI s16x4 ldstr(unsigned a) { return __builtin_bit_cast(s16x4, __builtin_amdgcn_ds_read_tr16_b64_v4i16((LAS v4i16_t*)(uintptr_t)a)); }
DI bf16x8 wfrag(unsigned a0, unsigned a1) { s16x4 lo = ldstr(a0), hi = ldstr(a1); return __builtin_shufflevector(lo, hi, 0, 1, 2, 3, 4, 5, 6, 7); }
DI bf16x8 asbf(u32x4 v) { return __builtin_bit_cast(bf16x8, v); }
DI float ex2(float v) { return __builtin_amdgcn_exp2f(v); }
DI float swap_max(float m) {
    auto rr = __builtin_amdgcn_permlane32_swap(__float_as_uint(m), __float_as_uint(m), false, false);
    return __builtin_fmaxf(__uint_as_float(rr[0]), __uint_as_float(rr[1]));
}
DI float from_hi(float v) {
    auto rr = __builtin_amdgcn_permlane32_swap(__float_as_uint(v), __float_as_uint(v), false, false);
    return __uint_as_float(rr[1]);
}
DI void pack16(const f32x16& a, u32x4& s0, u32x4& s1) {
    s0 = (u32x4){cvtpk(a[0], a[1]), cvtpk(a[2], a[3]), cvtpk(a[4], a[5]), cvtpk(a[6], a[7])};
    s1 = (u32x4){cvtpk(a[8], a[9]), cvtpk(a[10], a[11]), cvtpk(a[12], a[13]), cvtpk(a[14], a[15])};
}
DI float max16(const f32x16& s) {
    float m = __builtin_fmaxf(s[0], s[1]);
#pragma unroll
    for (int r = 2; r < 16; r += 2) m = __builtin_fmaxf(__builtin_fmaxf(m, s[r]), s[r + 1]);
    return m;
}
typedef __amdgpu_buffer_rsrc_t rsrc_t;
DI void st16c(rsrc_t r, u32x4 v, unsigned voff, unsigned soff) { __builtin_amdgcn_raw_buffer_store_b128(v, r, voff, soff, 16); }
DI bf16x8 ld16c(rsrc_t r, unsigned voff, unsigned soff) { return asbf(__builtin_amdgcn_raw_buffer_load_b128(r, voff, soff, 16)); }
DI void wait_flags(const char* ws, unsigned off, u64 tag, u64& a, u64& b) {
    asm volatile("s_waitcnt vmcnt(0)\n\tglobal_load_dwordx2 %0, %1, %2 sc1" : "=v"(a) : "v"(off), "s"(ws) : "memory");
#pragma unroll 1
    for (int it = 0; it < 30000; ++it) {
        asm volatile("s_sleep 2\n\tglobal_load_dwordx2 %0, %2, %3 sc1\n\ts_waitcnt vmcnt(1)" : "=&v"(b), "+v"(a) : "v"(off), "s"(ws) : "memory");
        if (__all(a == tag)) break;
        asm volatile("s_sleep 2\n\tglobal_load_dwordx2 %0, %2, %3 sc1\n\ts_waitcnt vmcnt(1)" : "=&v"(a), "+v"(b) : "v"(off), "s"(ws) : "memory");
        if (__all(b == tag)) break;
    }
}
#define KEEP2(a, b) asm volatile("" :: "v"(a), "v"(b))
DI void wait_flags_d(const char* ws, unsigned off, u64 tag) { u64 a, b; wait_flags(ws, off, tag, a, b); asm volatile("s_waitcnt vmcnt(0)" : "+v"(a), "+v"(b) :: "memory"); }
DI void set_flag(char* ws, unsigned off, u64 tag) { __hip_atomic_store((GAS u64*)(ws + off), tag, RLX_AGENT); }

#define STAMP(i) do {} while (0)
#define STAMPF(i) do {} while (0)
#define STARGS
#define STPASS

template <bool V>
DI unsigned pack_unit(f32x16 a, int u, int tile, int lane, u32x4& s0, u32x4& s1) {
    if (!V) {
        const bool isq = u < 16;
        const float sc = isq ? QSCALE : 1.0f;
#pragma unroll
        for (int i = 0; i < 16; ++i) a[i] *= sc;
        pack16(a, s0, s1);
        if (lane >> 5) { s1[2] = isq ? 0u : 0x00003F80u; s1[3] = 0u; }
        return isq ? BR_Q + (u * 64 + tile) * 2048 : BR_KV + ((u - 16) * 64 + tile) * 4096;
    } else {
        pack16(a, s0, s1);
        if ((lane & 31) == 28) { s0 = (u32x4){0x3F803F80u, 0x3F803F80u, 0x3F803F80u, 0x3F803F80u}; s1 = s0; }
        return BR_KV + ((u - 32) * 64 + tile) * 4096 + 2048;
    }
}
DI void store_o(const f32x16& o, float sc, rsrc_t rs, unsigned soff, int lane) {
    f32x16 v;
#pragma unroll
    for (int i = 0; i < 16; ++i) v[i] = o[i] * sc;
    u32x4 s0, s1; pack16(v, s0, s1);
    if (lane >> 5) { s1[2] = 0u; s1[3] = 0u; }
    st16c(rs, s0, lane * 16, soff); st16c(rs, s1, lane * 16 + 1024, soff);
}

#define MKTAG() do { u64 z = ((u64)__builtin_amdgcn_readfirstlane((unsigned)(ep >> 32)) << 32) | (unsigned)__builtin_amdgcn_readfirstlane((unsigned)ep); \
        z += 0x9E3779B97F4A7C15ull; z = (z ^ (z >> 30)) * 0xBF58476D1CE4E5B9ull; z = (z ^ (z >> 27)) * 0x94D049BB133111EBull; tag = z ^ (z >> 31); } while (0)
#define XB(i) (L_XB + (i) * 16384)
#define KW(i) (L_KW + (i) * 8192)
#define MM(VV, acc, wf, xfr) do { if (VV) acc = MFMA(xfr, wf, acc); else acc = MFMA(wf, xfr, acc); } while (0)
__device__ constexpr unsigned so_of(int c) { return c == 0 ? KW(0) : (c == 6 ? KW(2) : XB(c)); }
__device__ constexpr unsigned xo_of(int c) { return c == 6 ? XB(0) : XB(c); }

template <bool VK>
DI void stage1(bf16x8 (&xf)[28], const unsigned (&xa)[4], const unsigned (&xh)[4], unsigned ka0, unsigned ka1, rsrc_t rs, int h, int tile, int lane STARGS) {
    f32x16 acc0 = {}, acc1 = {};
    bf16x8 w0[2], w1[2];
#define XR(s) xf[s] = ldsr128(xo_of((s) >> 2) >= 65536 ? xh[(s) & 3] + (xo_of((s) >> 2) - 65536) : xa[(s) & 3] + xo_of((s) >> 2))
#define KF(base, s) wfrag((base) + (s) * 2048, (base) + (s) * 2048 + 512)
    XR(0); w0[0] = KF(ka0, 0); w1[0] = KF(ka1, 0); XR(1); w0[1] = KF(ka0, 1); w1[1] = KF(ka1, 1);
#pragma unroll
    for (int s = 0; s < 28; ++s) {
        if ((s & 3) == 2 && s < 26) { BAR_ONLY(); if (s == 10) STAMP(3); if (s == 22) STAMP(4); }
        SBAR();
        MM(VK, acc0, w0[s & 1], xf[s]); SBAR();
        if (s + 2 < 28) { XR(s + 2); w0[s & 1] = KF(ka0, s + 2); }
        SBAR();
        MM(VK, acc1, w1[s & 1], xf[s]); SBAR();
        if (s + 2 < 28) w1[s & 1] = KF(ka1, s + 2);
    }
    SBAR();
    u32x4 s0, s1;
    if (!VK) { pack16(acc0, s0, s1); if (lane >> 5) { s1[2] = 0x00003F80u; s1[3] = 0u; } } else (void)pack_unit<true>(acc0, 32, 0, lane, s0, s1);
    const unsigned off = WS_BR + BR_KV + ((2 * h & 15) * 64 + tile) * 4096 + (VK ? 2048 : 0);
    st16c(rs, s0, lane * 16, off); st16c(rs, s1, lane * 16 + 1024, off);
    if (!VK) { pack16(acc1, s0, s1); if (lane >> 5) { s1[2] = 0x00003F80u; s1[3] = 0u; } } else (void)pack_unit<true>(acc1, 32, 0, lane, s0, s1);
    st16c(rs, s0, lane * 16, off + 262144); st16c(rs, s1, lane * 16 + 1024, off + 262144);
}
template <bool V01, bool V2>
DI void stage2(const bf16x8 (&xf)[28], unsigned ql, unsigned qh, unsigned gl, bool spec, unsigned wlo, unsigned whi, char* ws, rsrc_t rs, unsigned lds0,
               int h, int cb, int j, int t, int lane, u64 tag, u32x4 (&bp)[6] STARGS) {
    const int tile = 4 * j + t;
    f32x16 aq = {}, acc0 = {}, acc1 = {}, acc2 = {};
    bf16x8 wq[2], w0[2], w1[2], w2[2];
    const unsigned gstride = spec ? 32u : 256u;
#define SADDR(lo, hi_, c) (so_of(c) >= 65536 ? (hi_) + (so_of(c) - 65536) : (lo) + so_of(c))
#define WF(s, u) wfrag(SADDR(wlo, whi, (s) >> 2) + ((s) & 3) * 3072 + (u) * 56, SADDR(wlo, whi, (s) >> 2) + ((s) & 3) * 3072 + 768 + (u) * 56)
#define QR(s) do { const unsigned a_ = spec ? gl + 128 * (s) : SADDR(ql, qh, (s) >> 2) + ((s) & 3) * 1024; wq[(s) & 1] = wfrag(a_, a_ + gstride); } while (0)
    QR(0); w0[0] = WF(0, 0); w1[0] = WF(0, 1); w2[0] = WF(0, 2); QR(1); w0[1] = WF(1, 0); w1[1] = WF(1, 1); w2[1] = WF(1, 2);
#pragma unroll
    for (int s = 0; s < 28; ++s) {
        if (s == 10 || s == 14 || s == 18) { BAR_ONLY(); if (s == 10) STAMP(6); if (s == 18) STAMP(7); }
        if (s == 8) {
            asm volatile("s_waitcnt vmcnt(0)" ::: "memory");
            if (lane == 0) { if (__hip_atomic_fetch_add((LAS unsigned*)(uintptr_t)(lds0 + L_CNT), 1u, __ATOMIC_RELAXED, __HIP_MEMORY_SCOPE_WORKGROUP) == 3u) set_flag(ws, WS_FA + (h * 16 + j) * 8, tag); }
            STAMP(8);
        }
        SBAR();
        aq = MFMA(wq[s & 1], xf[s], aq); SBAR();
        if (s + 2 < 28) QR(s + 2);
        SBAR();
        MM(V01, acc0, w0[s & 1], xf[s]); SBAR();
        if (s + 2 < 28) w0[s & 1] = WF(s + 2, 0);
        SBAR();
        MM(V01, acc1, w1[s & 1], xf[s]); SBAR();
        if (s + 2 < 28) w1[s & 1] = WF(s + 2, 1);
        SBAR();
        MM(V2, acc2, w2[s & 1], xf[s]); SBAR();
        if (s + 2 < 28) w2[s & 1] = WF(s + 2, 2);
    }
    SBAR();
    {
        if (lane >> 5) *(LAS float*)(uintptr_t)(lds0 + L_GL + t * 128 + (lane & 31) * 4) = aq[12];
#pragma unroll
        for (int i = 0; i < 16; ++i) aq[i] *= QSCALE;
        u32x4 s0, s1; pack16(aq, s0, s1);
        if (lane >> 5) { s1[2] = 0u; s1[3] = 0u; }
        *(LAS u32x4*)(uintptr_t)(lds0 + L_Q + t * 2048 + lane * 16) = s0; *(LAS u32x4*)(uintptr_t)(lds0 + L_Q + t * 2048 + 1024 + lane * 16) = s1;
    }
    (void)pack_unit<V01>(acc0, 3 * cb, tile, lane, bp[0], bp[1]); (void)pack_unit<V01>(acc1, 3 * cb + 1, tile, lane, bp[2], bp[3]); (void)pack_unit<V2>(acc2, 3 * cb + 2, tile, lane, bp[4], bp[5]);
}

__global__ __launch_bounds__(512, 2) void fused_kernel(const char* __restrict__ x, const char* __restrict__ wa, const char* __restrict__ woa,
                                                       const char* __restrict__ wb, const char* __restrict__ wob, const char* __restrict__ gw,
                                                       char* ws, char* __restrict__ out) {
    extern __shared__ __attribute__((aligned(16))) char smem[];
    const unsigned lds0 = (unsigned)(uintptr_t)(LAS char*)smem;
    const int tid = threadIdx.x, lane = tid & 63, w = __builtin_amdgcn_readfirstlane(tid >> 6), t = w & 3, g = w >> 2;
    const int bid = blockIdx.x, xg = bid & 7, idx = bid >> 3;
    const int h = (xg >> 1) * 4 + (idx & 3), j = (xg & 1) * 8 + (idx >> 2);
    const int r32 = lane & 31, hi = lane >> 5;
    const rsrc_t rs = __builtin_amdgcn_make_buffer_rsrc(ws, 0, WS_TOTAL, 0x00020000);
    const unsigned vl = lane * 16;
    asm volatile("" :: "s"(x), "s"(wa), "s"(woa), "s"(wb), "s"(wob), "s"(gw), "s"(ws), "s"(out));
    STAMPF(0);
    const int cb = (int)((0xCFEDBA8967542103ull >> (4 * h)) & 15);
    const int tile = 4 * j + t;
    const unsigned vxe = (lane >> 3) * 896 + (((lane & 7) ^ (lane >> 4)) << 4), lx = lds0 + t * 4096;
    const char* xb = x + (size_t)(j * 128 + t * 32) * 896;
    u64 ep, tag;
    if (g == 0) {
        unsigned gv0, gv1, dmc;
        {
            u64 pc; unsigned len;
            asm volatile("s_getpc_b64 %0\n.Lfk_ref:\n\ts_mov_b32 %1, .Lfk_end-.Lfk_ref" : "=s"(pc), "=s"(len));
            const unsigned co = (unsigned)((idx * 4 + t) * 4 + (lane & 3)) * 128u;
            asm volatile("global_load_ubyte %0, %1, %2" : "=v"(dmc) : "v"(co < len ? co : 0u), "s"(pc) : "memory");
        }
        GLDS(vxe, xb, lx, XB(0), 0); GLDS((vxe ^ 64u) + 7168, xb, lx, XB(0) + 1024, 0); GLDS(vxe + 14336, xb, lx, XB(0) + 2048, 0); GLDS((vxe ^ 64u) + 21504, xb, lx, XB(0) + 3072, 0);
        asm volatile("global_load_ushort %0, %1, %2" : "=v"(gv0) : "v"(tid * 2), "s"(gw) : "memory");
        asm volatile("global_load_ushort %0, %1, %2" : "=v"(gv1) : "v"((256 + (tid < 192 ? tid : 191)) * 2), "s"(gw) : "memory");
        asm volatile("global_load_dwordx2 %0, %1, %2 sc1" : "=v"(ep) : "v"(0u), "s"(ws) : "memory");
        const int fr = (r32 >> 1) & 7;
        unsigned xa[4], xh[4];
#pragma unroll
        for (int ks = 0; ks < 4; ++ks) { xa[ks] = lds0 + (4 * t + (r32 >> 3)) * 1024 + (r32 & 7) * 128 + (((2 * ks + hi) ^ fr) << 4); xh[ks] = xa[ks] + 65536; PIN(xa[ks]); PIN(xh[ks]); }
        const int q4 = (lane & 15) >> 2, p4 = lane & 3, ch = (lane >> 4) & 1;
        const unsigned o0 = ch * 32 + p4 * 8, o1 = o0 + 56, sw = (q4 >> 1) << 6;
        unsigned ka0 = lds0 + L_KW + (8 * hi + q4) * 128 + (o0 ^ sw), ka1 = lds0 + L_KW + (8 * hi + q4) * 128 + (o1 ^ sw); PIN(ka0); PIN(ka1);
        unsigned ql = lds0 + (8 * hi + q4) * 64 + ch * 32 + p4 * 8 + (h & 1) * 8, qh = ql + 65536; PIN(ql); PIN(qh);
        unsigned wlo = lds0 + 4096 + (8 * hi + q4) * 192 + ch * 32 + p4 * 8 + (cb & 1) * 8, whi = wlo + 65536; PIN(wlo); PIN(whi);
        const bool spec = ch == 1 && p4 == 3;
        const unsigned gl = lds0 + L_GT + 64 * hi + 8 * q4;
        asm volatile("s_waitcnt vmcnt(0)" : "+v"(gv0), "+v"(gv1), "+v"(ep) : "v"(dmc) : "memory");
        { u32x2 e = {gv0, 0u}; *(LAS u32x2*)(uintptr_t)(lds0 + L_GT + tid * 8) = e; }
        if (tid < 192) { u32x2 e = {gv1, 0u}; *(LAS u32x2*)(uintptr_t)(lds0 + L_GT + 2048 + tid * 8) = e; }
        if (tid == 0) *(LAS unsigned*)(uintptr_t)(lds0 + L_CNT) = 0u;
        MKTAG();
        bf16x8 xf[28]; u32x4 bp[6];
        STAMP(1);
        unsigned dm0, dm1;
        const unsigned l4 = lane < 56 ? lane : 55, l2 = lane < 28 ? lane : 27, trow = (j & 7) * 56 + t * 14;
        const unsigned tb = (trow + (l4 >> 2)) * 2688 + 168 * cb + ((l4 & 3) < 2 ? (l4 & 3) * 128 : 167), tq = (trow + (l2 >> 1)) * 2688 + 56 * h + (l2 & 1) * 55;
        BAR_ONLY(); STAMP(2);
        asm volatile("global_load_ubyte %0, %1, %2" : "=v"(dm0) : "v"(tb), "s"(wb) : "memory");
        asm volatile("global_load_ubyte %0, %1, %2" : "=v"(dm1) : "v"(tq), "s"(wa) : "memory");
        if (h < 8) stage1<false>(xf, xa, xh, ka0, ka1, rs, h, tile, lane STPASS); else stage1<true>(xf, xa, xh, ka0, ka1, rs, h, tile, lane STPASS);
        asm volatile("s_waitcnt vmcnt(4)" :: "v"(dm0), "v"(dm1));
        BAR_ONLY(); STAMP(5);
        if (cb <= 9) stage2<false, false>(xf, ql, qh, gl, spec, wlo, whi, ws, rs, lds0, h, cb, j, t, lane, tag, bp STPASS);
        else if (cb == 10) stage2<false, true>(xf, ql, qh, gl, spec, wlo, whi, ws, rs, lds0, h, cb, j, t, lane, tag, bp STPASS);
        else stage2<true, true>(xf, ql, qh, gl, spec, wlo, whi, ws, rs, lds0, h, cb, j, t, lane, tag, bp STPASS);
        STAMP(9);
        BAR_ONLY();
#pragma unroll
        for (int i = 0; i < 3; ++i) { const int u = 3 * cb + i;
            const unsigned off = WS_BR + BR_SZ + (u < 16 ? BR_Q + (u * 64 + tile) * 2048 : BR_KV + ((u & 15) * 64 + tile) * 4096 + (u >= 32 ? 2048 : 0));
            st16c(rs, bp[2 * i], vl, off); st16c(rs, bp[2 * i + 1], vl + 1024, off); }
    } else {
        const unsigned vx[4] = {vxe, (vxe ^ 64u) + 7168, vxe + 14336, (vxe ^ 64u) + 21504};
        const unsigned kc = (lane & 7) ^ (((lane >> 4) & 1) << 2);
        const unsigned vk0 = (lane >> 3) * 2688 + (kc > 6 ? 6 : kc) * 16, vk1 = vk0 + 8 * 2688;
        const char* wkb = wa + 896 + 112 * h + (size_t)(16 * t) * 2688;
        const unsigned lk = lds0 + t * 2048;
#define DMA_K(ck) do { const char* p_ = wkb + (size_t)(ck) * 64 * 2688; GLDS(vk0, p_, lk, KW(ck), 0); GLDS(vk1, p_, lk, KW(ck) + 1024, 0); } while (0)
#define XOFS(cx) ((cx) == 6 ? 0 : (cx) * 128)
#define DMA_X(cx) do { const char* p_ = (cx) == 6 ? xb + 768 : xb; GLDS(vx[0], p_, lx, xo_of(cx) - XOFS(cx), XOFS(cx)); GLDS(vx[1], p_, lx, xo_of(cx) + 1024 - XOFS(cx), XOFS(cx)); \
        GLDS(vx[2], p_, lx, xo_of(cx) + 2048 - XOFS(cx), XOFS(cx)); GLDS(vx[3], p_, lx, xo_of(cx) + 3072 - XOFS(cx), XOFS(cx)); } while (0)
        asm volatile("global_load_dwordx2 %0, %1, %2 sc1" : "=v"(ep) : "v"(0u), "s"(ws) : "memory");
        DMA_K(0); DMA_X(1); DMA_K(1);
        unsigned vw[3];
#pragma unroll
        for (int n = 0; n < 3; ++n) { const unsigned L = 64 * n + lane, row = (L * 43691u) >> 19, cc = L - row * 12; vw[n] = row * 2688 + (cc > 10 ? 10 : cc) * 16; }
        const unsigned vq = (lane >> 2) * 2688 + (lane & 3) * 16;
        const char* wqb = wa + (56 * h - 8 * (h & 1)) + (size_t)(16 * t) * 2688;
        const char* wbb = wb + (168 * cb - 8 * (cb & 1)) + (size_t)(16 * t) * 2688;
        const unsigned lq = lds0 + t * 1024, lw = lds0 + 4096 + t * 3072;
#define DMA_S(ck) do { GLDS(vq, wqb + (size_t)(ck) * 64 * 2688, lq, so_of(ck), 0); const char* p_ = wbb + (size_t)(ck) * 64 * 2688; \
        GLDS(vw[0], p_, lw, so_of(ck), 0); GLDS(vw[1], p_, lw, so_of(ck) + 1024, 0); GLDS(vw[2], p_, lw, so_of(ck) + 2048, 0); } while (0)
        STAMP(1);
        WAIT_BAR(6); STAMP(2); DMA_X(2); DMA_K(2);
        WAIT_BAR(6); DMA_X(3); DMA_K(3);
        WAIT_BAR(6); DMA_X(4); DMA_K(4);
        WAIT_BAR(6); STAMP(3); DMA_X(5); DMA_K(5);
        WAIT_BAR(6); DMA_X(6); DMA_K(6);
        WAIT_BAR(6); DMA_S(0); DMA_S(1);
        WAIT_BAR(8); STAMP(4); DMA_S(2); DMA_S(3);
        asm volatile("" : "+v"(ep)); MKTAG();
        const unsigned fo = WS_FA + ((((lane >> 4) & 1) * 8 + (h >> 1)) * 16 + (lane & 15)) * 8;
        WAIT_BAR(4); STAMP(5); DMA_S(4); DMA_S(5);
        WAIT_BAR(8); STAMP(6); DMA_S(6);
        WAIT_BAR(8);
        WAIT_BAR(0);
        STAMP(7);
        u64 pa, pb2; wait_flags(ws, fo, tag, pa, pb2);
        STAMP(8);
        const char* kv0 = ws + WS_BR + BR_KV + (size_t)h * 262144 + t * 8192; const unsigned lr = lds0 + t * 8192;
#define KV4(p, LC) do { GLDSC(vl, p, lr, LC, 0); GLDSC(vl, p, lr, LC, 1024); GLDSC(vl, p, lr, LC, 2048); GLDSC(vl, p, lr, LC, 3072); } while (0)
        KV4(kv0, L_RING); KV4(kv0 + 4096, L_RING + 4096); KV4(kv0 + 32768, L_RING + 32768); KV4(kv0 + 36864, L_RING + 36864);
        STAMP(9);
        WAIT_BAR(8);
        KEEP2(pa, pb2);
    }
    STAMP(10);
    const char* kvb = ws + WS_BR + BR_KV + (size_t)h * 262144 + w * 4096;
    const bf16x8 q0 = ldsr128(lds0 + L_Q + t * 2048 + vl);
    u32x4 q1 = __builtin_bit_cast(u32x4, ldsr128(lds0 + L_Q + t * 2048 + 1024 + vl));
    unsigned ra0 = lds0 + L_RING + g * 4096 + vl, ra2 = ra0 + 65536; PIN(ra0); PIN(ra2);
    f32x16 o = {}, sA, sB; u32x4 pA0, pA1, pB0 = {}, pB1 = {};
    bf16x8 k0 = ldsr128(ra0), k1 = ldsr128(ra0 + 1024), v0 = {}, v1 = {};
    float mref;
    {
        sA = MFMA(k0, q0, (f32x16){}); sB = MFMA(k1, asbf(q1), sA);
        const float m = swap_max(max16(sB));
        const unsigned nm = cvtpk(-m, 0.f);
        mref = -__uint_as_float(nm << 16);
        if (hi) q1[2] = nm;
        sA = MFMA(k1, asbf(q1), sA);
    }
    k0 = ldsr128(ra0 + 8192); k1 = ldsr128(ra0 + 8192 + 1024);
    const int tt = 4 * j + (h >> 2), cg = h & 3, c = t;
    float glog = *(const LAS float*)(uintptr_t)(lds0 + L_GL + (h >> 2) * 128 + r32 * 4); PIN(glog);
    const int col0 = cg < 3 ? cg * 128 : 320;
    const unsigned vwo = (lane >> 4) * 896 + (((lane & 15) ^ ((lane >> 4) << 2)) << 4);
    const char* wobp = wob + col0 * 2; const char* woap = woa + col0 * 2;
    const int w4 = w < 7 ? w * 4 : 24;
#define RA(slot, off) ((slot) == 2 ? ra2 + (off) : ra0 + (slot) * 32768 + (off))
#define EXP4(S, P, b, jj) do { S[b] = ex2(S[b]); S[b + 1] = ex2(S[b + 1]); S[b + 2] = ex2(S[b + 2]); S[b + 3] = ex2(S[b + 3]); \
        P[jj] = cvtpk(S[b], S[b + 1]); P[jj + 1] = cvtpk(S[b + 2], S[b + 3]); PIN(P); SBAR(); } while (0)
#define STEP(SC, SN, PC0, PC1, PP0, PP1, ka, va, H0, H1, H2, H3) do { \
        SN = MFMA(k0, q0, (f32x16){}); SBAR(); k0 = ldsr128(ka); H0; EXP4(SC, PC0, 0, 0); \
        SN = MFMA(k1, asbf(q1), SN); SBAR(); k1 = ldsr128((ka) + 1024); H1; EXP4(SC, PC0, 4, 2); \
        o = MFMA(v0, asbf(PP0), o); SBAR(); v0 = ldsr128((va) + 2048); H2; EXP4(SC, PC1, 8, 0); \
        o = MFMA(v1, asbf(PP1), o); SBAR(); v1 = ldsr128((va) + 3072); H3; EXP4(SC, PC1, 12, 2); \
    } while (0)
#define NOP_ do {} while (0)
    const unsigned lw4 = lds0 + w * 4096, lq4 = lds0 + w4 * 1024, lw7 = lds0 + w * 7168;
#define DWP(base, p, lds_s, LC) do { if ((p) & 1) GLDS(vwo, (base) + (size_t)((p) - 1) * 3584, lds_s, (LC) + (p) * 1024 - 3584, 3584); else GLDS(vwo, (base) + (size_t)(p) * 3584, lds_s, (LC) + (p) * 1024, 0); } while (0)
#define DKV(gr, i) do { if ((gr) < 6) GLDSC(vl, kvb + (size_t)((gr) + 2) * 32768, lw4, L_RING + (((gr) + 2) % 3) * 32768, (i) * 1024); \
        else if ((gr) == 7) GLDS(vwo, wobp + (size_t)(w4 + (i)) * 3584, lq4, L_QM0 + (i) * 1024, 0); \
        else GLDSC(vl, ofb + w * 4096, lw4, L_OF, (i) * 1024); } while (0)
#define DWO(gr, i) do { if ((gr) == 4 || ((gr) == 5 && (i) < 3)) DWP(wobp + (size_t)(56 + w * 7) * 3584, ((gr) - 4) * 4 + (i), lw7, L_QM2); } while (0)
    const unsigned bB = WS_BR + BR_SZ;
    const char* ofb = ws + bB + BR_O + (size_t)tt * 32768;
    u64 fl = 0;
    const unsigned fbo = WS_FB + (((16 * (lane % 3) + h) / 3) * 16 + ((lane & 4) && j > 0 ? j - 1 : j)) * 8;
#define FLB(gr) do { if ((gr) == 2) asm volatile("s_waitcnt vmcnt(4)\n\tglobal_load_dwordx2 %0, %1, %2 sc1" : "+v"(fl) : "v"(fbo), "s"(ws) : "memory"); } while (0)
#pragma unroll
    for (int gr = 0; gr < 8; ++gr) {
        const int sa = gr % 3, sn = (gr + 1) % 3;
        STEP(sA, sB, pA0, pA1, pB0, pB1, RA(sa, 16384), RA(sa, 0), NOP_, NOP_, NOP_, NOP_);
        STEP(sB, sA, pB0, pB1, pA0, pA1, RA(sa, 24576), RA(sa, 8192), FLB(gr), NOP_, NOP_, NOP_);
        if (gr == 0 && g == 0) asm volatile("s_barrier" ::: "memory"); else asm volatile("s_waitcnt vmcnt(0)\n\ts_barrier" : "+v"(fl) :: "memory");
        STAMP(11 + gr);
        if (gr == 1) {
            if (tid == 0) set_flag(ws, WS_FB + (cb * 16 + j) * 8, tag);
            {
                const int li = (w & 3) * 28 + (lane < 28 ? lane : 27);
                asm volatile("global_load_dwordx2 %0, %1, %2" : "=v"(fl) : "v"(((j & 7) * 56 + (li >> 1)) * 896 + (li & 1) * 128), "s"(g ? woap : wobp) : "memory"); }
            if (__builtin_expect(bid == 0 && w == 7, 0)) {
#pragma unroll 1
                for (int i = 0; i < 4; ++i) wait_flags_d(ws, WS_FA + (i * 64 + lane) * 8, tag);
                if (lane == 0) __hip_atomic_store((GAS u64*)(ws + WS_EPOCH), ep + 1, RLX_AGENT);
            }
        }
        if (gr == 2) {
            if (g == 0) {
                if (__builtin_expect(!__all(fl == tag), 0)) wait_flags_d(ws, fbo, tag);
                const char* qo = ws + bB + BR_Q + (size_t)(h * 64 + tile) * 2048; const char* ko = ws + bB + BR_KV + (size_t)(h * 64 + tile) * 4096;
                const unsigned lq = lds0 + t * 2048, lk = lds0 + t * 4096;
                GLDSC(vl, qo, lq, L_BOP + 20480, 0); GLDSC(vl, qo, lq, L_BOP + 20480, 1024);
                GLDSC(vl, ko, lk, L_BOP + 4096, 0); GLDSC(vl, ko, lk, L_BOP + 4096, 1024); GLDSC(vl, ko, lk, L_BOP + 4096, 2048); GLDSC(vl, ko, lk, L_BOP + 4096, 3072);
                if (t == 0) { const char* po = tile > 0 ? ko - 4096 : ko; GLDSC(vl, po, lds0, L_BOP, 0); GLDSC(vl, po, lds0, L_BOP, 1024); GLDSC(vl, po, lds0, L_BOP, 3072); }
            }
        }
        if (gr == 3) {
            if (g == 0) {
                int lo_ = lane; PIN(lo_);
                const unsigned long long M = (0x5555ull << ((lo_ & 31) + 18)) >> (4 * (lo_ >> 5));
                const unsigned bo = lds0 + L_BOP + t * 4096 + vl, bq = lds0 + L_BOP + 20480 + t * 2048 + vl;
                const bf16x8 bq0 = ldsr128(bq), bq1 = ldsr128(bq + 1024), bk0 = ldsr128(bo + 4096), bk1 = ldsr128(bo + 5120), bv0 = ldsr128(bo + 6144), bv1 = ldsr128(bo + 7168);
                const bf16x8 bp0 = ldsr128(bo), bp1 = ldsr128(bo + 1024), bpv = ldsr128(bo + 3072);
                int nlo = tile > 0 ? ~(unsigned)M : ~0u, nhi = ~(unsigned)(M >> 32);
                f32x16 cb, pb;
#pragma unroll
                for (int r = 0; r < 16; ++r) { const int cc = (r & 3) + 8 * (r >> 2);
                    int e0 = nhi << (31 - cc); PIN(e0); cb[r] = __uint_as_float((unsigned)(e0 >> 8) & 0xFF800000u);
                    if (r < 8) pb[r] = 0.f; else { int e1 = nlo << (31 - cc); PIN(e1); pb[r] = __uint_as_float((unsigned)(e1 >> 8) & 0xFF800000u); } }
                f32x16 sc = MFMA(bk0, bq0, cb); sc = MFMA(bk1, bq1, sc);
                f32x16 sp = MFMA(bp0, bq0, pb); sp = MFMA(bp1, bq1, sp);
                float m = max16(sc);
#pragma unroll
                for (int r = 8; r < 16; r += 2) m = __builtin_fmaxf(__builtin_fmaxf(m, sp[r]), sp[r + 1]);
                m = swap_max(m);
#pragma unroll
                for (int r = 0; r < 16; ++r) sc[r] = ex2(sc[r] - m);
#pragma unroll
                for (int r = 8; r < 16; ++r) sp[r] = ex2(sp[r] - m);
                u32x4 c0, c1, p0, p1; pack16(sc, c0, c1); pack16(sp, p0, p1);
                f32x16 ob = MFMA(bv0, asbf(c0), (f32x16){}); ob = MFMA(bv1, asbf(c1), ob); ob = MFMA(bpv, asbf(p1), ob);
                store_o(ob, __builtin_amdgcn_rcpf(from_hi(ob[12])), rs, bB + BR_O + (tile * 16 + h) * 2048, lane);
            }
        }
        if (gr == 4) { if (g == 0 && lane == 0) set_flag(ws, WS_FOB + (tile * 16 + h) * 8, tag); }
        if (gr == 5) asm volatile("global_load_dwordx2 %0, %1, %2 sc1" : "=v"(fl) : "v"(WS_FOB + (tt * 16 + (lane & 15)) * 8), "s"(ws) : "memory");
        if (gr == 6) { if (__builtin_expect(!__all(fl == tag), 0)) wait_flags_d(ws, WS_FOB + (tt * 16 + (lane & 15)) * 8, tag); }
        SBAR();
        STEP(sA, sB, pA0, pA1, pB0, pB1, RA(sn, 0), RA(sa, 16384), DKV(gr, 0), DKV(gr, 1), DKV(gr, 2), DKV(gr, 3));
        STEP(sB, sA, pB0, pB1, pA0, pA1, RA(sn, 8192), RA(sa, 24576), DWO(gr, 0), DWO(gr, 1), DWO(gr, 2), DWO(gr, 3));
    }
    o = MFMA(v0, asbf(pB0), o); o = MFMA(v1, asbf(pB1), o);
    STAMP(20);
    if (__builtin_expect(__any(hi && !(o[12] < 1.0e18f)), 0)) {
        if (hi) q1[2] = 0u;
        float m = -__builtin_inff(); o = (f32x16){};
        const unsigned kb = WS_BR + BR_KV + h * 262144 + g * 4096;
#pragma unroll 1
        for (int i = 0; i < 32; ++i) {
            const unsigned so = kb + i * 8192;
            const bf16x8 a0 = ld16c(rs, vl, so), a1 = ld16c(rs, vl + 1024, so), b0 = ld16c(rs, vl + 2048, so), b1 = ld16c(rs, vl + 3072, so);
            f32x16 s = MFMA(a0, q0, (f32x16){}); s = MFMA(a1, asbf(q1), s);
            const float mn = __builtin_fmaxf(m, swap_max(max16(s))), al = ex2(m - mn);
            m = mn;
#pragma unroll
            for (int r = 0; r < 16; ++r) { s[r] = ex2(s[r] - mn); o[r] *= al; }
            u32x4 e0, e1; pack16(s, e0, e1);
            o = MFMA(b0, asbf(e0), o); o = MFMA(b1, asbf(e1), o);
        }
        mref = m;
    }
    STAMP(28);
    const unsigned cbs = lds0 + L_SCR + t * 2048 + vl;
    if (g == 1) {
        u32x4 s0, s1; pack16(o, s0, s1);
        if (hi) { s1[2] = __float_as_uint(o[12]); s1[3] = __float_as_uint(mref); }
        *(LAS u32x4*)(uintptr_t)cbs = s0; *(LAS u32x4*)(uintptr_t)(cbs + 1024) = s1;
    }
    STAMP(29);
    BAR_ONLY();
    STAMP(30);
    const int q4 = (lane & 15) >> 2, p4 = lane & 3, ch = (lane >> 4) & 1;
    const unsigned wl = lds0 + (4 * hi + q4) * 256 + (((c ^ q4) << 6) + ch * 32 + p4 * 8);
    unsigned wqa = wl + L_QM0, wqb = wl + L_QM2;
    unsigned wka = hi ? wqa - 1024 : wqa, wkb = hi ? wqb - 1024 : wqb;
    unsigned oa = lds0 + L_OF + vl;
    PIN(wqa); PIN(wqb); PIN(wka); PIN(wkb); PIN(oa);
    const unsigned lc7 = lds0 + c * 7168;
    bf16x8 wf[4], of[4];
#define QOFF(q) ((q) == 1 ? L_QM1 - L_QM0 : ((q) == 3 ? L_QM3 - L_QM2 : 0))
#define OPW(s) wfrag(((s) < 16 ? wqa : wqb) + QOFF((s) >> 3) + (((s) & 7) >> 1) * 7168 + ((s) & 1) * 4096, \
                     (((s) & 7) == 7 ? ((s) < 16 ? wka : wkb) : ((s) < 16 ? wqa : wqb)) + QOFF((s) >> 3) + (((s) & 7) >> 1) * 7168 + ((s) & 1) * 4096 + 2048)
#define OPROJ(acc, N, KS, HOOK) do { \
        _Pragma("unroll") for (int i = 0; i < 4; ++i) { wf[i] = OPW(KS(i)); of[i] = ldsr128(oa + KS(i) * 1024); } \
        _Pragma("unroll") for (int i = 0; i < (N); ++i) { SBAR(); acc = MFMA(wf[i & 3], of[i & 3], acc); SBAR(); \
            if (i + 4 < (N)) { wf[i & 3] = OPW(KS(i + 4)); of[i & 3] = ldsr128(oa + KS(i + 4) * 1024); } HOOK(i); } SBAR(); } while (0)
#define NOHOOK(i) do {} while (0)
#define DW1(i_, k) if ((i_) == (k)) DWP(woap + (size_t)(56 + ((k) / 7) * 28 + c * 7) * 3584, (k) % 7, lc7, (k) < 7 ? L_QM2 : L_QM3)
#define HOOK_A23(i_) do { DW1(i_, 0); DW1(i_, 1); DW1(i_, 2); DW1(i_, 3); DW1(i_, 4); DW1(i_, 5); DW1(i_, 6); DW1(i_, 7); DW1(i_, 8); DW1(i_, 9); DW1(i_, 10); DW1(i_, 11); DW1(i_, 12); DW1(i_, 13); } while (0)
#define KS_23(i) ((i) + 16)
#define KS_01(i) (i)
#define KS_ALL(i) (((i) + 16) & 31)
#define DWQ(base, q, LC) do { _Pragma("unroll") for (int i_ = 0; i_ < 7; ++i_) { \
        if (i_ == 0) DWP((base) + (size_t)((q) * 28 + c * 7) * 3584, 0, lc7, LC); if (i_ == 1) DWP((base) + (size_t)((q) * 28 + c * 7) * 3584, 1, lc7, LC); \
        if (i_ == 2) DWP((base) + (size_t)((q) * 28 + c * 7) * 3584, 2, lc7, LC); if (i_ == 3) DWP((base) + (size_t)((q) * 28 + c * 7) * 3584, 3, lc7, LC); \
        if (i_ == 4) DWP((base) + (size_t)((q) * 28 + c * 7) * 3584, 4, lc7, LC); if (i_ == 5) DWP((base) + (size_t)((q) * 28 + c * 7) * 3584, 5, lc7, LC); \
        if (i_ == 6) DWP((base) + (size_t)((q) * 28 + c * 7) * 3584, 6, lc7, LC); } } while (0)
#define DWQ0(base, LC) do { const char* b_ = (base) + (size_t)(c * 7) * 3584; GLDS(vwo, b_, lc7, LC, 0); GLDS(vwo, b_ + 3584, lc7, (LC) + 1024, 0); GLDS(vwo, b_ + 2 * 3584, lc7, (LC) + 2048, 0); \
        GLDS(vwo, b_ + 3 * 3584, lc7, (LC) + 3072, 0); DWP(b_, 4, lc7, LC); DWP(b_, 5, lc7, LC); DWP(b_, 6, lc7, LC); } while (0)
    const unsigned pb = (c < 2 ? lds0 + L_SCR + c * 4096 : (c == 2 ? lds0 + 28672 : lds0 + 61440)) + vl;
    f32x16 acc = {};
    if (g == 1) {
        DWQ(wobp, 1, L_QM1);
        STAMP(21);
        OPROJ(acc, 16, KS_23, NOHOOK);
        STAMP(22);
        WAIT_BAR(0);
        STAMP(23);
        OPROJ(acc, 16, KS_01, HOOK_A23);
        const float g1 = 1.0f - __builtin_amdgcn_rcpf(1.0f + ex2(-1.4426950408889634f * glog));
#pragma unroll
        for (int i = 0; i < 4; ++i) *(LAS f32x4*)(uintptr_t)(pb + i * 1024) = (f32x4){acc[4 * i] * g1, acc[4 * i + 1] * g1, acc[4 * i + 2] * g1, acc[4 * i + 3] * g1};
        BAR_ONLY();
        STAMP(24);
        DWQ0(woap, L_QM0); DWQ(woap, 1, L_QM1);
        WAIT_BAR(0);
    } else {
        const u32x4 s0 = *(const LAS u32x4*)(uintptr_t)cbs, s1 = *(const LAS u32x4*)(uintptr_t)(cbs + 1024);
        const float l1 = from_hi(__uint_as_float(s1[2])), m1 = from_hi(__uint_as_float(s1[3])), l0 = from_hi(o[12]);
        const float mm = __builtin_fmaxf(mref, m1), a0 = ex2(mref - mm), a1 = ex2(m1 - mm);
        const float rl = __builtin_amdgcn_rcpf(l0 * a0 + l1 * a1), f0 = a0 * rl, f1 = a1 * rl;
        f32x16 v;
#pragma unroll
        for (int i = 0; i < 4; ++i) {
            v[2 * i] = o[2 * i] * f0 + __uint_as_float(s0[i] << 16) * f1; v[2 * i + 1] = o[2 * i + 1] * f0 + __uint_as_float(s0[i] & 0xffff0000u) * f1;
            v[8 + 2 * i] = o[8 + 2 * i] * f0 + __uint_as_float(s1[i] << 16) * f1; v[9 + 2 * i] = o[9 + 2 * i] * f0 + __uint_as_float(s1[i] & 0xffff0000u) * f1; }
        u32x4 e0, e1; pack16(v, e0, e1);
        if (hi) { e1[2] = 0u; e1[3] = 0u; }
        const unsigned so = WS_BR + BR_O + (tile * 16 + h) * 2048;
        st16c(rs, e0, vl, so); st16c(rs, e1, vl + 1024, so);
        STAMP(21);
        WAIT_BAR(2);
        STAMP(23);
        asm volatile("s_waitcnt vmcnt(0)" ::: "memory");
        if (lane == 0) set_flag(ws, WS_FOA + (tile * 16 + h) * 8, tag);
        const float gate = __builtin_amdgcn_rcpf(1.0f + ex2(-1.4426950408889634f * glog));
        STAMP(22);
        BAR_ONLY();
        STAMP(24);
        f32x16 pbv;
#pragma unroll
        for (int i = 0; i < 4; ++i) { const f32x4 e = *(const LAS f32x4*)(uintptr_t)(pb + i * 1024); pbv[4 * i] = e[0]; pbv[4 * i + 1] = e[1]; pbv[4 * i + 2] = e[2]; pbv[4 * i + 3] = e[3]; }
        u64 pa, pb2; wait_flags(ws, WS_FOA + (tt * 16 + (lane & 15)) * 8, tag, pa, pb2);
        STAMP(25);
        {   const char* ofa = ws + WS_BR + BR_O + (size_t)tt * 32768 + c * 8192; const unsigned lr = lds0 + c * 8192;
            GLDSC(vl, ofa, lr, L_OF, 0); GLDSC(vl, ofa, lr, L_OF, 1024); GLDSC(vl, ofa, lr, L_OF, 2048); GLDSC(vl, ofa, lr, L_OF, 3072);
            GLDSC(vl, ofa + 4096, lr, L_OF + 4096, 0); GLDSC(vl, ofa + 4096, lr, L_OF + 4096, 1024); GLDSC(vl, ofa + 4096, lr, L_OF + 4096, 2048); GLDSC(vl, ofa + 4096, lr, L_OF + 4096, 3072); }
        WAIT_BAR(0);
        KEEP2(pa, pb2);
        STAMP(26);
        OPROJ(acc, 32, KS_ALL, NOHOOK);
        if (cg < 3 || c >= 2) {
#pragma unroll
            for (int i = 0; i < 16; ++i) acc[i] = acc[i] * gate + pbv[i];
            u32x4 s0_, s1_; pack16(acc, s0_, s1_);
            auto r0 = __builtin_amdgcn_permlane32_swap(s0_[0], s0_[2], false, false), r1 = __builtin_amdgcn_permlane32_swap(s0_[1], s0_[3], false, false);
            auto r2 = __builtin_amdgcn_permlane32_swap(s1_[0], s1_[2], false, false), r3 = __builtin_amdgcn_permlane32_swap(s1_[1], s1_[3], false, false);
            char* op = out + (size_t)(tt * 32 + r32) * 896 + (col0 + 32 * c) * 2 + hi * 16;
            *(GAS u32x4*)op = (u32x4){r0[0], r1[0], r0[1], r1[1]};
            *(GAS u32x4*)(op + 32) = (u32x4){r2[0], r3[0], r2[1], r3[1]};
        }
    }
    STAMPF(27);
    asm volatile(".Lfk_end:");
}

extern "C" void kernel_launch(void* const* d_in, const int* in_sizes, int n_in,
                              void* d_out, int out_size, void* d_ws, size_t ws_size,
                              hipStream_t stream) {
    (void)in_sizes; (void)n_in; (void)out_size; (void)ws_size;
    (void)hipFuncSetAttribute((const void*)fused_kernel, hipFuncAttributeMaxDynamicSharedMemorySize, LDS_BYTES);
    fused_kernel<<<256, 512, LDS_BYTES, stream>>>((const char*)d_in[0], (const char*)d_in[1], (const char*)d_in[2], (const char*)d_in[4],
                                                  (const char*)d_in[5], (const char*)d_in[6], (char*)d_ws, (char*)d_out);
}
```

```cpp
#include <hip/hip_runtime.h>
#include <hip/hip_bf16.h>
#include <stdint.h>

typedef short bf16x8 __attribute__((ext_vector_type(8)));
typedef short s16x4 __attribute__((ext_vector_type(4)));
typedef float f32x16 __attribute__((ext_vector_type(16)));
typedef float f32x4 __attribute__((ext_vector_type(4)));
typedef float f32x2 __attribute__((ext_vector_type(2)));
typedef unsigned u32x4 __attribute__((ext_vector_type(4)));
typedef unsigned u32x2 __attribute__((ext_vector_type(2)));
typedef __bf16 bf2_t __attribute__((ext_vector_type(2)));
typedef unsigned long long u64;
#define LAS __attribute__((address_space(3)))
#define GAS __attribute__((address_space(1)))
#define DI __device__ __forceinline__
#define MFMA(a, b, c) __builtin_amdgcn_mfma_f32_32x32x16_bf16((a), (b), (c), 0, 0, 0)
#define SBAR() __builtin_amdgcn_sched_barrier(0)
#define PIN(x) asm volatile("" : "+v"(x))
#define RLX_AGENT __ATOMIC_RELAXED, __HIP_MEMORY_SCOPE_AGENT

template <int N> struct IC { static constexpr int value = N; };
constexpr float QSCALE = 0.18898223650461362f * 1.4426950408889634f;

constexpr unsigned WS_EPOCH = 0;
constexpr unsigned WS_FA = 4096;
constexpr unsigned WS_FB = WS_FA + 2048;
constexpr unsigned WS_FOA = WS_FB + 2048;
constexpr unsigned WS_FOB = WS_FOA + 8192;
constexpr unsigned WS_BR = 65536;
constexpr unsigned BR_Q = 0, BR_KV = 2097152, BR_O = 6291456, BR_SZ = 8388608;
constexpr unsigned WS_TOTAL = WS_BR + 2 * BR_SZ;

constexpr unsigned L_XB = 0;
constexpr unsigned L_KW = 98304;
constexpr unsigned L_GT = 155648;
constexpr unsigned L_CNT = 159232;
constexpr unsigned L_GL = 159744;
constexpr unsigned L_Q = 139264;
constexpr unsigned L_RING = 0;
constexpr unsigned L_BOP = 98304;
constexpr unsigned L_QM0 = 0, L_QM1 = 32768, L_QM2 = 98304, L_QM3 = 126976;
constexpr unsigned L_OF = 65536;
constexpr unsigned L_SCR = 155648;
constexpr unsigned LDS_BYTES = 163840;

DI unsigned cvtpk(float lo, float hi) { f32x2 v = {lo, hi}; return __builtin_bit_cast(unsigned, __builtin_convertvector(v, bf2_t)); }
#define GLDS(voff, sbase, lds_s, LC, OFF) asm volatile("s_add_i32 m0, %2, %3\n\ts_nop 0\n\tglobal_load_lds_dwordx4 %0, %1 offset:%4" \
        :: "v"(voff), "s"(sbase), "s"(__builtin_amdgcn_readfirstlane(lds_s)), "n"((int)(LC)), "n"((int)(OFF)) : "memory", "scc")
#define GLDSC(voff, sbase, lds_s, LC, OFF) asm volatile("s_add_i32 m0, %2, %3\n\ts_nop 0\n\tglobal_load_lds_dwordx4 %0, %1 offset:%4 sc1" \
        :: "v"(voff), "s"(sbase), "s"(__builtin_amdgcn_readfirstlane(lds_s)), "n"((int)(LC)), "n"((int)(OFF)) : "memory", "scc")
#define GLDS_IF(sel, voff, sbase, lds_s, LC, OFF) asm volatile("s_cmp_lg_u32 %5, 0\n\ts_cbranch_scc1 1f\n\ts_add_i32 m0, %2, %3\n\ts_nop 0\n\tglobal_load_lds_dwordx4 %0, %1 offset:%4\n1:" \
        :: "v"(voff), "s"(sbase), "s"(__builtin_amdgcn_readfirstlane(lds_s)), "n"((int)(LC)), "n"((int)(OFF)), "s"(sel) : "memory", "scc")
#define GLDSC_IF(sel, voff, sbase, lds_s, LC, OFF) asm volatile("s_cmp_lg_u32 %5, 0\n\ts_cbranch_scc1 1f\n\ts_add_i32 m0, %2, %3\n\ts_nop 0\n\tglobal_load_lds_dwordx4 %0, %1 offset:%4 sc1\n1:" \
        :: "v"(voff), "s"(sbase), "s"(__builtin_amdgcn_readfirstlane(lds_s)), "n"((int)(LC)), "n"((int)(OFF)), "s"(sel) : "memory", "scc")
#define WAIT_BAR(N) asm volatile("s_waitcnt vmcnt(" #N ") lgkmcnt(0)\n\ts_barrier" ::: "memory")
#define BAR_ONLY() asm volatile("s_waitcnt lgkmcnt(0)\n\ts_barrier" ::: "memory")
DI bf16x8 ldsr128(unsigned a) { return *(const LAS bf16x8*)(uintptr_t)a; }
typedef short v4i16_t __attribute__((ext_vector_type(4)));
DI s16x4 ldstr(unsigned a) { return __builtin_bit_cast(s16x4, __builtin_amdgcn_ds_read_tr16_b64_v4i16((LAS v4i16_t*)(uintptr_t)a)); }
DI bf16x8 wfrag(unsigned a0, unsigned a1) { s16x4 lo = ldstr(a0), hi = ldstr(a1); return __builtin_shufflevector(lo, hi, 0, 1, 2, 3, 4, 5, 6, 7); }
DI bf16x8 asbf(u32x4 v) { return __builtin_bit_cast(bf16x8, v); }
DI float ex2(float v) { return __builtin_amdgcn_exp2f(v); }
DI float swap_max(float m) {
    auto rr = __builtin_amdgcn_permlane32_swap(__float_as_uint(m), __float_as_uint(m), false, false);
    return __builtin_fmaxf(__uint_as_float(rr[0]), __uint_as_float(rr[1]));
}
DI float from_hi(float v) {
    auto rr = __builtin_amdgcn_permlane32_swap(__float_as_uint(v), __float_as_uint(v), false, false);
    return __uint_as_float(rr[1]);
}
DI void pack16(const f32x16& a, u32x4& s0, u32x4& s1) {
    s0 = (u32x4){cvtpk(a[0], a[1]), cvtpk(a[2], a[3]), cvtpk(a[4], a[5]), cvtpk(a[6], a[7])};
    s1 = (u32x4){cvtpk(a[8], a[9]), cvtpk(a[10], a[11]), cvtpk(a[12], a[13]), cvtpk(a[14], a[15])};
}
DI float max16(const f32x16& s) {
    float m = __builtin_fmaxf(s[0], s[1]);
#pragma unroll
    for (int r = 2; r < 16; r += 2) m = __builtin_fmaxf(__builtin_fmaxf(m, s[r]), s[r + 1]);
    return m;
}
typedef __amdgpu_buffer_rsrc_t rsrc_t;
DI void st16c(rsrc_t r, u32x4 v, unsigned voff, unsigned soff) { __builtin_amdgcn_raw_buffer_store_b128(v, r, voff, soff, 16); }
DI bf16x8 ld16c(rsrc_t r, unsigned voff, unsigned soff) { return asbf(__builtin_amdgcn_raw_buffer_load_b128(r, voff, soff, 16)); }
DI void wait_flags(const char* ws, unsigned off, u64 tag, u64& a, u64& b) {
    asm volatile("s_waitcnt vmcnt(0)\n\tglobal_load_dwordx2 %0, %1, %2 sc1" : "=v"(a) : "v"(off), "s"(ws) : "memory");
#pragma unroll 1
    for (int it = 0; it < 30000; ++it) {
        asm volatile("s_sleep 2\n\tglobal_load_dwordx2 %0, %2, %3 sc1\n\ts_waitcnt vmcnt(1)" : "=&v"(b), "+v"(a) : "v"(off), "s"(ws) : "memory");
        if (__all(a == tag)) break;
        asm volatile("s_sleep 2\n\tglobal_load_dwordx2 %0, %2, %3 sc1\n\ts_waitcnt vmcnt(1)" : "=&v"(a), "+v"(b) : "v"(off), "s"(ws) : "memory");
        if (__all(b == tag)) break;
    }
}
#define KEEP2(a, b) asm volatile("" :: "v"(a), "v"(b))
DI void wait_flags_d(const char* ws, unsigned off, u64 tag) { u64 a, b; wait_flags(ws, off, tag, a, b); asm volatile("s_waitcnt vmcnt(0)" : "+v"(a), "+v"(b) :: "memory"); }
DI void set_flag(char* ws, unsigned off, u64 tag) { __hip_atomic_store((GAS u64*)(ws + off), tag, RLX_AGENT); }

#define STAMP(i) do {} while (0)
#define STAMPF(i) do {} while (0)
#define STARGS
#define STPASS

DI void fix_v(u32x4& s0, u32x4& s1, unsigned m28) {
#pragma unroll
    for (int i = 0; i < 4; ++i) { s0[i] = (s0[i] & ~m28) | (0x3F803F80u & m28); s1[i] = (s1[i] & ~m28) | (0x3F803F80u & m28); }
}
DI void fix_pad(u32x4& s1, unsigned pad, unsigned mhi) { s1[2] = (s1[2] & ~mhi) | (pad & mhi); s1[3] &= ~mhi; }
template <bool V>
DI void pack_unit(f32x16 a, int u, unsigned m28, unsigned mhi, u32x4& s0, u32x4& s1) {
    if (!V) {
        const bool isq = u < 16;
        const float sc = isq ? QSCALE : 1.0f;
#pragma unroll
        for (int i = 0; i < 16; ++i) a[i] *= sc;
        pack16(a, s0, s1);
        fix_pad(s1, isq ? 0u : 0x00003F80u, mhi);
    } else { pack16(a, s0, s1); fix_v(s0, s1, m28); }
}
#define MKTAG() do { u64 z = ((u64)__builtin_amdgcn_readfirstlane((unsigned)(ep >> 32)) << 32) | (unsigned)__builtin_amdgcn_readfirstlane((unsigned)ep); \
        z += 0x9E3779B97F4A7C15ull; z = (z ^ (z >> 30)) * 0xBF58476D1CE4E5B9ull; z = (z ^ (z >> 27)) * 0x94D049BB133111EBull; tag = z ^ (z >> 31); } while (0)
#define XB(i) (L_XB + (i) * 16384)
#define KW(i) (L_KW + (i) * 8192)
#define MM(VV, acc, wf, xfr) do { if (VV) acc = MFMA(xfr, wf, acc); else acc = MFMA(wf, xfr, acc); } while (0)
__device__ constexpr unsigned so_of(int c) { return c == 0 ? KW(0) : (c == 6 ? KW(2) : XB(c)); }
__device__ constexpr unsigned xo_of(int c) { return c == 6 ? XB(0) : XB(c); }

template <bool VK>
DI void stage1(bf16x8 (&xf)[28], const unsigned (&xa)[4], const unsigned (&xh)[4], unsigned ka0, unsigned ka1, rsrc_t rs, int h, int tile, int lane, unsigned m28, unsigned mhi,
               unsigned gv0, unsigned gv1, unsigned dmc, u64 ep, u64& tag, unsigned lds0 STARGS) {
    f32x16 acc0 = {}, acc1 = {};
    bf16x8 w0[2], w1[2];
#define XR(s) xf[s] = ldsr128(xo_of((s) >> 2) >= 65536 ? xh[(s) & 3] + (xo_of((s) >> 2) - 65536) : xa[(s) & 3] + xo_of((s) >> 2))
#define KF(base, s) wfrag((base) + (s) * 2048, (base) + (s) * 2048 + 512)
    XR(0); w0[0] = KF(ka0, 0); w1[0] = KF(ka1, 0); XR(1); w0[1] = KF(ka0, 1); w1[1] = KF(ka1, 1);
#pragma unroll
    for (int s = 0; s < 28; ++s) {
        if ((s & 3) == 2 && s < 26) { BAR_ONLY(); if (s == 10) STAMP(3); if (s == 22) STAMP(4); }
        if (s == 12) {
            asm volatile("s_waitcnt vmcnt(0)" : "+v"(gv0), "+v"(gv1), "+v"(ep) : "v"(dmc) : "memory");
            const int tid = threadIdx.x;
            { u32x2 e = {gv0, 0u}; *(LAS u32x2*)(uintptr_t)(lds0 + L_GT + tid * 8) = e; }
            if (tid < 192) { u32x2 e = {gv1, 0u}; *(LAS u32x2*)(uintptr_t)(lds0 + L_GT + 2048 + tid * 8) = e; }
            if (tid == 0) *(LAS unsigned*)(uintptr_t)(lds0 + L_CNT) = 0u;
            MKTAG();
        }
        SBAR();
        MM(VK, acc0, w0[s & 1], xf[s]); SBAR();
        if (s + 2 < 28) { XR(s + 2); w0[s & 1] = KF(ka0, s + 2); }
        SBAR();
        MM(VK, acc1, w1[s & 1], xf[s]); SBAR();
        if (s + 2 < 28) w1[s & 1] = KF(ka1, s + 2);
    }
    SBAR();
    u32x4 s0, s1;
    pack16(acc0, s0, s1); if (!VK) fix_pad(s1, 0x00003F80u, mhi); else fix_v(s0, s1, m28);
    const unsigned off = WS_BR + BR_KV + ((2 * h & 15) * 64 + tile) * 4096 + (VK ? 2048 : 0);
    st16c(rs, s0, lane * 16, off); st16c(rs, s1, lane * 16 + 1024, off);
    pack16(acc1, s0, s1); if (!VK) fix_pad(s1, 0x00003F80u, mhi); else fix_v(s0, s1, m28);
    st16c(rs, s0, lane * 16, off + 262144); st16c(rs, s1, lane * 16 + 1024, off + 262144);
}
template <bool V01, bool V2>
DI void stage2(const bf16x8 (&xf)[28], unsigned ql, unsigned qh, unsigned gl, bool spec, unsigned wlo, unsigned whi, char* ws, rsrc_t rs, unsigned lds0,
               int h, int cb, int j, int t, int lane, u64 tag, u32x4 (&bp)[6], unsigned m28, unsigned mhi STARGS) {
    f32x16 aq = {}, acc0 = {}, acc1 = {}, acc2 = {};
    bf16x8 wq[2], w0[2], w1[2], w2[2];
    const unsigned gstride = spec ? 32u : 256u;
#define SADDR(lo, hi_, c) (so_of(c) >= 65536 ? (hi_) + (so_of(c) - 65536) : (lo) + so_of(c))
#define WF(s, u) wfrag(SADDR(wlo, whi, (s) >> 2) + ((s) & 3) * 3072 + (u) * 56, SADDR(wlo, whi, (s) >> 2) + ((s) & 3) * 3072 + 768 + (u) * 56)
#define QR(s) do { const unsigned a_ = spec ? gl + 128 * (s) : SADDR(ql, qh, (s) >> 2) + ((s) & 3) * 1024; wq[(s) & 1] = wfrag(a_, a_ + gstride); } while (0)
    QR(0); w0[0] = WF(0, 0); w1[0] = WF(0, 1); w2[0] = WF(0, 2); QR(1); w0[1] = WF(1, 0); w1[1] = WF(1, 1); w2[1] = WF(1, 2);
#pragma unroll
    for (int s = 0; s < 28; ++s) {
        if (s == 6 || s == 10 || s == 14 || s == 18) { BAR_ONLY(); if (s == 10) STAMP(6); if (s == 18) STAMP(7); }
        if (s == 9) {
            asm volatile("s_waitcnt vmcnt(0)" ::: "memory");
            if (lane == 0) { if (__hip_atomic_fetch_add((LAS unsigned*)(uintptr_t)(lds0 + L_CNT), 1u, __ATOMIC_RELAXED, __HIP_MEMORY_SCOPE_WORKGROUP) == 3u) set_flag(ws, WS_FA + (h * 16 + j) * 8, tag); }
            STAMP(8);
        }
        SBAR();
        aq = MFMA(wq[s & 1], xf[s], aq); SBAR();
        if (s + 2 < 28) QR(s + 2);
        SBAR();
        MM(V01, acc0, w0[s & 1], xf[s]); SBAR();
        if (s + 2 < 28) w0[s & 1] = WF(s + 2, 0);
        SBAR();
        MM(V01, acc1, w1[s & 1], xf[s]); SBAR();
        if (s + 2 < 28) w1[s & 1] = WF(s + 2, 1);
        SBAR();
        MM(V2, acc2, w2[s & 1], xf[s]); SBAR();
        if (s + 2 < 28) w2[s & 1] = WF(s + 2, 2);
    }
    SBAR();
    {
        if (lane >> 5) *(LAS float*)(uintptr_t)(lds0 + L_GL + t * 128 + (lane & 31) * 4) = aq[12];
#pragma unroll
        for (int i = 0; i < 16; ++i) aq[i] *= QSCALE;
        u32x4 s0, s1; pack16(aq, s0, s1);
        fix_pad(s1, 0u, mhi);
        *(LAS u32x4*)(uintptr_t)(lds0 + L_Q + t * 2048 + lane * 16) = s0; *(LAS u32x4*)(uintptr_t)(lds0 + L_Q + t * 2048 + 1024 + lane * 16) = s1;
    }
    pack_unit<V01>(acc0, 3 * cb, m28, mhi, bp[0], bp[1]); pack_unit<V01>(acc1, 3 * cb + 1, m28, mhi, bp[2], bp[3]); pack_unit<V2>(acc2, 3 * cb + 2, m28, mhi, bp[4], bp[5]);
}

__global__ __launch_bounds__(512, 2) void fused_kernel(const char* __restrict__ x, const char* __restrict__ wa, const char* __restrict__ woa,
                                                       const char* __restrict__ wb, const char* __restrict__ wob, const char* __restrict__ gw,
                                                       char* ws, char* __restrict__ out) {
    extern __shared__ __attribute__((aligned(16))) char smem[];
    const unsigned lds0 = (unsigned)(uintptr_t)(LAS char*)smem;
    const int tid = threadIdx.x, lane = tid & 63, w = __builtin_amdgcn_readfirstlane(tid >> 6), t = w & 3, g = w >> 2;
    const int bid = blockIdx.x, xg = bid & 7, idx = bid >> 3;
    const int h = (xg >> 1) * 4 + (idx & 3), j = (xg & 1) * 8 + (idx >> 2);
    const int r32 = lane & 31, hi = lane >> 5;
    const rsrc_t rs = __builtin_amdgcn_make_buffer_rsrc(ws, 0, WS_TOTAL, 0x00020000);
    const unsigned vl = lane * 16;
    asm volatile("" :: "s"(x), "s"(wa), "s"(woa), "s"(wb), "s"(wob), "s"(gw), "s"(ws), "s"(out));
    STAMPF(0);
    const int cb = (int)((0xCFEDBA8967542103ull >> (4 * h)) & 15);
    const int tile = 4 * j + t;
    const int rot = (j + 2 * (h & 3)) % 7;
#define CK(p) ((p) + rot >= 7 ? (p) + rot - 7 : (p) + rot)
    const unsigned vxe = (lane >> 3) * 896 + (((lane & 7) ^ (lane >> 4)) << 4), lx = lds0 + t * 4096;
    const char* xb = x + (size_t)(j * 128 + t * 32) * 896;
    u64 ep, tag;
    unsigned m28 = r32 == 28 ? ~0u : 0u, mhi = hi ? ~0u : 0u; PIN(m28); PIN(mhi);
    if (g == 0) {
        unsigned gv0, gv1, dmc;
        { const char* p_ = xb + rot * 128; GLDS(vxe, p_, lx, XB(0), 0); GLDS((vxe ^ 64u) + 7168, p_, lx, XB(0) + 1024, 0); GLDS(vxe + 14336, p_, lx, XB(0) + 2048, 0); GLDS((vxe ^ 64u) + 21504, p_, lx, XB(0) + 3072, 0); }
        {
            u64 pc; unsigned len;
            asm volatile("s_getpc_b64 %0\n.Lfk_ref:\n\ts_mov_b32 %1, .Lfk_end-.Lfk_ref" : "=s"(pc), "=s"(len));
            const unsigned co = (unsigned)((idx * 4 + t) * 4 + (lane & 3)) * 128u;
            asm volatile("global_load_ubyte %0, %1, %2" : "=v"(dmc) : "v"(co < len ? co : 0u), "s"(pc) : "memory");
        }
        asm volatile("global_load_ushort %0, %1, %2" : "=v"(gv0) : "v"((CK(t) * 64 + lane) * 2), "s"(gw) : "memory");
        asm volatile("global_load_ushort %0, %1, %2" : "=v"(gv1) : "v"((CK(t < 3 ? 4 + t : 6) * 64 + lane) * 2), "s"(gw) : "memory");
        asm volatile("global_load_dwordx2 %0, %1, %2 sc1" : "=v"(ep) : "v"(0u), "s"(ws) : "memory");
        const int fr = (r32 >> 1) & 7;
        unsigned xa[4], xh[4];
#pragma unroll
        for (int ks = 0; ks < 4; ++ks) { xa[ks] = lds0 + (4 * t + (r32 >> 3)) * 1024 + (r32 & 7) * 128 + (((2 * ks + hi) ^ fr) << 4); xh[ks] = xa[ks] + 65536; PIN(xa[ks]); PIN(xh[ks]); }
        const int q4 = (lane & 15) >> 2, p4 = lane & 3, ch = (lane >> 4) & 1;
        const unsigned o0 = ch * 32 + p4 * 8, o1 = o0 + 56, sw = (q4 >> 1) << 6;
        unsigned ka0 = lds0 + L_KW + (8 * hi + q4) * 128 + (o0 ^ sw), ka1 = lds0 + L_KW + (8 * hi + q4) * 128 + (o1 ^ sw); PIN(ka0); PIN(ka1);
        unsigned ql = lds0 + (8 * hi + q4) * 64 + ch * 32 + p4 * 8 + (h & 1) * 8, qh = ql + 65536; PIN(ql); PIN(qh);
        unsigned wlo = lds0 + 4096 + (8 * hi + q4) * 192 + ch * 32 + p4 * 8 + (cb & 1) * 8, whi = wlo + 65536; PIN(wlo); PIN(whi);
        const bool spec = ch == 1 && p4 == 3;
        const unsigned gl = lds0 + L_GT + 64 * hi + 8 * q4;
        bf16x8 xf[28]; u32x4 bp[6];
        STAMP(1);
        WAIT_BAR(4); STAMP(2);
        if (h < 8) stage1<false>(xf, xa, xh, ka0, ka1, rs, h, tile, lane, m28, mhi, gv0, gv1, dmc, ep, tag, lds0 STPASS); else stage1<true>(xf, xa, xh, ka0, ka1, rs, h, tile, lane, m28, mhi, gv0, gv1, dmc, ep, tag, lds0 STPASS);
        BAR_ONLY(); STAMP(5);
        if (cb <= 9) stage2<false, false>(xf, ql, qh, gl, spec, wlo, whi, ws, rs, lds0, h, cb, j, t, lane, tag, bp, m28, mhi STPASS);
        else if (cb == 10) stage2<false, true>(xf, ql, qh, gl, spec, wlo, whi, ws, rs, lds0, h, cb, j, t, lane, tag, bp, m28, mhi STPASS);
        else stage2<true, true>(xf, ql, qh, gl, spec, wlo, whi, ws, rs, lds0, h, cb, j, t, lane, tag, bp, m28, mhi STPASS);
        STAMP(9);
        BAR_ONLY();
#pragma unroll
        for (int i = 0; i < 3; ++i) { const int u = 3 * cb + i;
            const unsigned off = WS_BR + BR_SZ + (u < 16 ? BR_Q + (u * 64 + tile) * 2048 : BR_KV + ((u & 15) * 64 + tile) * 4096 + (u >= 32 ? 2048 : 0));
            st16c(rs, bp[2 * i], vl, off); st16c(rs, bp[2 * i + 1], vl + 1024, off); }
    } else {
        const unsigned vx[4] = {vxe, (vxe ^ 64u) + 7168, vxe + 14336, (vxe ^ 64u) + 21504};
        const unsigned kc = (lane & 7) ^ (((lane >> 4) & 1) << 2);
        const unsigned vk0 = (lane >> 3) * 2688 + (kc > 6 ? 6 : kc) * 16, vk1 = vk0 + 8 * 2688;
        const char* wkb = wa + 896 + 112 * h + (size_t)(16 * t) * 2688;
        const unsigned lk = lds0 + t * 2048;
#define DMA_K(ck) do { const char* p_ = wkb + (size_t)CK(ck) * 64 * 2688; GLDS(vk0, p_, lk, KW(ck), 0); GLDS(vk1, p_, lk, KW(ck) + 1024, 0); } while (0)
#define DMA_X(cx) do { const char* p_ = xb + CK(cx) * 128; GLDS(vx[0], p_, lx, xo_of(cx), 0); GLDS(vx[1], p_, lx, xo_of(cx) + 1024, 0); \
        GLDS(vx[2], p_, lx, xo_of(cx) + 2048, 0); GLDS(vx[3], p_, lx, xo_of(cx) + 3072, 0); } while (0)
        asm volatile("global_load_dwordx2 %0, %1, %2 sc1" : "=v"(ep) : "v"(0u), "s"(ws) : "memory");
        DMA_K(0); DMA_X(1); DMA_K(1);
        unsigned dmt;
        {
            const char* tp = lane < 9 ? ws + (size_t)lane * 2097152 + 32768 : (lane == 9 ? (const char*)out : (lane == 10 ? woa : (lane == 11 ? wob : wb)));
            asm volatile("global_load_ubyte %0, %1, off sc1" : "=v"(dmt) : "v"(tp) : "memory"); }
        unsigned vw[3];
#pragma unroll
        for (int n = 0; n < 3; ++n) { const unsigned L = 64 * n + lane, row = (L * 43691u) >> 19, cc = L - row * 12; vw[n] = row * 2688 + (cc > 10 ? 10 : cc) * 16; }
        const unsigned vq = (lane >> 2) * 2688 + (lane & 3) * 16;
        const char* wqb = wa + (56 * h - 8 * (h & 1)) + (size_t)(16 * t) * 2688;
        const char* wbb = wb + (168 * cb - 8 * (cb & 1)) + (size_t)(16 * t) * 2688;
        const unsigned lq = lds0 + t * 1024, lw = lds0 + 4096 + t * 3072;
#define DMA_S(ck) do { GLDS(vq, wqb + (size_t)CK(ck) * 64 * 2688, lq, so_of(ck), 0); const char* p_ = wbb + (size_t)CK(ck) * 64 * 2688; \
        GLDS(vw[0], p_, lw, so_of(ck), 0); GLDS(vw[1], p_, lw, so_of(ck) + 1024, 0); GLDS(vw[2], p_, lw, so_of(ck) + 2048, 0); } while (0)
        STAMP(1);
        WAIT_BAR(7); STAMP(2); DMA_X(2); DMA_K(2);
        WAIT_BAR(7); DMA_X(3); DMA_K(3);
        WAIT_BAR(6); DMA_X(4); DMA_K(4);
        WAIT_BAR(6); STAMP(3); DMA_X(5); DMA_K(5); DMA_S(0);
        WAIT_BAR(10); DMA_X(6); DMA_K(6); DMA_S(1);
        WAIT_BAR(14);
        WAIT_BAR(4); STAMP(4); DMA_S(2);
        asm volatile("" : "+v"(ep) : "v"(dmt)); MKTAG();
        const unsigned fo = WS_FA + ((((lane >> 4) & 1) * 8 + (h >> 1)) * 16 + (lane & 15)) * 8;
        WAIT_BAR(4); STAMP(5); DMA_S(3);
        WAIT_BAR(4); DMA_S(4); DMA_S(5);
        WAIT_BAR(8); STAMP(6); DMA_S(6);
        unsigned dm0;
        {
            const int li = (t & 1) * 56 + (lane < 56 ? lane : 55), c0 = (h & 3) < 3 ? (h & 3) * 256 : 640;
            asm volatile("global_load_ubyte %0, %1, %2" : "=v"(dm0) : "v"(((j & 7) * 56 + (li >> 1)) * 896 + (li & 1) * 128 + c0), "s"((t & 2) ? wob : woa) : "memory"); }
        WAIT_BAR(9);
        WAIT_BAR(0);
        asm volatile("" :: "v"(dm0));
        STAMP(7);
        u64 pa, pb2; wait_flags(ws, fo, tag, pa, pb2);
        STAMP(8);
        const char* kv0 = ws + WS_BR + BR_KV + (size_t)h * 262144 + t * 8192; const unsigned lr = lds0 + t * 8192;
#define KV4(p, LC) do { GLDSC(vl, p, lr, LC, 0); GLDSC(vl, p, lr, LC, 1024); GLDSC(vl, p, lr, LC, 2048); GLDSC(vl, p, lr, LC, 3072); } while (0)
        KV4(kv0, L_RING); KV4(kv0 + 4096, L_RING + 4096); KV4(kv0 + 32768, L_RING + 32768); KV4(kv0 + 36864, L_RING + 36864);
        STAMP(9);
        WAIT_BAR(8);
        KEEP2(pa, pb2);
    }
    STAMP(10);
    const char* kvb = ws + WS_BR + BR_KV + (size_t)h * 262144 + w * 4096;
    const bf16x8 q0 = ldsr128(lds0 + L_Q + t * 2048 + vl);
    u32x4 q1 = __builtin_bit_cast(u32x4, ldsr128(lds0 + L_Q + t * 2048 + 1024 + vl));
    unsigned ra0 = lds0 + L_RING + g * 4096 + vl, ra2 = ra0 + 65536; PIN(ra0); PIN(ra2);
    f32x16 o = {}, sA, sB; u32x4 pA0, pA1, pB0 = {}, pB1 = {};
    bf16x8 k0 = ldsr128(ra0), k1 = ldsr128(ra0 + 1024), v0 = {}, v1 = {};
    float mref;
    {
        sA = MFMA(k0, q0, (f32x16){}); sB = MFMA(k1, asbf(q1), sA);
        const float m = swap_max(max16(sB));
        const unsigned nm = cvtpk(-m, 0.f);
        mref = -__uint_as_float(nm << 16);
        q1[2] = (q1[2] & ~mhi) | (nm & mhi);
        sA = MFMA(k1, asbf(q1), sA);
    }
    k0 = ldsr128(ra0 + 8192); k1 = ldsr128(ra0 + 8192 + 1024);
    const int tt = 4 * j + (h >> 2), cg = h & 3, c = t;
    float glog = *(const LAS float*)(uintptr_t)(lds0 + L_GL + (h >> 2) * 128 + r32 * 4); PIN(glog);
    const int col0 = cg < 3 ? cg * 128 : 320;
    const unsigned vwo = (lane >> 4) * 896 + (((lane & 15) ^ ((lane >> 4) << 2)) << 4);
    const char* wobp = wob + col0 * 2; const char* woap = woa + col0 * 2;
    const int w4 = w < 7 ? w * 4 : 24;
#define RA(slot, off) ((slot) == 2 ? ra2 + (off) : ra0 + (slot) * 32768 + (off))
#define EXP4(S, P, b, jj) do { S[b] = ex2(S[b]); S[b + 1] = ex2(S[b + 1]); S[b + 2] = ex2(S[b + 2]); S[b + 3] = ex2(S[b + 3]); \
        P[jj] = cvtpk(S[b], S[b + 1]); P[jj + 1] = cvtpk(S[b + 2], S[b + 3]); PIN(P); SBAR(); } while (0)
#define STEP(SC, SN, PC0, PC1, PP0, PP1, ka, va, H0, H1, H2, H3) do { \
        SN = MFMA(k0, q0, (f32x16){}); SBAR(); k0 = ldsr128(ka); H0; EXP4(SC, PC0, 0, 0); \
        SN = MFMA(k1, asbf(q1), SN); SBAR(); k1 = ldsr128((ka) + 1024); H1; EXP4(SC, PC0, 4, 2); \
        o = MFMA(v0, asbf(PP0), o); SBAR(); v0 = ldsr128((va) + 2048); H2; EXP4(SC, PC1, 8, 0); \
        o = MFMA(v1, asbf(PP1), o); SBAR(); v1 = ldsr128((va) + 3072); H3; EXP4(SC, PC1, 12, 2); \
    } while (0)
#define NOP_ do {} while (0)
    const unsigned lw4 = lds0 + w * 4096, lq4 = lds0 + w4 * 1024, lw7 = lds0 + w * 7168;
#define DWP(base, p, lds_s, LC) do { if ((p) & 1) GLDS(vwo, (base) + (size_t)((p) - 1) * 3584, lds_s, (LC) + (p) * 1024 - 3584, 3584); else GLDS(vwo, (base) + (size_t)(p) * 3584, lds_s, (LC) + (p) * 1024, 0); } while (0)
#define DWP_IF(sel, base, p, lds_s, LC) do { if ((p) & 1) GLDS_IF(sel, vwo, (base) + (size_t)((p) - 1) * 3584, lds_s, (LC) + (p) * 1024 - 3584, 3584); else GLDS_IF(sel, vwo, (base) + (size_t)(p) * 3584, lds_s, (LC) + (p) * 1024, 0); } while (0)
#define DKV(sel, gr, i) do { if ((gr) < 6) GLDSC_IF(sel, vl, kvb + (size_t)((gr) + 2) * 32768, lw4, L_RING + (((gr) + 2) % 3) * 32768, (i) * 1024); \
        else if ((gr) == 7) GLDS_IF(sel, vwo, wobp + (size_t)(w4 + (i)) * 3584, lq4, L_QM0 + (i) * 1024, 0); \
        else GLDSC_IF(sel, vl, ofb + w * 4096, lw4, L_OF, (i) * 1024); } while (0)
    const char* wsrc0 = woap + (size_t)(g * 56 + t * 7) * 3584; const char* wsrc2 = wobp + (size_t)(56 + w * 7) * 3584;
#define DW2(sel, base, i) do { DWP_IF(sel, base, 2 * (i), lw7, L_QM2); if ((i) < 3) DWP_IF(sel, base, 2 * (i) + 1, lw7, L_QM2); } while (0)
#define DWO(sel, gr, i) do { if ((gr) == 0) DW2(sel, wsrc0, i); if ((gr) == 4) DW2(sel, wsrc0 + 28 * 3584, i); if ((gr) == 6) DW2(sel, wsrc2, i); } while (0)
    const int ng = g ^ 1;
    const int q4 = (lane & 15) >> 2, p4 = lane & 3, ch = (lane >> 4) & 1;
    const unsigned wl = lds0 + (4 * hi + q4) * 256 + (((c ^ q4) << 6) + ch * 32 + p4 * 8);
    unsigned wra = wl + (g ? L_QM3 : L_QM2), wrk = wra - (1024u & mhi); PIN(wra); PIN(wrk);
    bf16x8 war[16];
#define WAR(gr, k) do { if ((gr) == 1 || (gr) == 5) war[((gr) == 5 ? 8 : 0) + (k)] = wfrag(wra + ((k) >> 1) * 7168 + ((k) & 1) * 4096, ((k) == 7 ? wrk : wra) + ((k) >> 1) * 7168 + ((k) & 1) * 4096 + 2048); } while (0)
    const unsigned bB = WS_BR + BR_SZ;
    const char* ofb = ws + bB + BR_O + (size_t)tt * 32768;
    u64 fl = 0;
    const unsigned fbo = WS_FB + (((16 * (lane % 3) + h) / 3) * 16 + ((lane & 4) && j > 0 ? j - 1 : j)) * 8;
    auto granule = [&](auto grc) __attribute__((always_inline)) {
        constexpr int gr = decltype(grc)::value, sa = gr % 3, sn = (gr + 1) % 3;
        STEP(sA, sB, pA0, pA1, pB0, pB1, RA(sa, 16384), RA(sa, 0), NOP_, NOP_, NOP_, NOP_);
        STEP(sB, sA, pB0, pB1, pA0, pA1, RA(sa, 24576), RA(sa, 8192), NOP_, NOP_, NOP_, NOP_);
        if (gr == 0 && g == 0) asm volatile("s_barrier" ::: "memory"); else asm volatile("s_waitcnt vmcnt(0)\n\ts_barrier" : "+v"(fl) :: "memory");
        STAMP(11 + gr);
        if (gr == 1) {
            if (tid == 0) set_flag(ws, WS_FB + (cb * 16 + j) * 8, tag);
            if (__builtin_expect(bid == 0 && w == 7, 0)) {
#pragma unroll 1
                for (int i = 0; i < 4; ++i) wait_flags_d(ws, WS_FA + (i * 64 + lane) * 8, tag);
                if (lane == 0) __hip_atomic_store((GAS u64*)(ws + WS_EPOCH), ep + 1, RLX_AGENT);
            }
        }
        if (gr == 2) asm volatile("global_load_dwordx2 %0, %1, %2 sc1" : "=v"(fl) : "v"(fbo), "s"(ws) : "memory");
        if (gr == 3) {
            if (g == 0) {
                int lo_ = lane; PIN(lo_);
                const unsigned long long M = (0x5555ull << ((lo_ & 31) + 18)) >> (4 * (lo_ >> 5));
                const unsigned bo = lds0 + L_BOP + t * 4096 + vl, bq = lds0 + L_BOP + 20480 + t * 2048 + vl;
                const bf16x8 bq0 = ldsr128(bq), bq1 = ldsr128(bq + 1024), bk0 = ldsr128(bo + 4096), bk1 = ldsr128(bo + 5120), bv0 = ldsr128(bo + 6144), bv1 = ldsr128(bo + 7168);
                const bf16x8 bp0 = ldsr128(bo), bp1 = ldsr128(bo + 1024), bpv = ldsr128(bo + 3072);
                int nlo = tile > 0 ? ~(unsigned)M : ~0u, nhi = ~(unsigned)(M >> 32);
                f32x16 cb, pb;
#pragma unroll
                for (int r = 0; r < 16; ++r) { const int cc = (r & 3) + 8 * (r >> 2);
                    int e0 = nhi << (31 - cc); PIN(e0); cb[r] = __uint_as_float((unsigned)(e0 >> 8) & 0xFF800000u);
                    if (r < 8) pb[r] = 0.f; else { int e1 = nlo << (31 - cc); PIN(e1); pb[r] = __uint_as_float((unsigned)(e1 >> 8) & 0xFF800000u); } }
                f32x16 sc = MFMA(bk0, bq0, cb); sc = MFMA(bk1, bq1, sc);
                f32x16 sp = MFMA(bp0, bq0, pb); sp = MFMA(bp1, bq1, sp);
                float m = max16(sc);
#pragma unroll
                for (int r = 8; r < 16; r += 2) m = __builtin_fmaxf(__builtin_fmaxf(m, sp[r]), sp[r + 1]);
                m = swap_max(m);
#pragma unroll
                for (int r = 0; r < 16; ++r) sc[r] = ex2(sc[r] - m);
#pragma unroll
                for (int r = 8; r < 16; ++r) sp[r] = ex2(sp[r] - m);
                u32x4 c0, c1, p0, p1; pack16(sc, c0, c1); pack16(sp, p0, p1);
                f32x16 ob = MFMA(bv0, asbf(c0), (f32x16){}); ob = MFMA(bv1, asbf(c1), ob); ob = MFMA(bpv, asbf(p1), ob);
                { const float rl = __builtin_amdgcn_rcpf(from_hi(ob[12]));
#pragma unroll
                  for (int i = 0; i < 16; ++i) ob[i] *= rl;
                  u32x4 e0, e1; pack16(ob, e0, e1); fix_pad(e1, 0u, mhi);
                  st16c(rs, e0, vl, bB + BR_O + (tile * 16 + h) * 2048); st16c(rs, e1, vl + 1024, bB + BR_O + (tile * 16 + h) * 2048); }
            }
        }
        if (gr == 4) { if (g == 0 && lane == 0) set_flag(ws, WS_FOB + (tile * 16 + h) * 8, tag); }
        if (gr == 5) asm volatile("global_load_dwordx2 %0, %1, %2 sc1" : "=v"(fl) : "v"(WS_FOB + (tt * 16 + (lane & 15)) * 8), "s"(ws) : "memory");
        if (gr == 6) { if (__builtin_expect(!__all(fl == tag), 0)) wait_flags_d(ws, WS_FOB + (tt * 16 + (lane & 15)) * 8, tag); }
        SBAR();
        STEP(sA, sB, pA0, pA1, pB0, pB1, RA(sn, 0), RA(sa, 16384), { DKV(g, gr, 0); DWO(ng, gr, 0); WAR(gr, 0); }, { DKV(g, gr, 1); DWO(ng, gr, 1); WAR(gr, 1); }, { DKV(g, gr, 2); DWO(ng, gr, 2); WAR(gr, 2); }, { DKV(g, gr, 3); DWO(ng, gr, 3); WAR(gr, 3); });
        if (gr == 2) {
            if (g == 0) {
                asm volatile("s_waitcnt vmcnt(4)" : "+v"(fl) :: "memory");
                if (__builtin_expect(!__all(fl == tag), 0)) wait_flags_d(ws, fbo, tag);
                const char* qo = ws + bB + BR_Q + (size_t)(h * 64 + tile) * 2048; const char* ko = ws + bB + BR_KV + (size_t)(h * 64 + tile) * 4096;
                const unsigned lq = lds0 + t * 2048, lk = lds0 + t * 4096;
                GLDSC(vl, qo, lq, L_BOP + 20480, 0); GLDSC(vl, qo, lq, L_BOP + 20480, 1024);
                GLDSC(vl, ko, lk, L_BOP + 4096, 0); GLDSC(vl, ko, lk, L_BOP + 4096, 1024); GLDSC(vl, ko, lk, L_BOP + 4096, 2048); GLDSC(vl, ko, lk, L_BOP + 4096, 3072);
                if (t == 0) { const char* po = tile > 0 ? ko - 4096 : ko; GLDSC(vl, po, lds0, L_BOP, 0); GLDSC(vl, po, lds0, L_BOP, 1024); GLDSC(vl, po, lds0, L_BOP, 3072); }
            }
        }
        SBAR();
        STEP(sB, sA, pB0, pB1, pA0, pA1, RA(sn, 8192), RA(sa, 24576), { DKV(ng, gr, 0); DWO(g, gr, 0); WAR(gr, 4); }, { DKV(ng, gr, 1); DWO(g, gr, 1); WAR(gr, 5); }, { DKV(ng, gr, 2); DWO(g, gr, 2); WAR(gr, 6); }, { DKV(ng, gr, 3); DWO(g, gr, 3); WAR(gr, 7); });
    };
    granule(IC<0>{}); granule(IC<1>{}); granule(IC<2>{}); granule(IC<3>{}); granule(IC<4>{}); granule(IC<5>{}); granule(IC<6>{}); granule(IC<7>{});
    o = MFMA(v0, asbf(pB0), o); o = MFMA(v1, asbf(pB1), o);
    STAMP(20);
    if (__builtin_expect(__any(hi && !(o[12] < 1.0e18f)), 0)) {
        if (hi) q1[2] = 0u;
        float m = -__builtin_inff(); o = (f32x16){};
        const unsigned kb = WS_BR + BR_KV + h * 262144 + g * 4096;
#pragma unroll 1
        for (int i = 0; i < 32; ++i) {
            const unsigned so = kb + i * 8192;
            const bf16x8 a0 = ld16c(rs, vl, so), a1 = ld16c(rs, vl + 1024, so), b0 = ld16c(rs, vl + 2048, so), b1 = ld16c(rs, vl + 3072, so);
            f32x16 s = MFMA(a0, q0, (f32x16){}); s = MFMA(a1, asbf(q1), s);
            const float mn = __builtin_fmaxf(m, swap_max(max16(s))), al = ex2(m - mn);
            m = mn;
#pragma unroll
            for (int r = 0; r < 16; ++r) { s[r] = ex2(s[r] - mn); o[r] *= al; }
            u32x4 e0, e1; pack16(s, e0, e1);
            o = MFMA(b0, asbf(e0), o); o = MFMA(b1, asbf(e1), o);
        }
        mref = m;
    }
    STAMP(28);
    const unsigned cbs = lds0 + L_SCR + t * 2048 + vl;
    if (g == 1) {
        u32x4 s0, s1; pack16(o, s0, s1);
        s1[2] = (s1[2] & ~mhi) | (__float_as_uint(o[12]) & mhi); s1[3] = (s1[3] & ~mhi) | (__float_as_uint(mref) & mhi);
        *(LAS u32x4*)(uintptr_t)cbs = s0; *(LAS u32x4*)(uintptr_t)(cbs + 1024) = s1;
    }
    STAMP(29);
    BAR_ONLY();
    STAMP(30);
    unsigned wq = wl + (g ? L_QM2 : L_QM0), wk = wq - (1024u & mhi), oa = lds0 + L_OF + g * 16384 + vl;
    const unsigned qst = g ? L_QM3 - L_QM2 : L_QM1 - L_QM0;
    unsigned wq1 = wq + qst, wk1 = wk + qst;
    PIN(wq); PIN(wk); PIN(wq1); PIN(wk1); PIN(oa);
    bf16x8 wf[4], of[4];
#define OPW(s) wfrag(((s) < 8 ? wq : wq1) + (((s) & 7) >> 1) * 7168 + ((s) & 1) * 4096, \
                     (((s) & 7) == 7 ? ((s) < 8 ? wk : wk1) : ((s) < 8 ? wq : wq1)) + (((s) & 7) >> 1) * 7168 + ((s) & 1) * 4096 + 2048)
#define OPROJ_B() do { \
        _Pragma("unroll") for (int i = 0; i < 4; ++i) { wf[i] = OPW(i); of[i] = ldsr128(oa + i * 1024); } \
        _Pragma("unroll") for (int i = 0; i < 16; ++i) { SBAR(); accB = MFMA(wf[i & 3], of[i & 3], accB); SBAR(); \
            if (i + 4 < 16) { wf[i & 3] = OPW(i + 4); of[i & 3] = ldsr128(oa + (i + 4) * 1024); } } SBAR(); } while (0)
    f32x16 accB = {};
    const float gate = __builtin_amdgcn_rcpf(1.0f + ex2(-1.4426950408889634f * glog)), g1 = 1.0f - gate;
    if (g == 1) {
        {   const char* b_ = wobp + (size_t)(28 + c * 7) * 3584; const unsigned lc7 = lds0 + c * 7168;
            DWP(b_, 0, lc7, L_QM1); DWP(b_, 1, lc7, L_QM1); DWP(b_, 2, lc7, L_QM1); DWP(b_, 3, lc7, L_QM1); DWP(b_, 4, lc7, L_QM1); DWP(b_, 5, lc7, L_QM1); DWP(b_, 6, lc7, L_QM1); }
        STAMP(21);
        OPROJ_B();
        STAMP(22);
        WAIT_BAR(0);
        STAMP(23);
        u64 pa, pb2; wait_flags(ws, WS_FOA + (tt * 16 + (lane & 15)) * 8, tag, pa, pb2);
        STAMP(25);
        {   const char* ofa = ws + WS_BR + BR_O + (size_t)tt * 32768 + c * 8192; const unsigned lr = lds0 + c * 8192;
            GLDSC(vl, ofa, lr, L_QM2, 0); GLDSC(vl, ofa, lr, L_QM2, 1024); GLDSC(vl, ofa, lr, L_QM2, 2048); GLDSC(vl, ofa, lr, L_QM2, 3072);
            GLDSC(vl, ofa + 4096, lr, L_QM2 + 4096, 0); GLDSC(vl, ofa + 4096, lr, L_QM2 + 4096, 1024); GLDSC(vl, ofa + 4096, lr, L_QM2 + 4096, 2048); GLDSC(vl, ofa + 4096, lr, L_QM2 + 4096, 3072); }
        WAIT_BAR(0);
        KEEP2(pa, pb2);
    } else {
        const u32x4 s0 = *(const LAS u32x4*)(uintptr_t)cbs, s1 = *(const LAS u32x4*)(uintptr_t)(cbs + 1024);
        const float l1 = from_hi(__uint_as_float(s1[2])), m1 = from_hi(__uint_as_float(s1[3])), l0 = from_hi(o[12]);
        const float mm = __builtin_fmaxf(mref, m1), a0 = ex2(mref - mm), a1 = ex2(m1 - mm);
        const float rl = __builtin_amdgcn_rcpf(l0 * a0 + l1 * a1), f0 = a0 * rl, f1 = a1 * rl;
        f32x16 v;
#pragma unroll
        for (int i = 0; i < 4; ++i) {
            v[2 * i] = o[2 * i] * f0 + __uint_as_float(s0[i] << 16) * f1; v[2 * i + 1] = o[2 * i + 1] * f0 + __uint_as_float(s0[i] & 0xffff0000u) * f1;
            v[8 + 2 * i] = o[8 + 2 * i] * f0 + __uint_as_float(s1[i] << 16) * f1; v[9 + 2 * i] = o[9 + 2 * i] * f0 + __uint_as_float(s1[i] & 0xffff0000u) * f1; }
        u32x4 e0, e1; pack16(v, e0, e1);
        fix_pad(e1, 0u, mhi);
        const unsigned so = WS_BR + BR_O + (tile * 16 + h) * 2048;
        st16c(rs, e0, vl, so); st16c(rs, e1, vl + 1024, so);
        STAMP(21);
        WAIT_BAR(2);
        STAMP(23);
        asm volatile("s_waitcnt vmcnt(0)" ::: "memory");
        if (lane == 0) set_flag(ws, WS_FOA + (tile * 16 + h) * 8, tag);
        STAMP(22);
        OPROJ_B();
        BAR_ONLY();
    }
    STAMP(26);
    f32x16 acc = {};
    {   unsigned ob_ = lds0 + L_QM2 + g * 16384 + vl; PIN(ob_);
        bf16x8 og[8];
#pragma unroll
        for (int i = 0; i < 8; ++i) og[i] = ldsr128(ob_ + i * 1024);
#pragma unroll
        for (int i = 0; i < 16; ++i) { SBAR(); acc = MFMA(war[i], og[i & 7], acc); SBAR(); if (i < 8) og[i] = ldsr128(ob_ + (i + 8) * 1024); }
        SBAR();
    }
#pragma unroll
    for (int i = 0; i < 16; ++i) acc[i] = acc[i] * gate + accB[i] * g1;
    const unsigned rb = lds0 + L_OF + c * 4096 + vl;
    if (g == 1) {
#pragma unroll
        for (int i = 0; i < 4; ++i) *(LAS f32x4*)(uintptr_t)(rb + i * 1024) = (f32x4){acc[4 * i], acc[4 * i + 1], acc[4 * i + 2], acc[4 * i + 3]};
    }
    BAR_ONLY();
    if (g == 0 && (cg < 3 || c >= 2)) {
#pragma unroll
        for (int i = 0; i < 4; ++i) { const f32x4 e = *(const LAS f32x4*)(uintptr_t)(rb + i * 1024);
#pragma unroll
            for (int k = 0; k < 4; ++k) acc[4 * i + k] += e[k]; }
        u32x4 s0_, s1_; pack16(acc, s0_, s1_);
        auto r0 = __builtin_amdgcn_permlane32_swap(s0_[0], s0_[2], false, false), r1 = __builtin_amdgcn_permlane32_swap(s0_[1], s0_[3], false, false);
        auto r2 = __builtin_amdgcn_permlane32_swap(s1_[0], s1_[2], false, false), r3 = __builtin_amdgcn_permlane32_swap(s1_[1], s1_[3], false, false);
        char* op = out + (size_t)(tt * 32 + r32) * 896 + (col0 + 32 * c) * 2 + hi * 16;
        *(GAS u32x4*)op = (u32x4){r0[0], r1[0], r0[1], r1[1]};
        *(GAS u32x4*)(op + 32) = (u32x4){r2[0], r3[0], r2[1], r3[1]};
    }
    STAMPF(27);
    asm volatile(".Lfk_end:");
}

extern "C" void kernel_launch(void* const* d_in, const int* in_sizes, int n_in,
                              void* d_out, int out_size, void* d_ws, size_t ws_size,
                              hipStream_t stream) {
    (void)in_sizes; (void)n_in; (void)out_size; (void)ws_size;
    (void)hipFuncSetAttribute((const void*)fused_kernel, hipFuncAttributeMaxDynamicSharedMemorySize, LDS_BYTES);
    fused_kernel<<<256, 512, LDS_BYTES, stream>>>((const char*)d_in[0], (const char*)d_in[1], (const char*)d_in[2], (const char*)d_in[4],
                                                  (const char*)d_in[5], (const char*)d_in[6], (char*)d_ws, (char*)d_out);
}
```

```cpp
#include <hip/hip_runtime.h>
#include <hip/hip_bf16.h>
#include <stdint.h>

typedef short bf16x8 __attribute__((ext_vector_type(8)));
typedef short s16x4 __attribute__((ext_vector_type(4)));
typedef float f32x16 __attribute__((ext_vector_type(16)));
typedef float f32x4 __attribute__((ext_vector_type(4)));
typedef float f32x2 __attribute__((ext_vector_type(2)));
typedef unsigned u32x4 __attribute__((ext_vector_type(4)));
typedef unsigned u32x2 __attribute__((ext_vector_type(2)));
typedef __bf16 bf2_t __attribute__((ext_vector_type(2)));
typedef unsigned long long u64;
#define LAS __attribute__((address_space(3)))
#define GAS __attribute__((address_space(1)))
#define DI __device__ __forceinline__
#define MFMA(a, b, c) __builtin_amdgcn_mfma_f32_32x32x16_bf16((a), (b), (c), 0, 0, 0)
#define SBAR() __builtin_amdgcn_sched_barrier(0)
#define PIN(x) asm volatile("" : "+v"(x))
#define RLX_AGENT __ATOMIC_RELAXED, __HIP_MEMORY_SCOPE_AGENT

template <int N> struct IC { static constexpr int value = N; };
constexpr float QSCALE = 0.18898223650461362f * 1.4426950408889634f;

constexpr unsigned WS_EPOCH = 0;
constexpr unsigned WS_FA = 4096;
constexpr unsigned WS_FB = WS_FA + 2048;
constexpr unsigned WS_FOA = WS_FB + 2048;
constexpr unsigned WS_FOB = WS_FOA + 8192;
constexpr unsigned WS_BR = 65536;
constexpr unsigned BR_Q = 0, BR_KV = 2097152, BR_O = 6291456, BR_SZ = 8388608;
constexpr unsigned WS_TOTAL = WS_BR + 2 * BR_SZ;

constexpr unsigned L_XB = 0;
constexpr unsigned L_KW = 98304;
constexpr unsigned L_GT = 155648;
constexpr unsigned L_CNT = 159232;
constexpr unsigned L_GL = 159744;
constexpr unsigned L_Q = 139264;
constexpr unsigned L_RING = 0;
constexpr unsigned L_SA = 98304, L_SB = 126976;
constexpr unsigned L_OF = 65536;
constexpr unsigned L_OA = 32768;
constexpr unsigned L_SCR = 155648;
constexpr unsigned LDS_BYTES = 163840;

DI unsigned cvtpk(float lo, float hi) { f32x2 v = {lo, hi}; return __builtin_bit_cast(unsigned, __builtin_convertvector(v, bf2_t)); }
#define GLDS(voff, sbase, lds_s, LC, OFF) asm volatile("s_add_i32 m0, %2, %3\n\ts_nop 0\n\tglobal_load_lds_dwordx4 %0, %1 offset:%4" \
        :: "v"(voff), "s"(sbase), "s"(__builtin_amdgcn_readfirstlane(lds_s)), "n"((int)(LC)), "n"((int)(OFF)) : "memory", "scc")
#define GLDSC(voff, sbase, lds_s, LC, OFF) asm volatile("s_add_i32 m0, %2, %3\n\ts_nop 0\n\tglobal_load_lds_dwordx4 %0, %1 offset:%4 sc1" \
        :: "v"(voff), "s"(sbase), "s"(__builtin_amdgcn_readfirstlane(lds_s)), "n"((int)(LC)), "n"((int)(OFF)) : "memory", "scc")
#define GLDS_IF(sel, voff, sbase, lds_s, LC, OFF) asm volatile("s_cmp_lg_u32 %5, 0\n\ts_cbranch_scc1 1f\n\ts_add_i32 m0, %2, %3\n\ts_nop 0\n\tglobal_load_lds_dwordx4 %0, %1 offset:%4\n1:" \
        :: "v"(voff), "s"(sbase), "s"(__builtin_amdgcn_readfirstlane(lds_s)), "n"((int)(LC)), "n"((int)(OFF)), "s"(sel) : "memory", "scc")
#define GLDSC_IF(sel, voff, sbase, lds_s, LC, OFF) asm volatile("s_cmp_lg_u32 %5, 0\n\ts_cbranch_scc1 1f\n\ts_add_i32 m0, %2, %3\n\ts_nop 0\n\tglobal_load_lds_dwordx4 %0, %1 offset:%4 sc1\n1:" \
        :: "v"(voff), "s"(sbase), "s"(__builtin_amdgcn_readfirstlane(lds_s)), "n"((int)(LC)), "n"((int)(OFF)), "s"(sel) : "memory", "scc")
#define WAIT_BAR(N) asm volatile("s_waitcnt vmcnt(" #N ") lgkmcnt(0)\n\ts_barrier" ::: "memory")
#define BAR_ONLY() asm volatile("s_waitcnt lgkmcnt(0)\n\ts_barrier" ::: "memory")
DI bf16x8 ldsr128(unsigned a) { return *(const LAS bf16x8*)(uintptr_t)a; }
typedef short v4i16_t __attribute__((ext_vector_type(4)));
DI s16x4 ldstr(unsigned a) { return __builtin_bit_cast(s16x4, __builtin_amdgcn_ds_read_tr16_b64_v4i16((LAS v4i16_t*)(uintptr_t)a)); }
DI bf16x8 wfrag(unsigned a0, unsigned a1) { s16x4 lo = ldstr(a0), hi = ldstr(a1); return __builtin_shufflevector(lo, hi, 0, 1, 2, 3, 4, 5, 6, 7); }
DI bf16x8 asbf(u32x4 v) { return __builtin_bit_cast(bf16x8, v); }
DI float ex2(float v) { return __builtin_amdgcn_exp2f(v); }
DI float swap_max(float m) {
    auto rr = __builtin_amdgcn_permlane32_swap(__float_as_uint(m), __float_as_uint(m), false, false);
    return __builtin_fmaxf(__uint_as_float(rr[0]), __uint_as_float(rr[1]));
}
DI float from_hi(float v) {
    auto rr = __builtin_amdgcn_permlane32_swap(__float_as_uint(v), __float_as_uint(v), false, false);
    return __uint_as_float(rr[1]);
}
DI void pack16(const f32x16& a, u32x4& s0, u32x4& s1) {
    s0 = (u32x4){cvtpk(a[0], a[1]), cvtpk(a[2], a[3]), cvtpk(a[4], a[5]), cvtpk(a[6], a[7])};
    s1 = (u32x4){cvtpk(a[8], a[9]), cvtpk(a[10], a[11]), cvtpk(a[12], a[13]), cvtpk(a[14], a[15])};
}
DI float max16(const f32x16& s) {
    float m = __builtin_fmaxf(s[0], s[1]);
#pragma unroll
    for (int r = 2; r < 16; r += 2) m = __builtin_fmaxf(__builtin_fmaxf(m, s[r]), s[r + 1]);
    return m;
}
typedef __amdgpu_buffer_rsrc_t rsrc_t;
DI void st16c(rsrc_t r, u32x4 v, unsigned voff, unsigned soff) { __builtin_amdgcn_raw_buffer_store_b128(v, r, voff, soff, 16); }
DI bf16x8 ld16c(rsrc_t r, unsigned voff, unsigned soff) { return asbf(__builtin_amdgcn_raw_buffer_load_b128(r, voff, soff, 16)); }
DI void wait_flags(const char* ws, unsigned off, u64 tag, u64& a, u64& b) {
    asm volatile("s_waitcnt vmcnt(0)\n\tglobal_load_dwordx2 %0, %1, %2 sc1" : "=v"(a) : "v"(off), "s"(ws) : "memory");
#pragma unroll 1
    for (int it = 0; it < 30000; ++it) {
        asm volatile("s_sleep 2\n\tglobal_load_dwordx2 %0, %2, %3 sc1\n\ts_waitcnt vmcnt(1)" : "=&v"(b), "+v"(a) : "v"(off), "s"(ws) : "memory");
        if (__all(a == tag)) break;
        asm volatile("s_sleep 2\n\tglobal_load_dwordx2 %0, %2, %3 sc1\n\ts_waitcnt vmcnt(1)" : "=&v"(a), "+v"(b) : "v"(off), "s"(ws) : "memory");
        if (__all(b == tag)) break;
    }
}
#define KEEP2(a, b) asm volatile("" :: "v"(a), "v"(b))
DI void wait_flags_d(const char* ws, unsigned off, u64 tag) { u64 a, b; wait_flags(ws, off, tag, a, b); asm volatile("s_waitcnt vmcnt(0)" : "+v"(a), "+v"(b) :: "memory"); }
DI void set_flag(char* ws, unsigned off, u64 tag) { __hip_atomic_store((GAS u64*)(ws + off), tag, RLX_AGENT); }

#define STAMP(i) do {} while (0)
#define STAMPF(i) do {} while (0)
#define STARGS
#define STPASS

DI void fix_v(u32x4& s0, u32x4& s1, unsigned m28) {
#pragma unroll
    for (int i = 0; i < 4; ++i) { s0[i] = (s0[i] & ~m28) | (0x3F803F80u & m28); s1[i] = (s1[i] & ~m28) | (0x3F803F80u & m28); }
}
DI void fix_pad(u32x4& s1, unsigned pad, unsigned mhi) { s1[2] = (s1[2] & ~mhi) | (pad & mhi); s1[3] &= ~mhi; }
template <bool V>
DI void pack_unit(f32x16 a, int u, unsigned m28, unsigned mhi, u32x4& s0, u32x4& s1) {
    if (!V) {
        const bool isq = u < 16;
        const float sc = isq ? QSCALE : 1.0f;
#pragma unroll
        for (int i = 0; i < 16; ++i) a[i] *= sc;
        pack16(a, s0, s1);
        fix_pad(s1, isq ? 0u : 0x00003F80u, mhi);
    } else { pack16(a, s0, s1); fix_v(s0, s1, m28); }
}
#define MKTAG() do { u64 z = ((u64)__builtin_amdgcn_readfirstlane((unsigned)(ep >> 32)) << 32) | (unsigned)__builtin_amdgcn_readfirstlane((unsigned)ep); \
        z += 0x9E3779B97F4A7C15ull; z = (z ^ (z >> 30)) * 0xBF58476D1CE4E5B9ull; z = (z ^ (z >> 27)) * 0x94D049BB133111EBull; tag = z ^ (z >> 31); } while (0)
#define XB(i) (L_XB + (i) * 16384)
#define KW(i) (L_KW + (i) * 8192)
#define MM(VV, acc, wf, xfr) do { if (VV) acc = MFMA(xfr, wf, acc); else acc = MFMA(wf, xfr, acc); } while (0)
__device__ constexpr unsigned so_of(int c) { return c == 0 ? KW(0) : (c == 6 ? KW(2) : XB(c)); }
__device__ constexpr unsigned xo_of(int c) { return c == 6 ? XB(0) : XB(c); }

template <bool VK>
DI void stage1(bf16x8 (&xf)[28], const unsigned (&xa)[4], const unsigned (&xh)[4], unsigned ka0, unsigned ka1, rsrc_t rs, int h, int tile, int lane, unsigned m28, unsigned mhi,
               unsigned gv0, unsigned gv1, unsigned dmc, u64 ep, u64& tag, unsigned lds0 STARGS) {
    f32x16 acc0 = {}, acc1 = {};
    bf16x8 w0[2], w1[2];
#define XR(s) xf[s] = ldsr128(xo_of((s) >> 2) >= 65536 ? xh[(s) & 3] + (xo_of((s) >> 2) - 65536) : xa[(s) & 3] + xo_of((s) >> 2))
#define KF(base, s) wfrag((base) + (s) * 2048, (base) + (s) * 2048 + 512)
    XR(0); w0[0] = KF(ka0, 0); w1[0] = KF(ka1, 0); XR(1); w0[1] = KF(ka0, 1); w1[1] = KF(ka1, 1);
#pragma unroll
    for (int s = 0; s < 28; ++s) {
        if ((s & 3) == 2 && s < 26) { BAR_ONLY(); if (s == 10) STAMP(3); if (s == 22) STAMP(4); }
        if (s == 20) {
            asm volatile("s_waitcnt vmcnt(0)" : "+v"(gv0), "+v"(gv1), "+v"(ep) : "v"(dmc) : "memory");
            const int tid = threadIdx.x;
            { u32x2 e = {gv0, 0u}; *(LAS u32x2*)(uintptr_t)(lds0 + L_GT + tid * 8) = e; }
            if (tid < 192) { u32x2 e = {gv1, 0u}; *(LAS u32x2*)(uintptr_t)(lds0 + L_GT + 2048 + tid * 8) = e; }
            if (tid == 0) *(LAS unsigned*)(uintptr_t)(lds0 + L_CNT) = 0u;
            MKTAG();
        }
        SBAR();
        MM(VK, acc0, w0[s & 1], xf[s]); SBAR();
        if (s + 2 < 28) { XR(s + 2); w0[s & 1] = KF(ka0, s + 2); }
        SBAR();
        MM(VK, acc1, w1[s & 1], xf[s]); SBAR();
        if (s + 2 < 28) w1[s & 1] = KF(ka1, s + 2);
    }
    SBAR();
    u32x4 s0, s1;
    pack16(acc0, s0, s1); if (!VK) fix_pad(s1, 0x00003F80u, mhi); else fix_v(s0, s1, m28);
    const unsigned off = WS_BR + BR_KV + ((2 * h & 15) * 64 + tile) * 4096 + (VK ? 2048 : 0);
    st16c(rs, s0, lane * 16, off); st16c(rs, s1, lane * 16 + 1024, off);
    pack16(acc1, s0, s1); if (!VK) fix_pad(s1, 0x00003F80u, mhi); else fix_v(s0, s1, m28);
    st16c(rs, s0, lane * 16, off + 262144); st16c(rs, s1, lane * 16 + 1024, off + 262144);
}
template <bool V01, bool V2>
DI void stage2(const bf16x8 (&xf)[28], unsigned ql, unsigned qh, unsigned gl, bool spec, unsigned wlo, unsigned whi, char* ws, rsrc_t rs, unsigned lds0,
               int h, int cb, int j, int t, int lane, u64 tag, u32x4 (&bp)[6], unsigned m28, unsigned mhi STARGS) {
    f32x16 aq = {}, acc0 = {}, acc1 = {}, acc2 = {};
    bf16x8 wq[2], w0[2], w1[2], w2[2];
    const unsigned gstride = spec ? 32u : 256u;
    bool sent = false;
#define SADDR(lo, hi_, c) (so_of(c) >= 65536 ? (hi_) + (so_of(c) - 65536) : (lo) + so_of(c))
#define WF(s, u) wfrag(SADDR(wlo, whi, (s) >> 2) + ((s) & 3) * 3072 + (u) * 56, SADDR(wlo, whi, (s) >> 2) + ((s) & 3) * 3072 + 768 + (u) * 56)
#define QR(s) do { const unsigned a_ = spec ? gl + 128 * (s) : SADDR(ql, qh, (s) >> 2) + ((s) & 3) * 1024; wq[(s) & 1] = wfrag(a_, a_ + gstride); } while (0)
    QR(0); w0[0] = WF(0, 0); w1[0] = WF(0, 1); w2[0] = WF(0, 2); QR(1); w0[1] = WF(1, 0); w1[1] = WF(1, 1); w2[1] = WF(1, 2);
#pragma unroll
    for (int s = 0; s < 28; ++s) {
        if (s == 6 || s == 10 || s == 14 || s == 18) { BAR_ONLY(); if (s == 10) STAMP(6); if (s == 18) STAMP(7); }
        if (s >= 7 && s <= 15) {
            if (s == 15) asm volatile("s_waitcnt vmcnt(0)" ::: "memory");
            if (!sent && (s == 15 || (__builtin_amdgcn_s_getreg(7 | (3 << 11)) & 15) == 0)) {
                sent = true; asm volatile("" ::: "memory");
                if (lane == 0) { if (__hip_atomic_fetch_add((LAS unsigned*)(uintptr_t)(lds0 + L_CNT), 1u, __ATOMIC_RELAXED, __HIP_MEMORY_SCOPE_WORKGROUP) == 3u) set_flag(ws, WS_FA + (h * 16 + j) * 8, tag); }
                STAMP(8);
            }
        }
        SBAR();
        aq = MFMA(wq[s & 1], xf[s], aq); SBAR();
        if (s + 2 < 28) QR(s + 2);
        SBAR();
        MM(V01, acc0, w0[s & 1], xf[s]); SBAR();
        if (s + 2 < 28) w0[s & 1] = WF(s + 2, 0);
        SBAR();
        MM(V01, acc1, w1[s & 1], xf[s]); SBAR();
        if (s + 2 < 28) w1[s & 1] = WF(s + 2, 1);
        SBAR();
        MM(V2, acc2, w2[s & 1], xf[s]); SBAR();
        if (s + 2 < 28) w2[s & 1] = WF(s + 2, 2);
    }
    SBAR();
    {
        if (lane >> 5) *(LAS float*)(uintptr_t)(lds0 + L_GL + t * 128 + (lane & 31) * 4) = aq[12];
#pragma unroll
        for (int i = 0; i < 16; ++i) aq[i] *= QSCALE;
        u32x4 s0, s1; pack16(aq, s0, s1);
        fix_pad(s1, 0u, mhi);
        *(LAS u32x4*)(uintptr_t)(lds0 + L_Q + t * 2048 + lane * 16) = s0; *(LAS u32x4*)(uintptr_t)(lds0 + L_Q + t * 2048 + 1024 + lane * 16) = s1;
    }
    pack_unit<V01>(acc0, 3 * cb, m28, mhi, bp[0], bp[1]); pack_unit<V01>(acc1, 3 * cb + 1, m28, mhi, bp[2], bp[3]); pack_unit<V2>(acc2, 3 * cb + 2, m28, mhi, bp[4], bp[5]);
}

__global__ __launch_bounds__(512, 2) void fused_kernel(const char* __restrict__ x, const char* __restrict__ wa, const char* __restrict__ woa,
                                                       const char* __restrict__ wb, const char* __restrict__ wob, const char* __restrict__ gw,
                                                       char* ws, char* __restrict__ out) {
    extern __shared__ __attribute__((aligned(16))) char smem[];
    const unsigned lds0 = (unsigned)(uintptr_t)(LAS char*)smem;
    const int tid = threadIdx.x, lane = tid & 63, w = __builtin_amdgcn_readfirstlane(tid >> 6), t = w & 3, g = w >> 2;
    const int bid = blockIdx.x, xg = bid & 7, idx = bid >> 3;
    const int h = (xg >> 1) * 4 + (idx & 3), j = (xg & 1) * 8 + (idx >> 2);
    const int r32 = lane & 31, hi = lane >> 5;
    const rsrc_t rs = __builtin_amdgcn_make_buffer_rsrc(ws, 0, WS_TOTAL, 0x00020000);
    const unsigned vl = lane * 16;
    asm volatile("" :: "s"(x), "s"(wa), "s"(woa), "s"(wb), "s"(wob), "s"(gw), "s"(ws), "s"(out));
    u64 pc; unsigned len;
    asm volatile("s_getpc_b64 %0\n.Lfk_ref:\n\ts_mov_b32 %1, .Lfk_end-.Lfk_ref" : "=s"(pc), "=s"(len));
    STAMPF(0);
    const int cb = (int)((0xCFEDBA8967542103ull >> (4 * h)) & 15);
    const int tile = 4 * j + t;
    const int rot = (j + 2 * (h & 3)) % 7;
#define CK(p) ((p) + rot >= 7 ? (p) + rot - 7 : (p) + rot)
    const unsigned vxe = (lane >> 3) * 896 + (((lane & 7) ^ (lane >> 4)) << 4), lx = lds0 + t * 4096;
    const char* xb = x + (size_t)(j * 128 + t * 32) * 896;
    u64 ep, tag;
    unsigned m28 = r32 == 28 ? ~0u : 0u, mhi = hi ? ~0u : 0u; PIN(m28); PIN(mhi);
    if (g == 0) {
        unsigned gv0, gv1, dmc;
        { const char* p_ = xb + rot * 128; GLDS(vxe, p_, lx, XB(0), 0); GLDS((vxe ^ 64u) + 7168, p_, lx, XB(0) + 1024, 0); GLDS(vxe + 14336, p_, lx, XB(0) + 2048, 0); GLDS((vxe ^ 64u) + 21504, p_, lx, XB(0) + 3072, 0); }
        {
            const unsigned co = (unsigned)((idx * 4 + t) * 4 + (lane & 3)) * 128u; const int pl = lane - 8;
            const char* tp = (lane < 8 || lane > 20) ? (const char*)pc + (co < len ? co : 0u)
                           : (pl < 9 ? ws + (size_t)pl * 2097152 + 32768 : (pl == 9 ? (const char*)out : (pl == 10 ? woa : (pl == 11 ? wob : wb))));
            asm volatile("global_load_ubyte %0, %1, off sc1" : "=v"(dmc) : "v"(tp) : "memory");
        }
        asm volatile("global_load_ushort %0, %1, %2" : "=v"(gv0) : "v"((CK(t) * 64 + lane) * 2), "s"(gw) : "memory");
        asm volatile("global_load_ushort %0, %1, %2" : "=v"(gv1) : "v"((CK(t < 3 ? 4 + t : 6) * 64 + lane) * 2), "s"(gw) : "memory");
        asm volatile("global_load_dwordx2 %0, %1, %2 sc1" : "=v"(ep) : "v"(0u), "s"(ws) : "memory");
        const int fr = (r32 >> 1) & 7;
        unsigned xa[4], xh[4];
#pragma unroll
        for (int ks = 0; ks < 4; ++ks) { xa[ks] = lds0 + (4 * t + (r32 >> 3)) * 1024 + (r32 & 7) * 128 + (((2 * ks + hi) ^ fr) << 4); xh[ks] = xa[ks] + 65536; PIN(xa[ks]); PIN(xh[ks]); }
        const int q4 = (lane & 15) >> 2, p4 = lane & 3, ch = (lane >> 4) & 1;
        const unsigned o0 = ch * 32 + p4 * 8, o1 = o0 + 56, sw = (q4 >> 1) << 6;
        unsigned ka0 = lds0 + L_KW + (8 * hi + q4) * 128 + (o0 ^ sw), ka1 = lds0 + L_KW + (8 * hi + q4) * 128 + (o1 ^ sw); PIN(ka0); PIN(ka1);
        unsigned ql = lds0 + (8 * hi + q4) * 64 + ch * 32 + p4 * 8 + (h & 1) * 8, qh = ql + 65536; PIN(ql); PIN(qh);
        unsigned wlo = lds0 + 4096 + (8 * hi + q4) * 192 + ch * 32 + p4 * 8 + (cb & 1) * 8, whi = wlo + 65536; PIN(wlo); PIN(whi);
        const bool spec = ch == 1 && p4 == 3;
        const unsigned gl = lds0 + L_GT + 64 * hi + 8 * q4;
        bf16x8 xf[28]; u32x4 bp[6];
        STAMP(1);
        WAIT_BAR(4); STAMP(2);
        if (h < 8) stage1<false>(xf, xa, xh, ka0, ka1, rs, h, tile, lane, m28, mhi, gv0, gv1, dmc, ep, tag, lds0 STPASS); else stage1<true>(xf, xa, xh, ka0, ka1, rs, h, tile, lane, m28, mhi, gv0, gv1, dmc, ep, tag, lds0 STPASS);
        BAR_ONLY(); STAMP(5);
        if (cb <= 9) stage2<false, false>(xf, ql, qh, gl, spec, wlo, whi, ws, rs, lds0, h, cb, j, t, lane, tag, bp, m28, mhi STPASS);
        else if (cb == 10) stage2<false, true>(xf, ql, qh, gl, spec, wlo, whi, ws, rs, lds0, h, cb, j, t, lane, tag, bp, m28, mhi STPASS);
        else stage2<true, true>(xf, ql, qh, gl, spec, wlo, whi, ws, rs, lds0, h, cb, j, t, lane, tag, bp, m28, mhi STPASS);
        STAMP(9);
        BAR_ONLY();
#pragma unroll
        for (int i = 0; i < 3; ++i) { const int u = 3 * cb + i;
            const unsigned off = WS_BR + BR_SZ + (u < 16 ? BR_Q + (u * 64 + tile) * 2048 : BR_KV + ((u & 15) * 64 + tile) * 4096 + (u >= 32 ? 2048 : 0));
            st16c(rs, bp[2 * i], vl, off); st16c(rs, bp[2 * i + 1], vl + 1024, off); }
        STAMP(10);
        const int qp = t & 1, kh = t >> 1;
        const unsigned qad = lds0 + L_Q + qp * 4096 + vl;
        const bf16x8 qa0 = ldsr128(qad), qb0 = ldsr128(qad + 2048);
        u32x4 qa1 = __builtin_bit_cast(u32x4, ldsr128(qad + 1024)), qb1 = __builtin_bit_cast(u32x4, ldsr128(qad + 3072));
        unsigned ac = lds0 + L_RING + kh * 4096 + vl, an = ac + 32768, af = ac + 65536;
        f32x16 oa = {}, ob = {}, sA, sB; u32x4 pA0 = {}, pA1 = {}, pB0 = {}, pB1 = {};
        bf16x8 k0 = ldsr128(ac), k1 = ldsr128(ac + 1024), v0 = {}, v1 = {}, kn0, kn1;
        float mra, mrb;
        {
            sA = MFMA(k0, qa0, (f32x16){}); sB = MFMA(k0, qb0, (f32x16){});
            f32x16 ta = MFMA(k1, asbf(qa1), sA); sB = MFMA(k1, asbf(qb1), sB);
            const unsigned na = cvtpk(-swap_max(max16(ta)), 0.f), nb = cvtpk(-swap_max(max16(sB)), 0.f);
            mra = -__uint_as_float(na << 16); mrb = -__uint_as_float(nb << 16);
            qa1[2] = (qa1[2] & ~mhi) | (na & mhi); qb1[2] = (qb1[2] & ~mhi) | (nb & mhi);
            sA = MFMA(k1, asbf(qa1), sA);
        }
#define GRP(S, b, SP, bp, PP, jp) do { PP[jp] = cvtpk(SP[bp], SP[bp + 1]); PP[jp + 1] = cvtpk(SP[bp + 2], SP[bp + 3]); SBAR(); \
        S[b] = ex2(S[b]); S[b + 1] = ex2(S[b + 1]); S[b + 2] = ex2(S[b + 2]); S[b + 3] = ex2(S[b + 3]); PIN(PP); PIN(S); SBAR(); } while (0)
#define STEP2(KC0, KC1, KN0, KN1, ka, va) do { \
        KN0 = ldsr128(ka); ob = MFMA(v0, asbf(pB0), ob); SBAR(); GRP(sA, 0, sB, 12, pB1, 2); v0 = ldsr128((va) + 2048); \
        sB = MFMA(KC0, qb0, (f32x16){}); SBAR(); KN1 = ldsr128((ka) + 1024); GRP(sA, 4, sA, 0, pA0, 0); \
        sB = MFMA(KC1, asbf(qb1), sB); SBAR(); GRP(sA, 8, sA, 4, pA0, 2); \
        ob = MFMA(v1, asbf(pB1), ob); SBAR(); v1 = ldsr128((va) + 3072); GRP(sA, 12, sA, 8, pA1, 0); \
        oa = MFMA(v0, asbf(pA0), oa); SBAR(); GRP(sB, 0, sA, 12, pA1, 2); \
        sA = MFMA(KN0, qa0, (f32x16){}); SBAR(); GRP(sB, 4, sB, 0, pB0, 0); \
        sA = MFMA(KN1, asbf(qa1), sA); SBAR(); GRP(sB, 8, sB, 4, pB0, 2); \
        oa = MFMA(v1, asbf(pA1), oa); SBAR(); GRP(sB, 12, sB, 8, pB1, 0); \
    } while (0)
        sB = (f32x16){};
#pragma unroll 1
        for (int gr = 0; gr < 8; ++gr) {
            STEP2(k0, k1, kn0, kn1, ac + 8192, ac);
            STEP2(kn0, kn1, k0, k1, ac + 16384, ac + 8192);
            if (gr == 0) asm volatile("s_barrier" ::: "memory"); else asm volatile("s_waitcnt vmcnt(0)\n\ts_barrier" ::: "memory");
            STAMP(11 + gr);
            SBAR();
            STEP2(k0, k1, kn0, kn1, ac + 24576, ac + 16384);
            STEP2(kn0, kn1, k0, k1, an, ac + 24576);
            const unsigned tmp = ac; ac = an; an = af; af = tmp;
        }
        pB1[2] = cvtpk(sB[12], sB[13]); pB1[3] = cvtpk(sB[14], sB[15]);
        ob = MFMA(v0, asbf(pB0), ob); ob = MFMA(v1, asbf(pB1), ob);
        STAMP(20);
        if (__builtin_expect(__any(hi && !(oa[12] < 1.0e18f && ob[12] < 1.0e18f)), 0)) {
            qa1[2] &= ~mhi; qb1[2] &= ~mhi;
            mra = -__builtin_inff(); mrb = mra; oa = (f32x16){}; ob = (f32x16){};
            const unsigned kb = WS_BR + BR_KV + h * 262144 + kh * 4096;
#pragma unroll 1
            for (int i = 0; i < 32; ++i) {
                const unsigned so = kb + i * 8192;
                const bf16x8 a0 = ld16c(rs, vl, so), a1 = ld16c(rs, vl + 1024, so), b0 = ld16c(rs, vl + 2048, so), b1 = ld16c(rs, vl + 3072, so);
#define ONLINE(Q0, Q1, O, M) do { f32x16 s_ = MFMA(a0, Q0, (f32x16){}); s_ = MFMA(a1, asbf(Q1), s_); \
                const float mn = __builtin_fmaxf(M, swap_max(max16(s_))), al = ex2(M - mn); M = mn; \
                _Pragma("unroll") for (int r = 0; r < 16; ++r) { s_[r] = ex2(s_[r] - mn); O[r] *= al; } \
                u32x4 e0, e1; pack16(s_, e0, e1); O = MFMA(b0, asbf(e0), O); O = MFMA(b1, asbf(e1), O); } while (0)
                ONLINE(qa0, qa1, oa, mra); ONLINE(qb0, qb1, ob, mrb);
            }
        }
        {   const unsigned cw_ = lds0 + L_OA + t * 4096 + vl;
#define PUTP(O, M) do { O[13] = __uint_as_float((__float_as_uint(O[13]) & ~mhi) | (__float_as_uint(M) & mhi)); \
            _Pragma("unroll") for (int i = 0; i < 4; ++i) *(LAS f32x4*)(uintptr_t)(cw_ + i * 1024) = (f32x4){O[4 * i], O[4 * i + 1], O[4 * i + 2], O[4 * i + 3]}; } while (0)
            if (kh) { PUTP(oa, mra); oa = ob; mra = mrb; } else PUTP(ob, mrb);
        }
        STAMP(29);
        BAR_ONLY();
        STAMP(30);
        {   const unsigned cbs = lds0 + L_OA + (t ^ 2) * 4096 + vl;
            f32x16 o1;
#pragma unroll
            for (int i = 3; i >= 0; --i) { const f32x4 e = *(const LAS f32x4*)(uintptr_t)(cbs + i * 1024); o1[4 * i] = e[0]; o1[4 * i + 1] = e[1]; o1[4 * i + 2] = e[2]; o1[4 * i + 3] = e[3]; }
            const float m1 = from_hi(o1[13]);
            const float mm = __builtin_fmaxf(mra, m1), a0 = ex2(mra - mm), a1 = ex2(m1 - mm);
            const float rl = __builtin_amdgcn_rcpf(from_hi(oa[12] * a0 + o1[12] * a1)), f0 = a0 * rl, f1 = a1 * rl;
            f32x16 v;
#pragma unroll
            for (int i = 0; i < 16; ++i) v[i] = oa[i] * f0 + o1[i] * f1;
            u32x4 e0, e1; pack16(v, e0, e1);
            fix_pad(e1, 0u, mhi);
            const int ot = 4 * j + 2 * qp + kh;
            const unsigned so = WS_BR + BR_O + (ot * 16 + h) * 2048;
            st16c(rs, e0, vl, so); st16c(rs, e1, vl + 1024, so);
            STAMP(21);
            asm volatile("s_waitcnt lgkmcnt(0)\n\ts_barrier\n\ts_waitcnt vmcnt(0)" ::: "memory");
            if (lane == 0) set_flag(ws, WS_FOA + (ot * 16 + h) * 8, tag);
            STAMP(22);
        }
    } else {
        const unsigned vx[4] = {vxe, (vxe ^ 64u) + 7168, vxe + 14336, (vxe ^ 64u) + 21504};
        const unsigned kc = (lane & 7) ^ (((lane >> 4) & 1) << 2);
        const unsigned vk0 = (lane >> 3) * 2688 + (kc > 6 ? 6 : kc) * 16, vk1 = vk0 + 8 * 2688;
        const char* wkb = wa + 896 + 112 * h + (size_t)(16 * t) * 2688;
        const unsigned lk = lds0 + t * 2048;
#define DMA_K(ck) do { const char* p_ = wkb + (size_t)CK(ck) * 64 * 2688; GLDS(vk0, p_, lk, KW(ck), 0); GLDS(vk1, p_, lk, KW(ck) + 1024, 0); } while (0)
#define DMA_X(cx) do { const char* p_ = xb + CK(cx) * 128; GLDS(vx[0], p_, lx, xo_of(cx), 0); GLDS(vx[1], p_, lx, xo_of(cx) + 1024, 0); \
        GLDS(vx[2], p_, lx, xo_of(cx) + 2048, 0); GLDS(vx[3], p_, lx, xo_of(cx) + 3072, 0); } while (0)
        asm volatile("global_load_dwordx2 %0, %1, %2 sc1" : "=v"(ep) : "v"(0u), "s"(ws) : "memory");
        DMA_K(0); DMA_X(1); DMA_K(1);
        unsigned vw[3];
#pragma unroll
        for (int n = 0; n < 3; ++n) { const unsigned L = 64 * n + lane, row = (L * 43691u) >> 19, cc = L - row * 12; vw[n] = row * 2688 + (cc > 10 ? 10 : cc) * 16; }
        const unsigned vq = (lane >> 2) * 2688 + (lane & 3) * 16;
        const char* wqb = wa + (56 * h - 8 * (h & 1)) + (size_t)(16 * t) * 2688;
        const char* wbb = wb + (168 * cb - 8 * (cb & 1)) + (size_t)(16 * t) * 2688;
        const unsigned lq = lds0 + t * 1024, lw = lds0 + 4096 + t * 3072;
#define DMA_S(ck) do { GLDS(vq, wqb + (size_t)CK(ck) * 64 * 2688, lq, so_of(ck), 0); const char* p_ = wbb + (size_t)CK(ck) * 64 * 2688; \
        GLDS(vw[0], p_, lw, so_of(ck), 0); GLDS(vw[1], p_, lw, so_of(ck) + 1024, 0); GLDS(vw[2], p_, lw, so_of(ck) + 2048, 0); } while (0)
        STAMP(1);
        WAIT_BAR(6); STAMP(2); DMA_X(2); DMA_K(2);
        WAIT_BAR(6); DMA_X(3); DMA_K(3);
        WAIT_BAR(6); DMA_X(4); DMA_K(4);
        WAIT_BAR(6); STAMP(3); DMA_X(5); DMA_K(5); DMA_S(0);
        WAIT_BAR(10); DMA_X(6); DMA_K(6); DMA_S(1);
        WAIT_BAR(14);
        WAIT_BAR(4); STAMP(4); DMA_S(2);
        asm volatile("" : "+v"(ep)); MKTAG();
        const unsigned fo = WS_FA + ((((lane >> 4) & 1) * 8 + (h >> 1)) * 16 + (lane & 15)) * 8;
        WAIT_BAR(4); STAMP(5); DMA_S(3);
        WAIT_BAR(4); DMA_S(4); DMA_S(5);
        WAIT_BAR(8); STAMP(6); DMA_S(6);
        unsigned dm0;
        {
            const int li = (t & 1) * 56 + (lane < 56 ? lane : 55), c0 = (h & 3) < 3 ? (h & 3) * 256 : 640;
            asm volatile("global_load_ubyte %0, %1, %2" : "=v"(dm0) : "v"(((j & 7) * 56 + (li >> 1)) * 896 + (li & 1) * 128 + c0), "s"((t & 2) ? wob : woa) : "memory"); }
        WAIT_BAR(9);
        WAIT_BAR(0);
        asm volatile("" :: "v"(dm0));
        STAMP(7);
        u64 pa, pb2; wait_flags(ws, fo, tag, pa, pb2);
        STAMP(8);
        const char* kv0 = ws + WS_BR + BR_KV + (size_t)h * 262144 + t * 8192; const unsigned lr = lds0 + t * 8192;
#define KV4(p, LC) do { GLDSC(vl, p, lr, LC, 0); GLDSC(vl, p, lr, LC, 1024); GLDSC(vl, p, lr, LC, 2048); GLDSC(vl, p, lr, LC, 3072); } while (0)
        KV4(kv0, L_RING); KV4(kv0 + 4096, L_RING + 4096); KV4(kv0 + 32768, L_RING + 32768); KV4(kv0 + 36864, L_RING + 36864);
        STAMP(9);
        WAIT_BAR(8);
        KEEP2(pa, pb2);
        STAMP(10);
        const int tt = 4 * j + (h >> 2), cg = h & 3, c = t;
        float glog = *(const LAS float*)(uintptr_t)(lds0 + L_GL + (h >> 2) * 128 + r32 * 4);
        const int col0 = cg < 3 ? cg * 128 : 320;
        const unsigned vwo = (lane >> 4) * 896 + (((lane & 15) ^ ((lane >> 4) << 2)) << 4), lc7 = lds0 + c * 7168;
        const char* wobp = wob + col0 * 2 + (size_t)(c * 7) * 3584; const char* woap = woa + col0 * 2 + (size_t)(c * 7) * 3584;
#define DWP(base, p, LC) do { if ((p) & 1) GLDS(vwo, (base) + (size_t)((p) - 1) * 3584, lc7, (LC) + 4096 + (p) * 1024 - 3584 - 4096, 3584); else GLDS(vwo, (base) + (size_t)(p) * 3584, lc7, (LC) + (p) * 1024, 0); } while (0)
#define DWQ(base, q, LC) do { const char* b_ = (base) + (size_t)(q) * 28 * 3584; DWP(b_, 0, LC); DWP(b_, 1, LC); DWP(b_, 2, LC); DWP(b_, 3, LC); DWP(b_, 4, LC); DWP(b_, 5, LC); DWP(b_, 6, LC); } while (0)
#define DWQ0(base, q) do { const char* b_ = (base) + (size_t)(q) * 28 * 3584; GLDS(vwo, b_, lc7, 0, 0); GLDS(vwo, b_ + 3584, lc7, 1024, 0); GLDS(vwo, b_ + 2 * 3584, lc7, 2048, 0); \
        GLDS(vwo, b_ + 3 * 3584, lc7, 3072, 0); DWP(b_, 4, 0); DWP(b_, 5, 0); DWP(b_, 6, 0); } while (0)
#define KVG(gr) do { const char* p_ = kv0 + (size_t)(gr) * 32768; KV4(p_, L_RING + ((gr) % 3) * 32768); KV4(p_ + 4096, L_RING + ((gr) % 3) * 32768 + 4096); } while (0)
        const int q4 = (lane & 15) >> 2, p4 = lane & 3, ch = (lane >> 4) & 1;
        const unsigned wl = lds0 + (4 * hi + q4) * 256 + (((c ^ q4) << 6) + ch * 32 + p4 * 8);
        unsigned wsa = wl + L_SA, wsk = wsa - (1024u & mhi); PIN(wsa); PIN(wsk);
        unsigned wz = wl, wzk = wl - (1024u & mhi); PIN(wz); PIN(wzk);
#define WFR(B, BK, off, k) wfrag((B) + (off) + ((k) >> 1) * 7168 + ((k) & 1) * 4096, ((k) == 7 ? (BK) : (B)) + (off) + ((k) >> 1) * 7168 + ((k) & 1) * 4096 + 2048)
        bf16x8 war[32];
#define WARQ(q, off) do { _Pragma("unroll") for (int k = 0; k < 8; ++k) war[8 * (q) + k] = WFR(wsa, wsk, off, k); } while (0)
        const unsigned bB = WS_BR + BR_SZ;
        WAIT_BAR(0);
        STAMP(11);
        KVG(2); DWQ(woap, 0, L_SA);
        WAIT_BAR(0);
        STAMP(12);
        if (tid == 256) set_flag(ws, WS_FB + (cb * 16 + j) * 8, tag);
        KVG(3); DWQ(woap, 1, L_SB); WARQ(0, 0);
        f32x16 cbv, pbv;
        {   const unsigned long long M = (0x5555ull << (r32 + 18)) >> (4 * hi);
            int nlo = tile > 0 ? ~(unsigned)M : ~0u, nhi = ~(unsigned)(M >> 32);
#pragma unroll
            for (int r = 0; r < 16; ++r) { const int cc = (r & 3) + 8 * (r >> 2);
                int e0 = nhi << (31 - cc); PIN(e0); cbv[r] = __uint_as_float((unsigned)(e0 >> 8) & 0xFF800000u);
                if (r < 8) pbv[r] = 0.f; else { int e1 = nlo << (31 - cc); PIN(e1); pbv[r] = __uint_as_float((unsigned)(e1 >> 8) & 0xFF800000u); } }
            PIN(cbv); PIN(pbv); }
        const unsigned fbo = WS_FB + (((16 * (lane % 3) + h) / 3) * 16 + ((lane & 4) && j > 0 ? j - 1 : j)) * 8;
        WAIT_BAR(0);
        STAMP(13);
        KVG(4); DWQ(woap, 2, L_SA); WARQ(1, L_SB - L_SA);
        WAIT_BAR(0);
        STAMP(14);
        asm volatile("global_load_dwordx2 %0, %1, %2 sc1" : "=v"(pa) : "v"(fbo), "s"(ws) : "memory");
        KVG(5); WARQ(2, 0);
        asm volatile("s_waitcnt vmcnt(8)" : "+v"(pa) :: "memory");
        if (__builtin_expect(!__all(pa == tag), 0)) wait_flags_d(ws, fbo, tag);
        {   const char* qo = ws + bB + BR_Q + (size_t)(h * 64 + tile) * 2048; const char* ko = ws + bB + BR_KV + (size_t)(h * 64 + tile) * 4096;
            const unsigned lq2 = lds0 + t * 2048, lk2 = lds0 + t * 4096;
            GLDSC(vl, qo, lq2, L_SB + 20480, 0); GLDSC(vl, qo, lq2, L_SB + 20480, 1024);
            GLDSC(vl, ko, lk2, L_SB + 4096, 0); GLDSC(vl, ko, lk2, L_SB + 4096, 1024); GLDSC(vl, ko, lk2, L_SB + 4096, 2048); GLDSC(vl, ko, lk2, L_SB + 4096, 3072);
            if (t == 0) { const char* po = tile > 0 ? ko - 4096 : ko; GLDSC(vl, po, lds0, L_SB, 0); GLDSC(vl, po, lds0, L_SB, 1024); GLDSC(vl, po, lds0, L_SB, 3072); } }
        WAIT_BAR(0);
        STAMP(15);
        {
            const unsigned bo = lds0 + L_SB + t * 4096 + vl, bq = lds0 + L_SB + 20480 + t * 2048 + vl;
            const bf16x8 bq0 = ldsr128(bq), bq1 = ldsr128(bq + 1024), bk0 = ldsr128(bo + 4096), bk1 = ldsr128(bo + 5120), bv0 = ldsr128(bo + 6144), bv1 = ldsr128(bo + 7168);
            const bf16x8 bp0 = ldsr128(bo), bp1 = ldsr128(bo + 1024), bpv = ldsr128(bo + 3072);
            KVG(6); DWQ(woap, 3, L_SA);
            __builtin_amdgcn_s_setprio(1);
            f32x16 sc = MFMA(bk0, bq0, cbv); sc = MFMA(bk1, bq1, sc);
            f32x16 sp = MFMA(bp0, bq0, pbv); sp = MFMA(bp1, bq1, sp);
            float m = max16(sc);
#pragma unroll
            for (int r = 8; r < 16; r += 2) m = __builtin_fmaxf(__builtin_fmaxf(m, sp[r]), sp[r + 1]);
            m = swap_max(m);
#pragma unroll
            for (int r = 0; r < 16; ++r) sc[r] = ex2(sc[r] - m);
#pragma unroll
            for (int r = 8; r < 16; ++r) sp[r] = ex2(sp[r] - m);
            u32x4 c0, c1, p0, p1; pack16(sc, c0, c1); pack16(sp, p0, p1);
            f32x16 ob = MFMA(bv0, asbf(c0), (f32x16){}); ob = MFMA(bv1, asbf(c1), ob); ob = MFMA(bpv, asbf(p1), ob);
            const float rl = __builtin_amdgcn_rcpf(from_hi(ob[12]));
#pragma unroll
            for (int i = 0; i < 16; ++i) ob[i] *= rl;
            u32x4 e0, e1; pack16(ob, e0, e1); fix_pad(e1, 0u, mhi);
            st16c(rs, e0, vl, bB + BR_O + (tile * 16 + h) * 2048); st16c(rs, e1, vl + 1024, bB + BR_O + (tile * 16 + h) * 2048);
            __builtin_amdgcn_s_setprio(0);
        }
        WAIT_BAR(2);
        STAMP(16);
        KVG(7); DWQ(wobp, 0, L_SB); WARQ(3, 0);
        asm volatile("s_waitcnt vmcnt(15)" ::: "memory");
        if (lane == 0) set_flag(ws, WS_FOB + (tile * 16 + h) * 8, tag);
        const unsigned foo = WS_FOB + (tt * 16 + (lane & 15)) * 8;
        WAIT_BAR(1);
        STAMP(17);
        DWQ(wobp, 1, L_SA);
        WAIT_BAR(0);
        STAMP(18);
        asm volatile("global_load_dwordx2 %0, %1, %2 sc1" : "=v"(pa) : "v"(foo), "s"(ws) : "memory");
        DWQ0(wobp, 2);
        asm volatile("s_waitcnt vmcnt(7)" : "+v"(pa) :: "memory");
        if (__builtin_expect(!__all(pa == tag), 0)) wait_flags_d(ws, foo, tag);
        {   const char* ofb = ws + bB + BR_O + (size_t)tt * 32768 + c * 8192; KV4(ofb, L_OF); KV4(ofb + 4096, L_OF + 4096); }
        unsigned oad = lds0 + L_OF + vl; PIN(oad);
        f32x16 accB = {};
        bf16x8 wf[4], of[4];
#define OPB(B, BK, off, kbase) do { \
        _Pragma("unroll") for (int i = 0; i < 4; ++i) { wf[i] = WFR(B, BK, off, i); of[i] = ldsr128(oad + ((kbase) + i) * 1024); } \
        _Pragma("unroll") for (int i = 0; i < 8; ++i) { SBAR(); accB = MFMA(wf[i & 3], of[i & 3], accB); SBAR(); \
            if (i < 4) { wf[i] = WFR(B, BK, off, i + 4); of[i] = ldsr128(oad + ((kbase) + i + 4) * 1024); } } SBAR(); } while (0)
        BAR_ONLY();
        STAMP(30);
        WAIT_BAR(0);
        STAMP(23);
        DWQ(wobp, 3, L_OA);
        OPB(wsa, wsk, L_SB - L_SA, 0); OPB(wsa, wsk, 0, 8); OPB(wz, wzk, 0, 16);
        WAIT_BAR(0);
        OPB(wz, wzk, L_OA, 24);
        const float gate = __builtin_amdgcn_rcpf(1.0f + ex2(-1.4426950408889634f * glog)), g1 = 1.0f - gate;
#pragma unroll
        for (int i = 0; i < 16; ++i) accB[i] *= g1;
        STAMP(24);
        wait_flags(ws, WS_FOA + (tt * 16 + (lane & 15)) * 8, tag, pa, pb2);
        STAMP(25);
        {
            const char* ofa = ws + WS_BR + BR_O + (size_t)tt * 32768 + c * 4096; const unsigned lp = lds0 + c * 4096;
            GLDSC(vl, ofa, lp, 0, 0); GLDSC(vl, ofa, lp, 0, 1024); GLDSC(vl, ofa, lp, 0, 2048); GLDSC(vl, ofa, lp, 0, 3072);
            GLDSC(vl, ofa + 16384, lp, 16384, 0); GLDSC(vl, ofa + 16384, lp, 16384, 1024); GLDSC(vl, ofa + 16384, lp, 16384, 2048); GLDSC(vl, ofa + 16384, lp, 16384, 3072); }
        WAIT_BAR(4);
        KEEP2(pa, pb2);
        STAMP(26);
        f32x16 acc = {};
        {   unsigned ob_ = lds0 + vl; PIN(ob_);
            bf16x8 og[8];
#pragma unroll
            for (int i = 0; i < 8; ++i) og[i] = ldsr128(ob_ + i * 1024);
#pragma unroll
            for (int i = 0; i < 32; ++i) { if (i == 8) { WAIT_BAR(0); } SBAR(); acc = MFMA(war[i], og[i & 7], acc); SBAR(); if (i < 24) og[i & 7] = ldsr128(ob_ + (i + 8) * 1024); }
            SBAR();
        }
        if (bid == 0 && tid == 256) __hip_atomic_store((GAS u64*)(ws + WS_EPOCH), ep + 1, RLX_AGENT);
        if (cg < 3 || c >= 2) {
#pragma unroll
            for (int i = 0; i < 16; ++i) acc[i] = acc[i] * gate + accB[i];
            u32x4 s0_, s1_; pack16(acc, s0_, s1_);
            auto r0 = __builtin_amdgcn_permlane32_swap(s0_[0], s0_[2], false, false), r1 = __builtin_amdgcn_permlane32_swap(s0_[1], s0_[3], false, false);
            auto r2 = __builtin_amdgcn_permlane32_swap(s1_[0], s1_[2], false, false), r3 = __builtin_amdgcn_permlane32_swap(s1_[1], s1_[3], false, false);
            char* op = out + (size_t)(tt * 32 + r32) * 896 + (col0 + 32 * c) * 2 + hi * 16;
            *(GAS u32x4*)op = (u32x4){r0[0], r1[0], r0[1], r1[1]};
            *(GAS u32x4*)(op + 32) = (u32x4){r2[0], r3[0], r2[1], r3[1]};
        }
    }
    STAMPF(27);
    asm volatile(".Lfk_end:");
}

extern "C" void kernel_launch(void* const* d_in, const int* in_sizes, int n_in,
                              void* d_out, int out_size, void* d_ws, size_t ws_size,
                              hipStream_t stream) {
    (void)in_sizes; (void)n_in; (void)out_size; (void)ws_size;
    (void)hipFuncSetAttribute((const void*)fused_kernel, hipFuncAttributeMaxDynamicSharedMemorySize, LDS_BYTES);
    fused_kernel<<<256, 512, LDS_BYTES, stream>>>((const char*)d_in[0], (const char*)d_in[1], (const char*)d_in[2], (const char*)d_in[4],
                                                  (const char*)d_in[5], (const char*)d_in[6], (char*)d_ws, (char*)d_out);
}
```

```cpp
#include <hip/hip_runtime.h>
#include <hip/hip_bf16.h>
#include <stdint.h>

typedef short bf16x8 __attribute__((ext_vector_type(8)));
typedef short s16x4 __attribute__((ext_vector_type(4)));
typedef float f32x16 __attribute__((ext_vector_type(16)));
typedef float f32x4 __attribute__((ext_vector_type(4)));
typedef float f32x2 __attribute__((ext_vector_type(2)));
typedef unsigned u32x4 __attribute__((ext_vector_type(4)));
typedef unsigned u32x2 __attribute__((ext_vector_type(2)));
typedef __bf16 bf2_t __attribute__((ext_vector_type(2)));
typedef unsigned long long u64;
#define LAS __attribute__((address_space(3)))
#define GAS __attribute__((address_space(1)))
#define DI __device__ __forceinline__
#define MFMA(a, b, c) __builtin_amdgcn_mfma_f32_32x32x16_bf16((a), (b), (c), 0, 0, 0)
#define SBAR() __builtin_amdgcn_sched_barrier(0)
#define PIN(x) asm volatile("" : "+v"(x))
#define RLX_AGENT __ATOMIC_RELAXED, __HIP_MEMORY_SCOPE_AGENT

template <int N> struct IC { static constexpr int value = N; };
constexpr float QSCALE = 0.18898223650461362f * 1.4426950408889634f;

constexpr unsigned WS_EPOCH = 0;
constexpr unsigned WS_FA = 4096;
constexpr unsigned WS_FB = WS_FA + 2048;
constexpr unsigned WS_FOA = WS_FB + 2048;
constexpr unsigned WS_FOB = WS_FOA + 8192;
constexpr unsigned WS_BR = 65536;
constexpr unsigned BR_Q = 0, BR_KV = 2097152, BR_O = 6291456, BR_SZ = 8388608;
constexpr unsigned WS_TOTAL = WS_BR + 2 * BR_SZ;

constexpr unsigned L_XB = 0;
constexpr unsigned L_KW = 98304;
constexpr unsigned L_GT = 155648;
constexpr unsigned L_CNT = 159232;
constexpr unsigned L_GL = 159744;
constexpr unsigned L_Q = 139264;
constexpr unsigned L_RING = 0;
constexpr unsigned L_SA = 98304, L_SB = 126976;
constexpr unsigned L_OF = 65536;
constexpr unsigned L_OA = 32768;
constexpr unsigned L_SCR = 155648;
constexpr unsigned LDS_BYTES = 163840;

DI unsigned cvtpk(float lo, float hi) { f32x2 v = {lo, hi}; return __builtin_bit_cast(unsigned, __builtin_convertvector(v, bf2_t)); }
#define GLDS(voff, sbase, lds_s, LC, OFF) asm volatile("s_add_i32 m0, %2, %3\n\ts_nop 0\n\tglobal_load_lds_dwordx4 %0, %1 offset:%4" \
        :: "v"(voff), "s"(sbase), "s"(__builtin_amdgcn_readfirstlane(lds_s)), "n"((int)(LC)), "n"((int)(OFF)) : "memory", "scc")
#define GLDSC(voff, sbase, lds_s, LC, OFF) asm volatile("s_add_i32 m0, %2, %3\n\ts_nop 0\n\tglobal_load_lds_dwordx4 %0, %1 offset:%4 sc1" \
        :: "v"(voff), "s"(sbase), "s"(__builtin_amdgcn_readfirstlane(lds_s)), "n"((int)(LC)), "n"((int)(OFF)) : "memory", "scc")
#define GLDS_IF(sel, voff, sbase, lds_s, LC, OFF) asm volatile("s_cmp_lg_u32 %5, 0\n\ts_cbranch_scc1 1f\n\ts_add_i32 m0, %2, %3\n\ts_nop 0\n\tglobal_load_lds_dwordx4 %0, %1 offset:%4\n1:" \
        :: "v"(voff), "s"(sbase), "s"(__builtin_amdgcn_readfirstlane(lds_s)), "n"((int)(LC)), "n"((int)(OFF)), "s"(sel) : "memory", "scc")
#define GLDSC_IF(sel, voff, sbase, lds_s, LC, OFF) asm volatile("s_cmp_lg_u32 %5, 0\n\ts_cbranch_scc1 1f\n\ts_add_i32 m0, %2, %3\n\ts_nop 0\n\tglobal_load_lds_dwordx4 %0, %1 offset:%4 sc1\n1:" \
        :: "v"(voff), "s"(sbase), "s"(__builtin_amdgcn_readfirstlane(lds_s)), "n"((int)(LC)), "n"((int)(OFF)), "s"(sel) : "memory", "scc")
#define WAIT_BAR(N) asm volatile("s_waitcnt vmcnt(" #N ") lgkmcnt(0)\n\ts_barrier" ::: "memory")
#define BAR_ONLY() asm volatile("s_waitcnt lgkmcnt(0)\n\ts_barrier" ::: "memory")
DI bf16x8 ldsr128(unsigned a) { return *(const LAS bf16x8*)(uintptr_t)a; }
typedef short v4i16_t __attribute__((ext_vector_type(4)));
DI s16x4 ldstr(unsigned a) { return __builtin_bit_cast(s16x4, __builtin_amdgcn_ds_read_tr16_b64_v4i16((LAS v4i16_t*)(uintptr_t)a)); }
DI bf16x8 wfrag(unsigned a0, unsigned a1) { s16x4 lo = ldstr(a0), hi = ldstr(a1); return __builtin_shufflevector(lo, hi, 0, 1, 2, 3, 4, 5, 6, 7); }
DI bf16x8 asbf(u32x4 v) { return __builtin_bit_cast(bf16x8, v); }
DI float ex2(float v) { return __builtin_amdgcn_exp2f(v); }
DI float swap_max(float m) {
    auto rr = __builtin_amdgcn_permlane32_swap(__float_as_uint(m), __float_as_uint(m), false, false);
    return __builtin_fmaxf(__uint_as_float(rr[0]), __uint_as_float(rr[1]));
}
DI float from_hi(float v) {
    auto rr = __builtin_amdgcn_permlane32_swap(__float_as_uint(v), __float_as_uint(v), false, false);
    return __uint_as_float(rr[1]);
}
DI void pack16(const f32x16& a, u32x4& s0, u32x4& s1) {
    s0 = (u32x4){cvtpk(a[0], a[1]), cvtpk(a[2], a[3]), cvtpk(a[4], a[5]), cvtpk(a[6], a[7])};
    s1 = (u32x4){cvtpk(a[8], a[9]), cvtpk(a[10], a[11]), cvtpk(a[12], a[13]), cvtpk(a[14], a[15])};
}
DI float max16(const f32x16& s) {
    float m = __builtin_fmaxf(s[0], s[1]);
#pragma unroll
    for (int r = 2; r < 16; r += 2) m = __builtin_fmaxf(__builtin_fmaxf(m, s[r]), s[r + 1]);
    return m;
}
typedef __amdgpu_buffer_rsrc_t rsrc_t;
DI void st16c(rsrc_t r, u32x4 v, unsigned voff, unsigned soff) { __builtin_amdgcn_raw_buffer_store_b128(v, r, voff, soff, 16); }
DI bf16x8 ld16c(rsrc_t r, unsigned voff, unsigned soff) { return asbf(__builtin_amdgcn_raw_buffer_load_b128(r, voff, soff, 16)); }
DI void wait_flags(const char* ws, unsigned off, u64 tag, u64& a, u64& b) {
    asm volatile("s_waitcnt vmcnt(0)\n\tglobal_load_dwordx2 %0, %1, %2 sc1" : "=v"(a) : "v"(off), "s"(ws) : "memory");
#pragma unroll 1
    for (int it = 0; it < 30000; ++it) {
        asm volatile("s_sleep 2\n\tglobal_load_dwordx2 %0, %2, %3 sc1\n\ts_waitcnt vmcnt(1)" : "=&v"(b), "+v"(a) : "v"(off), "s"(ws) : "memory");
        if (__all(a == tag)) break;
        asm volatile("s_sleep 2\n\tglobal_load_dwordx2 %0, %2, %3 sc1\n\ts_waitcnt vmcnt(1)" : "=&v"(a), "+v"(b) : "v"(off), "s"(ws) : "memory");
        if (__all(b == tag)) break;
    }
}
#define KEEP2(a, b) asm volatile("" :: "v"(a), "v"(b))
DI void wait_flags_d(const char* ws, unsigned off, u64 tag) { u64 a, b; wait_flags(ws, off, tag, a, b); asm volatile("s_waitcnt vmcnt(0)" : "+v"(a), "+v"(b) :: "memory"); }
DI void set_flag(char* ws, unsigned off, u64 tag) { __hip_atomic_store((GAS u64*)(ws + off), tag, RLX_AGENT); }

#define STAMP(i) do {} while (0)
#define STAMPF(i) do {} while (0)
#define STARGS
#define STPASS

DI void fix_v(u32x4& s0, u32x4& s1, unsigned m28) {
#pragma unroll
    for (int i = 0; i < 4; ++i) { s0[i] = (s0[i] & ~m28) | (0x3F803F80u & m28); s1[i] = (s1[i] & ~m28) | (0x3F803F80u & m28); }
}
DI void fix_pad(u32x4& s1, unsigned pad, unsigned mhi) { s1[2] = (s1[2] & ~mhi) | (pad & mhi); s1[3] &= ~mhi; }
template <bool V>
DI void pack_unit(f32x16 a, int u, unsigned m28, unsigned mhi, u32x4& s0, u32x4& s1) {
    if (!V) {
        const bool isq = u < 16;
        const float sc = isq ? QSCALE : 1.0f;
#pragma unroll
        for (int i = 0; i < 16; ++i) a[i] *= sc;
        pack16(a, s0, s1);
        fix_pad(s1, isq ? 0u : 0x00003F80u, mhi);
    } else { pack16(a, s0, s1); fix_v(s0, s1, m28); }
}
#define MKTAG() do { u64 z = ((u64)__builtin_amdgcn_readfirstlane((unsigned)(ep >> 32)) << 32) | (unsigned)__builtin_amdgcn_readfirstlane((unsigned)ep); \
        z += 0x9E3779B97F4A7C15ull; z = (z ^ (z >> 30)) * 0xBF58476D1CE4E5B9ull; z = (z ^ (z >> 27)) * 0x94D049BB133111EBull; tag = z ^ (z >> 31); } while (0)
#define XB(i) (L_XB + (i) * 16384)
#define KW(i) (L_KW + (i) * 8192)
#define MM(VV, acc, wf, xfr) do { if (VV) acc = MFMA(xfr, wf, acc); else acc = MFMA(wf, xfr, acc); } while (0)
__device__ constexpr unsigned so_of(int c) { return c == 0 ? KW(0) : (c == 6 ? KW(2) : XB(c)); }
__device__ constexpr unsigned xo_of(int c) { return c == 6 ? XB(0) : XB(c); }

template <bool VK>
DI void stage1(bf16x8 (&xf)[28], const unsigned (&xa)[4], const unsigned (&xh)[4], unsigned ka0, unsigned ka1, rsrc_t rs, int h, int tile, int lane, unsigned m28, unsigned mhi,
               unsigned gv0, unsigned gv1, unsigned dmc, unsigned dmi, u64 ep, u64& tag, unsigned lds0 STARGS) {
    f32x16 acc0 = {}, acc1 = {};
    bf16x8 w0[2], w1[2];
#define XR(s) xf[s] = ldsr128(xo_of((s) >> 2) >= 65536 ? xh[(s) & 3] + (xo_of((s) >> 2) - 65536) : xa[(s) & 3] + xo_of((s) >> 2))
#define KF(base, s) wfrag((base) + (s) * 2048, (base) + (s) * 2048 + 512)
    XR(0); w0[0] = KF(ka0, 0); w1[0] = KF(ka1, 0); XR(1); w0[1] = KF(ka0, 1); w1[1] = KF(ka1, 1);
#pragma unroll
    for (int s = 0; s < 28; ++s) {
        if ((s & 3) == 2 && s < 26) { BAR_ONLY(); if (s == 10) STAMP(3); if (s == 22) STAMP(4); }
        if (s == 20) {
            asm volatile("s_waitcnt vmcnt(0)" : "+v"(gv0), "+v"(gv1), "+v"(ep) : "v"(dmc), "v"(dmi) : "memory");
            const int tid = threadIdx.x;
            { u32x2 e = {gv0, 0u}; *(LAS u32x2*)(uintptr_t)(lds0 + L_GT + tid * 8) = e; }
            if (tid < 192) { u32x2 e = {gv1, 0u}; *(LAS u32x2*)(uintptr_t)(lds0 + L_GT + 2048 + tid * 8) = e; }
            if (tid == 0) *(LAS unsigned*)(uintptr_t)(lds0 + L_CNT) = 0u;
            MKTAG();
        }
        SBAR();
        MM(VK, acc0, w0[s & 1], xf[s]); SBAR();
        if (s + 2 < 28) { XR(s + 2); w0[s & 1] = KF(ka0, s + 2); }
        SBAR();
        MM(VK, acc1, w1[s & 1], xf[s]); SBAR();
        if (s + 2 < 28) w1[s & 1] = KF(ka1, s + 2);
    }
    SBAR();
    u32x4 s0, s1;
    pack16(acc0, s0, s1); if (!VK) fix_pad(s1, 0x00003F80u, mhi); else fix_v(s0, s1, m28);
    const unsigned off = WS_BR + BR_KV + ((2 * h & 15) * 64 + tile) * 4096 + (VK ? 2048 : 0);
    st16c(rs, s0, lane * 16, off); st16c(rs, s1, lane * 16 + 1024, off);
    pack16(acc1, s0, s1); if (!VK) fix_pad(s1, 0x00003F80u, mhi); else fix_v(s0, s1, m28);
    st16c(rs, s0, lane * 16, off + 262144); st16c(rs, s1, lane * 16 + 1024, off + 262144);
}
template <bool V01, bool V2>
DI void stage2(const bf16x8 (&xf)[28], unsigned ql, unsigned qh, unsigned gl, bool spec, unsigned wlo, unsigned whi, char* ws, rsrc_t rs, unsigned lds0,
               int h, int cb, int j, int t, int lane, u64 tag, u32x4 (&bp)[6], unsigned m28, unsigned mhi STARGS) {
    f32x16 aq = {}, acc0 = {}, acc1 = {}, acc2 = {};
    bf16x8 wq[2], w0[2], w1[2], w2[2];
    const unsigned gstride = spec ? 32u : 256u;
    bool sent = false;
#define SADDR(lo, hi_, c) (so_of(c) >= 65536 ? (hi_) + (so_of(c) - 65536) : (lo) + so_of(c))
#define WF(s, u) wfrag(SADDR(wlo, whi, (s) >> 2) + ((s) & 3) * 3072 + (u) * 56, SADDR(wlo, whi, (s) >> 2) + ((s) & 3) * 3072 + 768 + (u) * 56)
#define QR(s) do { const unsigned a_ = spec ? gl + 128 * (s) : SADDR(ql, qh, (s) >> 2) + ((s) & 3) * 1024; wq[(s) & 1] = wfrag(a_, a_ + gstride); } while (0)
    QR(0); w0[0] = WF(0, 0); w1[0] = WF(0, 1); w2[0] = WF(0, 2); QR(1); w0[1] = WF(1, 0); w1[1] = WF(1, 1); w2[1] = WF(1, 2);
#pragma unroll
    for (int s = 0; s < 28; ++s) {
        if (s == 6 || s == 10 || s == 14 || s == 18) { BAR_ONLY(); if (s == 10) STAMP(6); if (s == 18) STAMP(7); }
        if (s >= 7 && s <= 15) {
            if (s == 15) asm volatile("s_waitcnt vmcnt(0)" ::: "memory");
            if (!sent && (s == 15 || (__builtin_amdgcn_s_getreg(7 | (3 << 11)) & 15) == 0)) {
                sent = true; asm volatile("" ::: "memory");
                if (lane == 0) { if (__hip_atomic_fetch_add((LAS unsigned*)(uintptr_t)(lds0 + L_CNT), 1u, __ATOMIC_RELAXED, __HIP_MEMORY_SCOPE_WORKGROUP) == 3u) set_flag(ws, WS_FA + (h * 16 + j) * 8, tag); }
                STAMP(8);
            }
        }
        SBAR();
        aq = MFMA(wq[s & 1], xf[s], aq); SBAR();
        if (s + 2 < 28) QR(s + 2);
        SBAR();
        MM(V01, acc0, w0[s & 1], xf[s]); SBAR();
        if (s + 2 < 28) w0[s & 1] = WF(s + 2, 0);
        SBAR();
        MM(V01, acc1, w1[s & 1], xf[s]); SBAR();
        if (s + 2 < 28) w1[s & 1] = WF(s + 2, 1);
        SBAR();
        MM(V2, acc2, w2[s & 1], xf[s]); SBAR();
        if (s + 2 < 28) w2[s & 1] = WF(s + 2, 2);
    }
    SBAR();
    {
        if (lane >> 5) *(LAS float*)(uintptr_t)(lds0 + L_GL + t * 128 + (lane & 31) * 4) = aq[12];
#pragma unroll
        for (int i = 0; i < 16; ++i) aq[i] *= QSCALE;
        u32x4 s0, s1; pack16(aq, s0, s1);
        fix_pad(s1, 0u, mhi);
        *(LAS u32x4*)(uintptr_t)(lds0 + L_Q + t * 2048 + lane * 16) = s0; *(LAS u32x4*)(uintptr_t)(lds0 + L_Q + t * 2048 + 1024 + lane * 16) = s1;
    }
    pack_unit<V01>(acc0, 3 * cb, m28, mhi, bp[0], bp[1]); pack_unit<V01>(acc1, 3 * cb + 1, m28, mhi, bp[2], bp[3]); pack_unit<V2>(acc2, 3 * cb + 2, m28, mhi, bp[4], bp[5]);
}

__global__ __launch_bounds__(512, 2) void fused_kernel(const char* __restrict__ x, const char* __restrict__ wa, const char* __restrict__ woa,
                                                       const char* __restrict__ wb, const char* __restrict__ wob, const char* __restrict__ gw,
                                                       char* ws, char* __restrict__ out) {
    extern __shared__ __attribute__((aligned(16))) char smem[];
    const unsigned lds0 = (unsigned)(uintptr_t)(LAS char*)smem;
    const int tid = threadIdx.x, lane = tid & 63, w = __builtin_amdgcn_readfirstlane(tid >> 6), t = w & 3, g = w >> 2;
    const int bid = blockIdx.x, xg = bid & 7, idx = bid >> 3;
    const int h = (xg >> 1) * 4 + (idx & 3), j = (xg & 1) * 8 + (idx >> 2);
    const int r32 = lane & 31, hi = lane >> 5;
    const rsrc_t rs = __builtin_amdgcn_make_buffer_rsrc(ws, 0, WS_TOTAL, 0x00020000);
    const unsigned vl = lane * 16;
    asm volatile("" :: "s"(x), "s"(wa), "s"(woa), "s"(wb), "s"(wob), "s"(gw), "s"(ws), "s"(out));
    u64 pc; unsigned len;
    asm volatile("s_getpc_b64 %0\n.Lfk_ref:\n\ts_mov_b32 %1, .Lfk_end-.Lfk_ref" : "=s"(pc), "=s"(len));
    STAMPF(0);
    const int cb = (int)((0xCFEDBA8967542103ull >> (4 * h)) & 15);
    const int tile = 4 * j + t;
    const int rot = (j + 2 * (h & 3)) % 7;
#define CK(p) ((p) + rot >= 7 ? (p) + rot - 7 : (p) + rot)
    const unsigned vxe = (lane >> 3) * 896 + (((lane & 7) ^ (lane >> 4)) << 4), lx = lds0 + t * 4096;
    const char* xb = x + (size_t)(j * 128 + t * 32) * 896;
    unsigned dmi;
    {
        const unsigned co = (unsigned)((idx * 8 + w) * 2 + (lane & 1)) * 128u;
        asm volatile("global_load_ubyte %0, %1, %2" : "=v"(dmi) : "v"(co < len ? co : 0u), "s"(pc) : "memory"); }
    {
        const char* p_ = xb + rot * 128;
        GLDS_IF(g, vxe, p_, lx, XB(0), 0); GLDS_IF(g, (vxe ^ 64u) + 7168, p_, lx, XB(0) + 1024, 0); GLDS_IF(g, vxe + 14336, p_, lx, XB(0) + 2048, 0); GLDS_IF(g, (vxe ^ 64u) + 21504, p_, lx, XB(0) + 3072, 0); }
    u64 ep, tag;
    unsigned m28 = r32 == 28 ? ~0u : 0u, mhi = hi ? ~0u : 0u; PIN(m28); PIN(mhi);
    if (g == 0) {
        unsigned gv0, gv1, dmc;
        {
            const int pl = lane & 15;
            const char* tp = pl < 9 ? ws + (size_t)pl * 2097152 + 32768 : (pl == 9 ? (const char*)out : (pl == 10 ? woa : (pl == 11 ? wob : wb)));
            asm volatile("global_load_ubyte %0, %1, off sc1" : "=v"(dmc) : "v"(tp) : "memory");
        }
        asm volatile("global_load_ushort %0, %1, %2" : "=v"(gv0) : "v"((CK(t) * 64 + lane) * 2), "s"(gw) : "memory");
        asm volatile("global_load_ushort %0, %1, %2" : "=v"(gv1) : "v"((CK(t < 3 ? 4 + t : 6) * 64 + lane) * 2), "s"(gw) : "memory");
        asm volatile("global_load_dwordx2 %0, %1, %2 sc1" : "=v"(ep) : "v"(0u), "s"(ws) : "memory");
        const int fr = (r32 >> 1) & 7;
        unsigned xa[4], xh[4];
#pragma unroll
        for (int ks = 0; ks < 4; ++ks) { xa[ks] = lds0 + (4 * t + (r32 >> 3)) * 1024 + (r32 & 7) * 128 + (((2 * ks + hi) ^ fr) << 4); xh[ks] = xa[ks] + 65536; PIN(xa[ks]); PIN(xh[ks]); }
        const int q4 = (lane & 15) >> 2, p4 = lane & 3, ch = (lane >> 4) & 1;
        const unsigned o0 = ch * 32 + p4 * 8, o1 = o0 + 56, sw = (q4 >> 1) << 6;
        unsigned ka0 = lds0 + L_KW + (8 * hi + q4) * 128 + (o0 ^ sw), ka1 = lds0 + L_KW + (8 * hi + q4) * 128 + (o1 ^ sw); PIN(ka0); PIN(ka1);
        unsigned ql = lds0 + (8 * hi + q4) * 64 + ch * 32 + p4 * 8 + (h & 1) * 8, qh = ql + 65536; PIN(ql); PIN(qh);
        unsigned wlo = lds0 + 4096 + (8 * hi + q4) * 192 + ch * 32 + p4 * 8 + (cb & 1) * 8, whi = wlo + 65536; PIN(wlo); PIN(whi);
        const bool spec = ch == 1 && p4 == 3;
        const unsigned gl = lds0 + L_GT + 64 * hi + 8 * q4;
        bf16x8 xf[28]; u32x4 bp[6];
        STAMP(1);
        WAIT_BAR(4); STAMP(2);
        if (h < 8) stage1<false>(xf, xa, xh, ka0, ka1, rs, h, tile, lane, m28, mhi, gv0, gv1, dmc, dmi, ep, tag, lds0 STPASS); else stage1<true>(xf, xa, xh, ka0, ka1, rs, h, tile, lane, m28, mhi, gv0, gv1, dmc, dmi, ep, tag, lds0 STPASS);
        BAR_ONLY(); STAMP(5);
        if (cb <= 9) stage2<false, false>(xf, ql, qh, gl, spec, wlo, whi, ws, rs, lds0, h, cb, j, t, lane, tag, bp, m28, mhi STPASS);
        else if (cb == 10) stage2<false, true>(xf, ql, qh, gl, spec, wlo, whi, ws, rs, lds0, h, cb, j, t, lane, tag, bp, m28, mhi STPASS);
        else stage2<true, true>(xf, ql, qh, gl, spec, wlo, whi, ws, rs, lds0, h, cb, j, t, lane, tag, bp, m28, mhi STPASS);
        STAMP(9);
        BAR_ONLY();
#pragma unroll
        for (int i = 0; i < 3; ++i) { const int u = 3 * cb + i;
            const unsigned off = WS_BR + BR_SZ + (u < 16 ? BR_Q + (u * 64 + tile) * 2048 : BR_KV + ((u & 15) * 64 + tile) * 4096 + (u >= 32 ? 2048 : 0));
            st16c(rs, bp[2 * i], vl, off); st16c(rs, bp[2 * i + 1], vl + 1024, off); }
        STAMP(10);
        const int qp = t & 1, kh = t >> 1;
        const unsigned qad = lds0 + L_Q + qp * 4096 + kh * 2048 + vl, qbd = qad ^ 2048u;
        const bf16x8 qa0 = ldsr128(qad), qb0 = ldsr128(qbd);
        u32x4 qa1 = __builtin_bit_cast(u32x4, ldsr128(qad + 1024)), qb1 = __builtin_bit_cast(u32x4, ldsr128(qbd + 1024));
        unsigned ac = lds0 + L_RING + kh * 4096 + vl, an = ac + 32768, af = ac + 65536;
        f32x16 oa = {}, ob = {}, sA, sB; u32x4 pA0 = {}, pA1 = {}, pB0 = {}, pB1 = {};
        bf16x8 k0 = ldsr128(ac), k1 = ldsr128(ac + 1024), v0 = {}, v1 = {}, kn0, kn1;
        float mra, mrb;
        {
            sA = MFMA(k0, qa0, (f32x16){}); sB = MFMA(k0, qb0, (f32x16){});
            f32x16 ta = MFMA(k1, asbf(qa1), sA); sB = MFMA(k1, asbf(qb1), sB);
            const unsigned na = cvtpk(-swap_max(max16(ta)), 0.f), nb = cvtpk(-swap_max(max16(sB)), 0.f);
            mra = -__uint_as_float(na << 16); mrb = -__uint_as_float(nb << 16);
            qa1[2] = (qa1[2] & ~mhi) | (na & mhi); qb1[2] = (qb1[2] & ~mhi) | (nb & mhi);
            sA = MFMA(k1, asbf(qa1), sA);
        }
#define GRP(S, b, SP, bp, PP, jp) do { PP[jp] = cvtpk(SP[bp], SP[bp + 1]); PP[jp + 1] = cvtpk(SP[bp + 2], SP[bp + 3]); SBAR(); \
        S[b] = ex2(S[b]); S[b + 1] = ex2(S[b + 1]); S[b + 2] = ex2(S[b + 2]); S[b + 3] = ex2(S[b + 3]); PIN(PP); PIN(S); SBAR(); } while (0)
#define STEP2(KC0, KC1, KN0, KN1, VP1, VC1, ka, va) do { \
        KN0 = ldsr128(ka); ob = MFMA(v0, asbf(pB0), ob); SBAR(); GRP(sA, 0, sB, 12, pB1, 2); KN1 = ldsr128((ka) + 1024); \
        sB = MFMA(KC0, qb0, (f32x16){}); SBAR(); VC1 = ldsr128((va) + 3072); GRP(sA, 4, sA, 0, pA0, 0); v0 = ldsr128((va) + 2048); \
        sB = MFMA(KC1, asbf(qb1), sB); SBAR(); GRP(sA, 8, sA, 4, pA0, 2); \
        ob = MFMA(VP1, asbf(pB1), ob); SBAR(); GRP(sA, 12, sA, 8, pA1, 0); \
        oa = MFMA(v0, asbf(pA0), oa); SBAR(); GRP(sB, 0, sA, 12, pA1, 2); \
        sA = MFMA(KN0, qa0, (f32x16){}); SBAR(); GRP(sB, 4, sB, 0, pB0, 0); \
        sA = MFMA(KN1, asbf(qa1), sA); SBAR(); GRP(sB, 8, sB, 4, pB0, 2); \
        oa = MFMA(VC1, asbf(pA1), oa); SBAR(); GRP(sB, 12, sB, 8, pB1, 0); \
    } while (0)
        sB = (f32x16){};
        bf16x8 v1n;
#pragma unroll 1
        for (int gr = 0; gr < 8; ++gr) {
            STEP2(k0, k1, kn0, kn1, v1, v1n, ac + 8192, ac);
            STEP2(kn0, kn1, k0, k1, v1n, v1, ac + 16384, ac + 8192);
            if (gr == 0) asm volatile("s_barrier" ::: "memory"); else asm volatile("s_waitcnt vmcnt(0)\n\ts_barrier" ::: "memory");
            STAMP(11 + gr);
            SBAR();
            STEP2(k0, k1, kn0, kn1, v1, v1n, ac + 24576, ac + 16384);
            STEP2(kn0, kn1, k0, k1, v1n, v1, an, ac + 24576);
            const unsigned tmp = ac; ac = an; an = af; af = tmp;
        }
        pB1[2] = cvtpk(sB[12], sB[13]); pB1[3] = cvtpk(sB[14], sB[15]);
        ob = MFMA(v0, asbf(pB0), ob); ob = MFMA(v1, asbf(pB1), ob);
        STAMP(20);
#define SAFE_LIMIT 1.0e18f
        if (__builtin_expect(__any(hi && !(oa[12] < SAFE_LIMIT && ob[12] < SAFE_LIMIT)), 0)) {
            qa1[2] &= ~mhi; qb1[2] &= ~mhi;
            mra = -__builtin_inff(); mrb = mra; oa = (f32x16){}; ob = (f32x16){};
            const unsigned kb = WS_BR + BR_KV + h * 262144 + kh * 4096;
#pragma unroll 1
            for (int i = 0; i < 32; ++i) {
                const unsigned so = kb + i * 8192;
                const bf16x8 a0 = ld16c(rs, vl, so), a1 = ld16c(rs, vl + 1024, so), b0 = ld16c(rs, vl + 2048, so), b1 = ld16c(rs, vl + 3072, so);
#define ONLINE(Q0, Q1, O, M) do { f32x16 s_ = MFMA(a0, Q0, (f32x16){}); s_ = MFMA(a1, asbf(Q1), s_); \
                const float mn = __builtin_fmaxf(M, swap_max(max16(s_))), al = ex2(M - mn); M = mn; \
                _Pragma("unroll") for (int r = 0; r < 16; ++r) { s_[r] = ex2(s_[r] - mn); O[r] *= al; } \
                u32x4 e0, e1; pack16(s_, e0, e1); O = MFMA(b0, asbf(e0), O); O = MFMA(b1, asbf(e1), O); } while (0)
                ONLINE(qa0, qa1, oa, mra); ONLINE(qb0, qb1, ob, mrb);
            }
        }
        {   const unsigned cw_ = lds0 + L_OA + t * 4096 + vl;
#define PUTP(O, M) do { O[13] = __uint_as_float((__float_as_uint(O[13]) & ~mhi) | (__float_as_uint(M) & mhi)); \
            _Pragma("unroll") for (int i = 0; i < 4; ++i) *(LAS f32x4*)(uintptr_t)(cw_ + i * 1024) = (f32x4){O[4 * i], O[4 * i + 1], O[4 * i + 2], O[4 * i + 3]}; } while (0)
            PUTP(ob, mrb);
        }
        STAMP(29);
        BAR_ONLY();
        STAMP(30);
        {   const unsigned cbs = lds0 + L_OA + (t ^ 2) * 4096 + vl;
            f32x16 o1;
#pragma unroll
            for (int i = 3; i >= 0; --i) { const f32x4 e = *(const LAS f32x4*)(uintptr_t)(cbs + i * 1024); o1[4 * i] = e[0]; o1[4 * i + 1] = e[1]; o1[4 * i + 2] = e[2]; o1[4 * i + 3] = e[3]; }
            const float m1 = from_hi(o1[13]);
            const float mm = __builtin_fmaxf(mra, m1), a0 = ex2(mra - mm), a1 = ex2(m1 - mm);
            const float rl = __builtin_amdgcn_rcpf(from_hi(oa[12] * a0 + o1[12] * a1)), f0 = a0 * rl, f1 = a1 * rl;
            f32x16 v;
#pragma unroll
            for (int i = 0; i < 16; ++i) v[i] = oa[i] * f0 + o1[i] * f1;
            u32x4 e0, e1; pack16(v, e0, e1);
            fix_pad(e1, 0u, mhi);
            const int ot = 4 * j + 2 * qp + kh;
            const unsigned so = WS_BR + BR_O + (ot * 16 + h) * 2048;
            st16c(rs, e0, vl, so); st16c(rs, e1, vl + 1024, so);
            STAMP(21);
            asm volatile("s_waitcnt lgkmcnt(0)\n\ts_barrier\n\ts_waitcnt vmcnt(0)" ::: "memory");
            if (lane == 0) set_flag(ws, WS_FOA + (ot * 16 + h) * 8, tag);
            STAMP(22);
        }
    } else {
        const unsigned vx[4] = {vxe, (vxe ^ 64u) + 7168, vxe + 14336, (vxe ^ 64u) + 21504};
        const unsigned kc = (lane & 7) ^ (((lane >> 4) & 1) << 2);
        const unsigned vk0 = (lane >> 3) * 2688 + (kc > 6 ? 6 : kc) * 16, vk1 = vk0 + 8 * 2688;
        const char* wkb = wa + 896 + 112 * h + (size_t)(16 * t) * 2688;
        const unsigned lk = lds0 + t * 2048;
#define DMA_K(ck) do { const char* p_ = wkb + (size_t)CK(ck) * 64 * 2688; GLDS(vk0, p_, lk, KW(ck), 0); GLDS(vk1, p_, lk, KW(ck) + 1024, 0); } while (0)
#define DMA_X(cx) do { const char* p_ = xb + CK(cx) * 128; GLDS(vx[0], p_, lx, xo_of(cx), 0); GLDS(vx[1], p_, lx, xo_of(cx) + 1024, 0); \
        GLDS(vx[2], p_, lx, xo_of(cx) + 2048, 0); GLDS(vx[3], p_, lx, xo_of(cx) + 3072, 0); } while (0)
        asm volatile("global_load_dwordx2 %0, %1, %2 sc1" : "=v"(ep) : "v"(0u), "s"(ws) : "memory");
        DMA_K(0); DMA_X(1); DMA_K(1);
        unsigned vw[3];
#pragma unroll
        for (int n = 0; n < 3; ++n) { const unsigned L = 64 * n + lane, row = (L * 43691u) >> 19, cc = L - row * 12; vw[n] = row * 2688 + (cc > 10 ? 10 : cc) * 16; }
        const unsigned vq = (lane >> 2) * 2688 + (lane & 3) * 16;
        const char* wqb = wa + (56 * h - 8 * (h & 1)) + (size_t)(16 * t) * 2688;
        const char* wbb = wb + (168 * cb - 8 * (cb & 1)) + (size_t)(16 * t) * 2688;
        const unsigned lq = lds0 + t * 1024, lw = lds0 + 4096 + t * 3072;
#define DMA_S(ck) do { GLDS(vq, wqb + (size_t)CK(ck) * 64 * 2688, lq, so_of(ck), 0); const char* p_ = wbb + (size_t)CK(ck) * 64 * 2688; \
        GLDS(vw[0], p_, lw, so_of(ck), 0); GLDS(vw[1], p_, lw, so_of(ck) + 1024, 0); GLDS(vw[2], p_, lw, so_of(ck) + 2048, 0); } while (0)
        STAMP(1);
        WAIT_BAR(6); asm volatile("" :: "v"(dmi)); STAMP(2); DMA_X(2); DMA_K(2);
        WAIT_BAR(6); DMA_X(3); DMA_K(3);
        WAIT_BAR(6); DMA_X(4); DMA_K(4);
        WAIT_BAR(6); STAMP(3); DMA_X(5); DMA_K(5); DMA_S(0);
        WAIT_BAR(10); DMA_X(6); DMA_K(6); DMA_S(1);
        WAIT_BAR(14);
        WAIT_BAR(4); STAMP(4); DMA_S(2);
        asm volatile("" : "+v"(ep)); MKTAG();
        const unsigned fo = WS_FA + ((((lane >> 4) & 1) * 8 + (h >> 1)) * 16 + (lane & 15)) * 8;
        WAIT_BAR(4); STAMP(5); DMA_S(3);
        WAIT_BAR(4); DMA_S(4); DMA_S(5);
        WAIT_BAR(8); STAMP(6); DMA_S(6);
        unsigned dm0;
        {
            const int li = (t & 1) * 56 + (lane < 56 ? lane : 55), c0 = (h & 3) < 3 ? (h & 3) * 256 : 640;
            asm volatile("global_load_ubyte %0, %1, %2" : "=v"(dm0) : "v"(((j & 7) * 56 + (li >> 1)) * 896 + (li & 1) * 128 + c0), "s"((t & 2) ? wob : woa) : "memory"); }
        f32x16 cbv, pbv;
        {   const unsigned long long M = (0x5555ull << (r32 + 18)) >> (4 * hi);
            int nlo = tile > 0 ? ~(unsigned)M : ~0u, nhi = ~(unsigned)(M >> 32);
#pragma unroll
            for (int r = 0; r < 16; ++r) { const int cc = (r & 3) + 8 * (r >> 2);
                int e0 = nhi << (31 - cc); PIN(e0); cbv[r] = __uint_as_float((unsigned)(e0 >> 8) & 0xFF800000u);
                if (r < 8) pbv[r] = 0.f; else { int e1 = nlo << (31 - cc); PIN(e1); pbv[r] = __uint_as_float((unsigned)(e1 >> 8) & 0xFF800000u); } }
            PIN(cbv); PIN(pbv); }
        WAIT_BAR(9);
        WAIT_BAR(0);
        asm volatile("" :: "v"(dm0));
        STAMP(7);
        u64 pa, pb2; wait_flags(ws, fo, tag, pa, pb2);
        STAMP(8);
        const char* kv0 = ws + WS_BR + BR_KV + (size_t)h * 262144 + t * 8192; const unsigned lr = lds0 + t * 8192;
#define KV4(p, LC) do { GLDSC(vl, p, lr, LC, 0); GLDSC(vl, p, lr, LC, 1024); GLDSC(vl, p, lr, LC, 2048); GLDSC(vl, p, lr, LC, 3072); } while (0)
        KV4(kv0, L_RING); KV4(kv0 + 4096, L_RING + 4096); KV4(kv0 + 32768, L_RING + 32768); KV4(kv0 + 36864, L_RING + 36864);
        STAMP(9);
        WAIT_BAR(8);
        KEEP2(pa, pb2);
        STAMP(10);
        const int tt = 4 * j + (h >> 2), cg = h & 3, c = t;
        float glog = *(const LAS float*)(uintptr_t)(lds0 + L_GL + (h >> 2) * 128 + r32 * 4);
        const int col0 = cg < 3 ? cg * 128 : 320;
        const unsigned vwo = (lane >> 4) * 896 + (((lane & 15) ^ ((lane >> 4) << 2)) << 4), lc7 = lds0 + c * 7168;
        const char* wobp = wob + col0 * 2 + (size_t)(c * 7) * 3584; const char* woap = woa + col0 * 2 + (size_t)(c * 7) * 3584;
#define DWP(base, p, LC) do { if ((p) & 1) GLDS(vwo, (base) + (size_t)((p) - 1) * 3584, lc7, (LC) + 4096 + (p) * 1024 - 3584 - 4096, 3584); else GLDS(vwo, (base) + (size_t)(p) * 3584, lc7, (LC) + (p) * 1024, 0); } while (0)
#define DWQ(base, q, LC) do { const char* b_ = (base) + (size_t)(q) * 28 * 3584; DWP(b_, 0, LC); DWP(b_, 1, LC); DWP(b_, 2, LC); DWP(b_, 3, LC); DWP(b_, 4, LC); DWP(b_, 5, LC); DWP(b_, 6, LC); } while (0)
#define DWQ0(base, q) do { const char* b_ = (base) + (size_t)(q) * 28 * 3584; GLDS(vwo, b_, lc7, 0, 0); GLDS(vwo, b_ + 3584, lc7, 1024, 0); GLDS(vwo, b_ + 2 * 3584, lc7, 2048, 0); \
        GLDS(vwo, b_ + 3 * 3584, lc7, 3072, 0); DWP(b_, 4, 0); DWP(b_, 5, 0); DWP(b_, 6, 0); } while (0)
#define KVG(gr) do { const char* p_ = kv0 + (size_t)(gr) * 32768; KV4(p_, L_RING + ((gr) % 3) * 32768); KV4(p_ + 4096, L_RING + ((gr) % 3) * 32768 + 4096); } while (0)
        const int q4 = (lane & 15) >> 2, p4 = lane & 3, ch = (lane >> 4) & 1;
        const unsigned wl = lds0 + (4 * hi + q4) * 256 + (((c ^ q4) << 6) + ch * 32 + p4 * 8);
        unsigned wsa = wl + L_SA, wsk = wsa - (1024u & mhi); PIN(wsa); PIN(wsk);
        unsigned wz = wl, wzk = wl - (1024u & mhi); PIN(wz); PIN(wzk);
#define WFR(B, BK, off, k) wfrag((B) + (off) + ((k) >> 1) * 7168 + ((k) & 1) * 4096, ((k) == 7 ? (BK) : (B)) + (off) + ((k) >> 1) * 7168 + ((k) & 1) * 4096 + 2048)
        bf16x8 war[32];
#define WARQ(q, off) do { _Pragma("unroll") for (int k = 0; k < 8; ++k) war[8 * (q) + k] = WFR(wsa, wsk, off, k); } while (0)
        const unsigned bB = WS_BR + BR_SZ;
        WAIT_BAR(0);
        STAMP(11);
        KVG(2); DWQ(woap, 0, L_SA);
        WAIT_BAR(0);
        STAMP(12);
        if (tid == 256) set_flag(ws, WS_FB + (cb * 16 + j) * 8, tag);
        KVG(3); DWQ(woap, 1, L_SB); WARQ(0, 0);
        const unsigned fbo = WS_FB + (((16 * (lane % 3) + h) / 3) * 16 + ((lane & 4) && j > 0 ? j - 1 : j)) * 8;
        WAIT_BAR(0);
        STAMP(13);
        KVG(4); DWQ(woap, 2, L_SA); WARQ(1, L_SB - L_SA);
        WAIT_BAR(0);
        STAMP(14);
        asm volatile("global_load_dwordx2 %0, %1, %2 sc1" : "=v"(pa) : "v"(fbo), "s"(ws) : "memory");
        KVG(5); WARQ(2, 0);
        asm volatile("s_waitcnt vmcnt(8)" : "+v"(pa) :: "memory");
        if (__builtin_expect(!__all(pa == tag), 0)) wait_flags_d(ws, fbo, tag);
        {   const char* qo = ws + bB + BR_Q + (size_t)(h * 64 + tile) * 2048; const char* ko = ws + bB + BR_KV + (size_t)(h * 64 + tile) * 4096;
            const unsigned lq2 = lds0 + t * 2048, lk2 = lds0 + t * 4096;
            GLDSC(vl, qo, lq2, L_SB + 20480, 0); GLDSC(vl, qo, lq2, L_SB + 20480, 1024);
            GLDSC(vl, ko, lk2, L_SB + 4096, 0); GLDSC(vl, ko, lk2, L_SB + 4096, 1024); GLDSC(vl, ko, lk2, L_SB + 4096, 2048); GLDSC(vl, ko, lk2, L_SB + 4096, 3072);
            if (t == 0) { const char* po = tile > 0 ? ko - 4096 : ko; GLDSC(vl, po, lds0, L_SB, 0); GLDSC(vl, po, lds0, L_SB, 1024); GLDSC(vl, po, lds0, L_SB, 3072); } }
        WAIT_BAR(0);
        STAMP(15);
        {
            const unsigned bo = lds0 + L_SB + t * 4096 + vl, bq = lds0 + L_SB + 20480 + t * 2048 + vl;
            const bf16x8 bq0 = ldsr128(bq), bq1 = ldsr128(bq + 1024), bk0 = ldsr128(bo + 4096), bk1 = ldsr128(bo + 5120), bv0 = ldsr128(bo + 6144), bv1 = ldsr128(bo + 7168);
            const bf16x8 bp0 = ldsr128(bo), bp1 = ldsr128(bo + 1024), bpv = ldsr128(bo + 3072);
            KVG(6); DWQ(woap, 3, L_SA);
            __builtin_amdgcn_s_setprio(1);
            f32x16 sc = MFMA(bk0, bq0, cbv); sc = MFMA(bk1, bq1, sc);
            f32x16 sp = MFMA(bp0, bq0, pbv); sp = MFMA(bp1, bq1, sp);
            float m = max16(sc);
#pragma unroll
            for (int r = 8; r < 16; r += 2) m = __builtin_fmaxf(__builtin_fmaxf(m, sp[r]), sp[r + 1]);
            m = swap_max(m);
#pragma unroll
            for (int r = 0; r < 16; ++r) sc[r] = ex2(sc[r] - m);
#pragma unroll
            for (int r = 8; r < 16; ++r) sp[r] = ex2(sp[r] - m);
            u32x4 c0, c1, p0, p1; pack16(sc, c0, c1); pack16(sp, p0, p1);
            f32x16 ob = MFMA(bv0, asbf(c0), (f32x16){}); ob = MFMA(bv1, asbf(c1), ob); ob = MFMA(bpv, asbf(p1), ob);
            const float rl = __builtin_amdgcn_rcpf(from_hi(ob[12]));
#pragma unroll
            for (int i = 0; i < 16; ++i) ob[i] *= rl;
            u32x4 e0, e1; pack16(ob, e0, e1); fix_pad(e1, 0u, mhi);
            st16c(rs, e0, vl, bB + BR_O + (tile * 16 + h) * 2048); st16c(rs, e1, vl + 1024, bB + BR_O + (tile * 16 + h) * 2048);
            __builtin_amdgcn_s_setprio(0);
        }
        WAIT_BAR(2);
        STAMP(16);
        KVG(7); DWQ(wobp, 0, L_SB); WARQ(3, 0);
        asm volatile("s_waitcnt vmcnt(15)" ::: "memory");
        if (lane == 0) set_flag(ws, WS_FOB + (tile * 16 + h) * 8, tag);
        const unsigned foo = WS_FOB + (tt * 16 + (lane & 15)) * 8;
        WAIT_BAR(1);
        STAMP(17);
        DWQ(wobp, 1, L_SA);
        WAIT_BAR(0);
        STAMP(18);
        asm volatile("global_load_dwordx2 %0, %1, %2 sc1" : "=v"(pa) : "v"(foo), "s"(ws) : "memory");
        DWQ0(wobp, 2);
        asm volatile("s_waitcnt vmcnt(7)" : "+v"(pa) :: "memory");
        if (__builtin_expect(!__all(pa == tag), 0)) wait_flags_d(ws, foo, tag);
        {   const char* ofb = ws + bB + BR_O + (size_t)tt * 32768 + c * 8192; KV4(ofb, L_OF); KV4(ofb + 4096, L_OF + 4096); }
        unsigned oad = lds0 + L_OF + vl; PIN(oad);
        f32x16 accB = {};
        bf16x8 wf[4], of[4];
#define OPB(B, BK, off, kbase) do { \
        _Pragma("unroll") for (int i = 0; i < 4; ++i) { wf[i] = WFR(B, BK, off, i); of[i] = ldsr128(oad + ((kbase) + i) * 1024); } \
        _Pragma("unroll") for (int i = 0; i < 8; ++i) { SBAR(); accB = MFMA(wf[i & 3], of[i & 3], accB); SBAR(); \
            if (i < 4) { wf[i] = WFR(B, BK, off, i + 4); of[i] = ldsr128(oad + ((kbase) + i + 4) * 1024); } } SBAR(); } while (0)
        BAR_ONLY();
        STAMP(30);
        WAIT_BAR(0);
        STAMP(23);
        DWQ(wobp, 3, L_OA);
        OPB(wsa, wsk, L_SB - L_SA, 0); OPB(wsa, wsk, 0, 8); OPB(wz, wzk, 0, 16);
        WAIT_BAR(0);
        OPB(wz, wzk, L_OA, 24);
        float gate = __builtin_amdgcn_rcpf(1.0f + ex2(-1.4426950408889634f * glog)); const float g1 = 1.0f - gate;
#pragma unroll
        for (int i = 0; i < 16; ++i) accB[i] *= g1;
        unsigned oo = (tt * 32 + r32) * 896 + (col0 + 32 * c) * 2 + hi * 16;
        PIN(accB); PIN(gate); PIN(oo);
        STAMP(24);
        wait_flags(ws, WS_FOA + (tt * 16 + (lane & 15)) * 8, tag, pa, pb2);
        STAMP(25);
        {
            const char* ofa = ws + WS_BR + BR_O + (size_t)tt * 32768 + c * 4096; const unsigned lp = lds0 + c * 4096;
            GLDSC(vl, ofa, lp, 0, 0); GLDSC(vl, ofa, lp, 0, 1024); GLDSC(vl, ofa, lp, 0, 2048); GLDSC(vl, ofa, lp, 0, 3072);
            GLDSC(vl, ofa + 16384, lp, 16384, 0); GLDSC(vl, ofa + 16384, lp, 16384, 1024); GLDSC(vl, ofa + 16384, lp, 16384, 2048); GLDSC(vl, ofa + 16384, lp, 16384, 3072); }
        WAIT_BAR(4);
        KEEP2(pa, pb2);
        STAMP(26);
        f32x16 acc = {};
        {   unsigned ob_ = lds0 + vl; PIN(ob_);
            bf16x8 og[8];
#pragma unroll
            for (int i = 0; i < 8; ++i) og[i] = ldsr128(ob_ + i * 1024);
#pragma unroll
            for (int i = 0; i < 32; ++i) { if (i == 8) { WAIT_BAR(0); } SBAR(); acc = MFMA(war[i], og[i & 7], acc); SBAR(); if (i < 24) og[i & 7] = ldsr128(ob_ + (i + 8) * 1024); }
            SBAR();
        }
        if (bid == 0 && tid == 256) __hip_atomic_store((GAS u64*)(ws + WS_EPOCH), ep + 1, RLX_AGENT);
        if (cg < 3 || c >= 2) {
#pragma unroll
            for (int i = 0; i < 16; ++i) acc[i] = acc[i] * gate + accB[i];
            u32x4 s0_, s1_; pack16(acc, s0_, s1_);
            auto r0 = __builtin_amdgcn_permlane32_swap(s0_[0], s0_[2], false, false), r1 = __builtin_amdgcn_permlane32_swap(s0_[1], s0_[3], false, false);
            auto r2 = __builtin_amdgcn_permlane32_swap(s1_[0], s1_[2], false, false), r3 = __builtin_amdgcn_permlane32_swap(s1_[1], s1_[3], false, false);
            char* op = out + oo;
            *(GAS u32x4*)op = (u32x4){r0[0], r1[0], r0[1], r1[1]};
            *(GAS u32x4*)(op + 32) = (u32x4){r2[0], r3[0], r2[1], r3[1]};
        }
    }
    STAMPF(27);
    asm volatile(".Lfk_end:");
}

extern "C" void kernel_launch(void* const* d_in, const int* in_sizes, int n_in,
                              void* d_out, int out_size, void* d_ws, size_t ws_size,
                              hipStream_t stream) {
    (void)in_sizes; (void)n_in; (void)out_size; (void)ws_size;
    (void)hipFuncSetAttribute((const void*)fused_kernel, hipFuncAttributeMaxDynamicSharedMemorySize, LDS_BYTES);
    fused_kernel<<<256, 512, LDS_BYTES, stream>>>((const char*)d_in[0], (const char*)d_in[1], (const char*)d_in[2], (const char*)d_in[4],
                                                  (const char*)d_in[5], (const char*)d_in[6], (char*)d_ws, (char*)d_out);
}
```

```cpp
#include <hip/hip_runtime.h>
#include <hip/hip_bf16.h>
#include <stdint.h>

typedef short bf16x8 __attribute__((ext_vector_type(8)));
typedef short s16x4 __attribute__((ext_vector_type(4)));
typedef float f32x16 __attribute__((ext_vector_type(16)));
typedef float f32x4 __attribute__((ext_vector_type(4)));
typedef float f32x2 __attribute__((ext_vector_type(2)));
typedef unsigned u32x4 __attribute__((ext_vector_type(4)));
typedef unsigned u32x2 __attribute__((ext_vector_type(2)));
typedef __bf16 bf2_t __attribute__((ext_vector_type(2)));
typedef unsigned long long u64;
#define LAS __attribute__((address_space(3)))
#define GAS __attribute__((address_space(1)))
#define DI __device__ __forceinline__
#define MFMA(a, b, c) __builtin_amdgcn_mfma_f32_32x32x16_bf16((a), (b), (c), 0, 0, 0)
#define SBAR() __builtin_amdgcn_sched_barrier(0)
#define PIN(x) asm volatile("" : "+v"(x))
#define RLX_AGENT __ATOMIC_RELAXED, __HIP_MEMORY_SCOPE_AGENT

constexpr float QSCALE = 0.18898223650461362f * 1.4426950408889634f;

constexpr unsigned WS_EPOCH = 0;
constexpr unsigned WS_FA = 4096;
constexpr unsigned WS_FB = WS_FA + 2048;
constexpr unsigned WS_FOA = WS_FB + 2048;
constexpr unsigned WS_FOB = WS_FOA + 8192;
constexpr unsigned WS_BR = 65536;
constexpr unsigned BR_Q = 0, BR_KV = 2097152, BR_O = 6291456, BR_SZ = 8388608;
constexpr unsigned WS_TOTAL = WS_BR + 2 * BR_SZ;

constexpr unsigned L_XB = 0;
constexpr unsigned L_KW = 98304;
constexpr unsigned L_GT = 155648;
constexpr unsigned L_CNT = 159232;
constexpr unsigned L_GL = 159744;
constexpr unsigned L_Q = 139264;
constexpr unsigned L_RING = 0;
constexpr unsigned L_SA = 98304, L_SB = 126976;
constexpr unsigned L_OF = 65536;
constexpr unsigned L_OA = 32768;
constexpr unsigned L_SCR = 155648;
constexpr unsigned LDS_BYTES = 163840;

DI unsigned cvtpk(float lo, float hi) { f32x2 v = {lo, hi}; return __builtin_bit_cast(unsigned, __builtin_convertvector(v, bf2_t)); }
#define GLDS(voff, sbase, lds_s, LC, OFF) asm volatile("s_add_i32 m0, %2, %3\n\ts_nop 0\n\tglobal_load_lds_dwordx4 %0, %1 offset:%4" \
        :: "v"(voff), "s"(sbase), "s"(__builtin_amdgcn_readfirstlane(lds_s)), "n"((int)(LC)), "n"((int)(OFF)) : "memory", "scc")
#define GLDSC(voff, sbase, lds_s, LC, OFF) asm volatile("s_add_i32 m0, %2, %3\n\ts_nop 0\n\tglobal_load_lds_dwordx4 %0, %1 offset:%4 sc1" \
        :: "v"(voff), "s"(sbase), "s"(__builtin_amdgcn_readfirstlane(lds_s)), "n"((int)(LC)), "n"((int)(OFF)) : "memory", "scc")
#define GLDS_IF(sel, voff, sbase, lds_s, LC, OFF) asm volatile("s_cmp_lg_u32 %5, 0\n\ts_cbranch_scc1 1f\n\ts_add_i32 m0, %2, %3\n\ts_nop 0\n\tglobal_load_lds_dwordx4 %0, %1 offset:%4\n1:" \
        :: "v"(voff), "s"(sbase), "s"(__builtin_amdgcn_readfirstlane(lds_s)), "n"((int)(LC)), "n"((int)(OFF)), "s"(sel) : "memory", "scc")
#define WAIT_BAR(N) asm volatile("s_waitcnt vmcnt(" #N ") lgkmcnt(0)\n\ts_barrier" ::: "memory")
#define BAR_ONLY() asm volatile("s_waitcnt lgkmcnt(0)\n\ts_barrier" ::: "memory")
DI bf16x8 ldsr128(unsigned a) { return *(const LAS bf16x8*)(uintptr_t)a; }
typedef short v4i16_t __attribute__((ext_vector_type(4)));
DI s16x4 ldstr(unsigned a) { return __builtin_bit_cast(s16x4, __builtin_amdgcn_ds_read_tr16_b64_v4i16((LAS v4i16_t*)(uintptr_t)a)); }
DI bf16x8 wfrag(unsigned a0, unsigned a1) { s16x4 lo = ldstr(a0), hi = ldstr(a1); return __builtin_shufflevector(lo, hi, 0, 1, 2, 3, 4, 5, 6, 7); }
DI bf16x8 asbf(u32x4 v) { return __builtin_bit_cast(bf16x8, v); }
DI float ex2(float v) { return __builtin_amdgcn_exp2f(v); }
DI float swap_max(float m) {
    auto rr = __builtin_amdgcn_permlane32_swap(__float_as_uint(m), __float_as_uint(m), false, false);
    return __builtin_fmaxf(__uint_as_float(rr[0]), __uint_as_float(rr[1]));
}
DI float from_hi(float v) {
    auto rr = __builtin_amdgcn_permlane32_swap(__float_as_uint(v), __float_as_uint(v), false, false);
    return __uint_as_float(rr[1]);
}
DI void pack16(const f32x16& a, u32x4& s0, u32x4& s1) {
    s0 = (u32x4){cvtpk(a[0], a[1]), cvtpk(a[2], a[3]), cvtpk(a[4], a[5]), cvtpk(a[6], a[7])};
    s1 = (u32x4){cvtpk(a[8], a[9]), cvtpk(a[10], a[11]), cvtpk(a[12], a[13]), cvtpk(a[14], a[15])};
}
DI float max16(const f32x16& s) {
    float m = __builtin_fmaxf(s[0], s[1]);
#pragma unroll
    for (int r = 2; r < 16; r += 2) m = __builtin_fmaxf(__builtin_fmaxf(m, s[r]), s[r + 1]);
    return m;
}
typedef __amdgpu_buffer_rsrc_t rsrc_t;
DI void st16c(rsrc_t r, u32x4 v, unsigned voff, unsigned soff) { __builtin_amdgcn_raw_buffer_store_b128(v, r, voff, soff, 16); }
DI bf16x8 ld16c(rsrc_t r, unsigned voff, unsigned soff) { return asbf(__builtin_amdgcn_raw_buffer_load_b128(r, voff, soff, 16)); }
DI void wait_flags_d(const char* ws, unsigned off, u64 tag) {
#pragma unroll 1
    for (int it = 0; it < 40000; ++it) {
        u64 a;
        asm volatile("global_load_dwordx2 %0, %1, %2 sc1\n\ts_waitcnt vmcnt(0)" : "=&v"(a) : "v"(off), "s"(ws) : "memory");
        if (__all(a == tag)) break;
        __builtin_amdgcn_s_sleep(4);
    }
}
DI void set_flag(char* ws, unsigned off, u64 tag) { __hip_atomic_store((GAS u64*)(ws + off), tag, RLX_AGENT); }


DI void fix_v(u32x4& s0, u32x4& s1, unsigned m28) {
    unsigned keep = (m28 & 1u) - 1u, ones = m28 & 0x3F803F80u; PIN(keep); PIN(ones);
#pragma unroll
    for (int i = 0; i < 4; ++i) { s0[i] = (s0[i] & keep) | ones; s1[i] = (s1[i] & keep) | ones; }
}
DI void lds_arrive(unsigned a, int lane) { if (lane == 0) __hip_atomic_fetch_add((LAS unsigned*)(uintptr_t)a, 1u, __ATOMIC_RELAXED, __HIP_MEMORY_SCOPE_WORKGROUP); }
DI void lds_wait(unsigned a, unsigned n) {
#pragma unroll 1
    for (int it = 0; it < 2000000; ++it) {
        if (__builtin_amdgcn_readfirstlane(__hip_atomic_load((LAS unsigned*)(uintptr_t)a, __ATOMIC_RELAXED, __HIP_MEMORY_SCOPE_WORKGROUP)) >= n) break;
        __builtin_amdgcn_s_sleep(1);
    }
    asm volatile("" ::: "memory");
}
DI void fix_pad(u32x4& s1, unsigned pad, unsigned mhi) { s1[2] = (s1[2] & ~mhi) | (pad & mhi); s1[3] &= ~mhi; }
template <bool V>
DI void pack_unit(f32x16 a, int u, unsigned m28, unsigned mhi, u32x4& s0, u32x4& s1) {
    if (!V) {
        const bool isq = u < 16;
        const float sc = isq ? QSCALE : 1.0f;
#pragma unroll
        for (int i = 0; i < 16; ++i) a[i] *= sc;
        pack16(a, s0, s1);
        fix_pad(s1, isq ? 0u : 0x00003F80u, mhi);
    } else { pack16(a, s0, s1); fix_v(s0, s1, m28); }
}
#define MKTAG() do { u64 z = ((u64)__builtin_amdgcn_readfirstlane((unsigned)(ep >> 32)) << 32) | (unsigned)__builtin_amdgcn_readfirstlane((unsigned)ep); \
        z += 0x9E3779B97F4A7C15ull; z = (z ^ (z >> 30)) * 0xBF58476D1CE4E5B9ull; z = (z ^ (z >> 27)) * 0x94D049BB133111EBull; tag = z ^ (z >> 31); } while (0)
#define XB(i) (L_XB + (i) * 16384)
#define KW(i) (L_KW + (i) * 8192)
#define MM(VV, acc, wf, xfr) do { if (VV) acc = MFMA(xfr, wf, acc); else acc = MFMA(wf, xfr, acc); } while (0)
__device__ constexpr unsigned so_of(int c) { return c == 0 ? KW(0) : (c == 6 ? KW(2) : XB(c)); }
__device__ constexpr unsigned xo_of(int c) { return c == 6 ? XB(0) : XB(c); }

template <bool VK>
DI void stage1(bf16x8 (&xf)[28], const unsigned (&xa)[4], const unsigned (&xh)[4], unsigned ka0, unsigned ka1, rsrc_t rs, int h, int tile, int lane, unsigned m28, unsigned mhi,
               unsigned gv0, unsigned gv1, unsigned dmc, unsigned dmi, u64 ep, u64& tag, unsigned lds0) {
    f32x16 acc0 = {}, acc1 = {};
    bf16x8 w0[2], w1[2];
#define XR(s) xf[s] = ldsr128(xo_of((s) >> 2) >= 65536 ? xh[(s) & 3] + (xo_of((s) >> 2) - 65536) : xa[(s) & 3] + xo_of((s) >> 2))
#define KF(base, s) wfrag((base) + (s) * 2048, (base) + (s) * 2048 + 512)
    XR(0); w0[0] = KF(ka0, 0); w1[0] = KF(ka1, 0); XR(1); w0[1] = KF(ka0, 1); w1[1] = KF(ka1, 1);
#pragma unroll
    for (int s = 0; s < 28; ++s) {
        if ((s & 3) == 2 && s < 26) { BAR_ONLY(); }
        if (s == 20) {
            asm volatile("s_waitcnt vmcnt(0)" ::: "memory"); SBAR();
            asm volatile("" : "+v"(gv0), "+v"(gv1), "+v"(ep) : "v"(dmc), "v"(dmi) : "memory");
            const int tid = threadIdx.x;
            { u32x2 e = {gv0, 0u}; *(LAS u32x2*)(uintptr_t)(lds0 + L_GT + tid * 8) = e; }
            if (tid < 192) { u32x2 e = {gv1, 0u}; *(LAS u32x2*)(uintptr_t)(lds0 + L_GT + 2048 + tid * 8) = e; }
            if (tid == 0) *(LAS u32x2*)(uintptr_t)(lds0 + L_CNT) = (u32x2){0u, 0u};
            MKTAG();
        }
        SBAR();
        MM(VK, acc0, w0[s & 1], xf[s]); SBAR();
        if (s + 2 < 28) { XR(s + 2); w0[s & 1] = KF(ka0, s + 2); }
        SBAR();
        MM(VK, acc1, w1[s & 1], xf[s]); SBAR();
        if (s + 2 < 28) w1[s & 1] = KF(ka1, s + 2);
    }
    SBAR();
    u32x4 s0, s1;
    pack16(acc0, s0, s1); if (!VK) fix_pad(s1, 0x00003F80u, mhi); else fix_v(s0, s1, m28);
    const unsigned off = WS_BR + BR_KV + ((2 * h & 15) * 64 + tile) * 4096 + (VK ? 2048 : 0);
    st16c(rs, s0, lane * 16, off); st16c(rs, s1, lane * 16 + 1024, off);
    pack16(acc1, s0, s1); if (!VK) fix_pad(s1, 0x00003F80u, mhi); else fix_v(s0, s1, m28);
    st16c(rs, s0, lane * 16, off + 262144); st16c(rs, s1, lane * 16 + 1024, off + 262144);
}
template <bool V01, bool V2>
DI void stage2(const bf16x8 (&xf)[28], unsigned ql, unsigned qh, unsigned gl, bool spec, unsigned wlo, unsigned whi, char* ws, rsrc_t rs, unsigned lds0,
               int h, int cb, int j, int t, int lane, u64 tag, u32x4 (&bp)[6], unsigned m28, unsigned mhi) {
    f32x16 aq = {}, acc0 = {}, acc1 = {}, acc2 = {};
    bf16x8 wq[2], w0[2], w1[2], w2[2];
    const unsigned gstride = spec ? 32u : 256u;
    bool sent = false;
#define SADDR(lo, hi_, c) (so_of(c) >= 65536 ? (hi_) + (so_of(c) - 65536) : (lo) + so_of(c))
#define WF(s, u) wfrag(SADDR(wlo, whi, (s) >> 2) + ((s) & 3) * 3072 + (u) * 56, SADDR(wlo, whi, (s) >> 2) + ((s) & 3) * 3072 + 768 + (u) * 56)
#define QR(s) do { const unsigned a_ = spec ? gl + 128 * (s) : SADDR(ql, qh, (s) >> 2) + ((s) & 3) * 1024; wq[(s) & 1] = wfrag(a_, a_ + gstride); } while (0)
    QR(0); w0[0] = WF(0, 0); w1[0] = WF(0, 1); w2[0] = WF(0, 2); QR(1); w0[1] = WF(1, 0); w1[1] = WF(1, 1); w2[1] = WF(1, 2);
#pragma unroll
    for (int s = 0; s < 28; ++s) {
        if (s == 6 || s == 10 || s == 14 || s == 18) { BAR_ONLY(); }
        if (s >= 7 && s <= 15) {
            if (s == 15) asm volatile("s_waitcnt vmcnt(0)" ::: "memory");
            if (!sent && (s == 15 || (__builtin_amdgcn_s_getreg(7 | (3 << 11)) & 15) == 0)) {
                sent = true; asm volatile("" ::: "memory");
                if (lane == 0) { if (__hip_atomic_fetch_add((LAS unsigned*)(uintptr_t)(lds0 + L_CNT), 1u, __ATOMIC_RELAXED, __HIP_MEMORY_SCOPE_WORKGROUP) == 3u) set_flag(ws, WS_FA + (h * 16 + j) * 8, tag); }
            }
        }
        SBAR();
        aq = MFMA(wq[s & 1], xf[s], aq); SBAR();
        if (s + 2 < 28) QR(s + 2);
        SBAR();
        MM(V01, acc0, w0[s & 1], xf[s]); SBAR();
        if (s + 2 < 28) w0[s & 1] = WF(s + 2, 0);
        SBAR();
        MM(V01, acc1, w1[s & 1], xf[s]); SBAR();
        if (s + 2 < 28) w1[s & 1] = WF(s + 2, 1);
        SBAR();
        MM(V2, acc2, w2[s & 1], xf[s]); SBAR();
        if (s + 2 < 28) w2[s & 1] = WF(s + 2, 2);
    }
    SBAR();
    {
        if (lane >> 5) *(LAS float*)(uintptr_t)(lds0 + L_GL + t * 128 + (lane & 31) * 4) = aq[12];
#pragma unroll
        for (int i = 0; i < 16; ++i) aq[i] *= QSCALE;
        u32x4 s0, s1; pack16(aq, s0, s1);
        fix_pad(s1, 0u, mhi);
        *(LAS u32x4*)(uintptr_t)(lds0 + L_Q + t * 2048 + lane * 16) = s0; *(LAS u32x4*)(uintptr_t)(lds0 + L_Q + t * 2048 + 1024 + lane * 16) = s1;
    }
    pack_unit<V01>(acc0, 3 * cb, m28, mhi, bp[0], bp[1]); pack_unit<V01>(acc1, 3 * cb + 1, m28, mhi, bp[2], bp[3]); pack_unit<V2>(acc2, 3 * cb + 2, m28, mhi, bp[4], bp[5]);
}

__global__ __launch_bounds__(512, 2) void fused_kernel(const char* __restrict__ x, const char* __restrict__ wa, const char* __restrict__ woa,
                                                       const char* __restrict__ wb, const char* __restrict__ wob, const char* __restrict__ gw,
                                                       char* ws, char* __restrict__ out) {
    extern __shared__ __attribute__((aligned(16))) char smem[];
    const unsigned lds0 = (unsigned)(uintptr_t)(LAS char*)smem;
    const int tid = threadIdx.x, lane = tid & 63, w = __builtin_amdgcn_readfirstlane(tid >> 6), t = w & 3, g = w >> 2;
    const int bid = blockIdx.x, xg = bid & 7, idx = bid >> 3;
    const int h = (xg >> 1) * 4 + (idx & 3), j = (xg & 1) * 8 + (idx >> 2);
    const int r32 = lane & 31, hi = lane >> 5;
    const rsrc_t rs = __builtin_amdgcn_make_buffer_rsrc(ws, 0, WS_TOTAL, 0x00020000);
    const unsigned vl = lane * 16;
    asm volatile("" :: "s"(x), "s"(wa), "s"(woa), "s"(wb), "s"(wob), "s"(gw), "s"(ws), "s"(out));
    u64 pc; unsigned len;
    asm volatile("s_getpc_b64 %0\n.Lfk_ref:\n\ts_mov_b32 %1, .Lfk_end-.Lfk_ref" : "=s"(pc), "=s"(len));
    const int cb = (int)((0xCFEDBA8967542103ull >> (4 * h)) & 15);
    const int tile = 4 * j + t;
    const int rot = (j + 2 * (h & 3)) % 7;
#define CK(p) ((p) + rot >= 7 ? (p) + rot - 7 : (p) + rot)
    const unsigned vxe = (lane >> 3) * 896 + (((lane & 7) ^ (lane >> 4)) << 4), lx = lds0 + t * 4096;
    const char* xb = x + (size_t)(j * 128 + t * 32) * 896;
    unsigned dmi;
    {
        const unsigned co = (unsigned)((idx * 8 + w) * 2 + (lane & 1)) * 128u;
        asm volatile("global_load_ubyte %0, %1, %2" : "=v"(dmi) : "v"(co < len ? co : 0u), "s"(pc) : "memory"); }
    {
        const char* p_ = xb + rot * 128;
        GLDS_IF(g, vxe, p_, lx, XB(0), 0); GLDS_IF(g, (vxe ^ 64u) + 7168, p_, lx, XB(0) + 1024, 0); GLDS_IF(g, vxe + 14336, p_, lx, XB(0) + 2048, 0); GLDS_IF(g, (vxe ^ 64u) + 21504, p_, lx, XB(0) + 3072, 0); }
    u64 ep, tag;
    unsigned m28 = r32 == 28 ? ~0u : (r32 > 28 ? 1u : 0u), mhi = hi ? ~0u : 0u; PIN(m28); PIN(mhi);
    if (g == 0) {
        unsigned gv0, gv1, dmc;
        {
            const int pl = lane & 15;
            const char* tp = pl < 9 ? ws + (size_t)pl * 2097152 + 32768 : (pl == 9 ? (const char*)out : (pl == 10 ? woa : (pl == 11 ? wob : wb)));
            asm volatile("global_load_ubyte %0, %1, off sc1" : "=v"(dmc) : "v"(tp) : "memory");
        }
        asm volatile("global_load_ushort %0, %1, %2" : "=v"(gv0) : "v"((CK(t) * 64 + lane) * 2), "s"(gw) : "memory");
        asm volatile("global_load_ushort %0, %1, %2" : "=v"(gv1) : "v"((CK(t < 3 ? 4 + t : 6) * 64 + lane) * 2), "s"(gw) : "memory");
        asm volatile("global_load_dwordx2 %0, %1, %2 sc1" : "=v"(ep) : "v"(0u), "s"(ws) : "memory");
        const int fr = (r32 >> 1) & 7;
        unsigned xa[4], xh[4];
#pragma unroll
        for (int ks = 0; ks < 4; ++ks) { xa[ks] = lds0 + (4 * t + (r32 >> 3)) * 1024 + (r32 & 7) * 128 + (((2 * ks + hi) ^ fr) << 4); xh[ks] = xa[ks] + 65536; PIN(xa[ks]); PIN(xh[ks]); }
        const int q4 = (lane & 15) >> 2, p4 = lane & 3, ch = (lane >> 4) & 1;
        const unsigned o0 = ch * 32 + p4 * 8, o1 = o0 + 56, sw = (q4 >> 1) << 6;
        unsigned ka0 = lds0 + L_KW + (8 * hi + q4) * 128 + (o0 ^ sw), ka1 = lds0 + L_KW + (8 * hi + q4) * 128 + (o1 ^ sw); PIN(ka0); PIN(ka1);
        unsigned ql = lds0 + (8 * hi + q4) * 64 + ch * 32 + p4 * 8 + (h & 1) * 8, qh = ql + 65536; PIN(ql); PIN(qh);
        unsigned wlo = lds0 + 4096 + (8 * hi + q4) * 192 + ch * 32 + p4 * 8 + (cb & 1) * 8, whi = wlo + 65536; PIN(wlo); PIN(whi);
        const bool spec = ch == 1 && p4 == 3;
        const unsigned gl = lds0 + L_GT + 64 * hi + 8 * q4;
        bf16x8 xf[28]; u32x4 bp[6];
        WAIT_BAR(4);
        if (h < 8) stage1<false>(xf, xa, xh, ka0, ka1, rs, h, tile, lane, m28, mhi, gv0, gv1, dmc, dmi, ep, tag, lds0); else stage1<true>(xf, xa, xh, ka0, ka1, rs, h, tile, lane, m28, mhi, gv0, gv1, dmc, dmi, ep, tag, lds0);
        BAR_ONLY();
        if (cb <= 9) stage2<false, false>(xf, ql, qh, gl, spec, wlo, whi, ws, rs, lds0, h, cb, j, t, lane, tag, bp, m28, mhi);
        else if (cb == 10) stage2<false, true>(xf, ql, qh, gl, spec, wlo, whi, ws, rs, lds0, h, cb, j, t, lane, tag, bp, m28, mhi);
        else stage2<true, true>(xf, ql, qh, gl, spec, wlo, whi, ws, rs, lds0, h, cb, j, t, lane, tag, bp, m28, mhi);
        BAR_ONLY();
#pragma unroll
        for (int i = 0; i < 3; ++i) { const int u = 3 * cb + i;
            const unsigned off = WS_BR + BR_SZ + (u < 16 ? BR_Q + (u * 64 + tile) * 2048 : BR_KV + ((u & 15) * 64 + tile) * 4096 + (u >= 32 ? 2048 : 0));
            st16c(rs, bp[2 * i], vl, off); st16c(rs, bp[2 * i + 1], vl + 1024, off); }
        const int qp = t & 1, kh = t >> 1;
        const unsigned qad = lds0 + L_Q + qp * 4096 + kh * 2048 + vl, qbd = qad ^ 2048u;
        const bf16x8 qa0 = ldsr128(qad), qb0 = ldsr128(qbd);
        u32x4 qa1 = __builtin_bit_cast(u32x4, ldsr128(qad + 1024)), qb1 = __builtin_bit_cast(u32x4, ldsr128(qbd + 1024));
        unsigned ac = lds0 + L_RING + kh * 4096 + vl, an = ac + 32768, af = ac + 65536;
        f32x16 oa = {}, ob = {}, sA, sB; u32x4 pA0 = {}, pA1 = {}, pB0 = {}, pB1 = {};
        bf16x8 k0 = ldsr128(ac), k1 = ldsr128(ac + 1024), v0 = {}, v1 = {}, kn0, kn1;
        float mra, mrb;
        {
            sA = MFMA(k0, qa0, (f32x16){}); sB = MFMA(k0, qb0, (f32x16){});
            f32x16 ta = MFMA(k1, asbf(qa1), sA); sB = MFMA(k1, asbf(qb1), sB);
            const unsigned na = cvtpk(-swap_max(max16(ta)), 0.f), nb = cvtpk(-swap_max(max16(sB)), 0.f);
            mra = -__uint_as_float(na << 16); mrb = -__uint_as_float(nb << 16);
            qa1[2] = (qa1[2] & ~mhi) | (na & mhi); qb1[2] = (qb1[2] & ~mhi) | (nb & mhi);
            sA = MFMA(k1, asbf(qa1), sA);
        }
#define GRP(S, b, SP, bp, PP, jp) do { PP[jp] = cvtpk(SP[bp], SP[bp + 1]); PP[jp + 1] = cvtpk(SP[bp + 2], SP[bp + 3]); SBAR(); \
        S[b] = ex2(S[b]); S[b + 1] = ex2(S[b + 1]); S[b + 2] = ex2(S[b + 2]); S[b + 3] = ex2(S[b + 3]); PIN(PP); PIN(S); SBAR(); } while (0)
#define STEP2(KC0, KC1, KN0, KN1, VP1, VC1, ka, va) do { \
        KN0 = ldsr128(ka); ob = MFMA(v0, asbf(pB0), ob); SBAR(); GRP(sA, 0, sB, 12, pB1, 2); KN1 = ldsr128((ka) + 1024); \
        sB = MFMA(KC0, qb0, (f32x16){}); SBAR(); VC1 = ldsr128((va) + 3072); GRP(sA, 4, sA, 0, pA0, 0); v0 = ldsr128((va) + 2048); \
        sB = MFMA(KC1, asbf(qb1), sB); SBAR(); GRP(sA, 8, sA, 4, pA0, 2); \
        ob = MFMA(VP1, asbf(pB1), ob); SBAR(); GRP(sA, 12, sA, 8, pA1, 0); \
        oa = MFMA(v0, asbf(pA0), oa); SBAR(); GRP(sB, 0, sA, 12, pA1, 2); \
        sA = MFMA(KN0, qa0, (f32x16){}); SBAR(); GRP(sB, 4, sB, 0, pB0, 0); \
        sA = MFMA(KN1, asbf(qa1), sA); SBAR(); GRP(sB, 8, sB, 4, pB0, 2); \
        oa = MFMA(VC1, asbf(pA1), oa); SBAR(); GRP(sB, 12, sB, 8, pB1, 0); \
    } while (0)
        sB = (f32x16){};
        bf16x8 v1n;
#pragma unroll 1
        for (int gr = 0; gr < 8; ++gr) {
            STEP2(k0, k1, kn0, kn1, v1, v1n, ac + 8192, ac);
            STEP2(kn0, kn1, k0, k1, v1n, v1, ac + 16384, ac + 8192);
            if (gr == 0) asm volatile("s_barrier" ::: "memory"); else asm volatile("s_waitcnt vmcnt(0)\n\ts_barrier" ::: "memory");
            SBAR();
            STEP2(k0, k1, kn0, kn1, v1, v1n, ac + 24576, ac + 16384);
            STEP2(kn0, kn1, k0, k1, v1n, v1, an, ac + 24576);
            const unsigned tmp = ac; ac = an; an = af; af = tmp;
        }
        pB1[2] = cvtpk(sB[12], sB[13]); pB1[3] = cvtpk(sB[14], sB[15]);
        ob = MFMA(v0, asbf(pB0), ob); ob = MFMA(v1, asbf(pB1), ob);
                if (__builtin_expect(__any(hi && !(oa[12] < 1.0e18f && ob[12] < 1.0e18f)), 0)) {
            qa1[2] &= ~mhi; qb1[2] &= ~mhi;
            mra = -__builtin_inff(); mrb = mra; oa = (f32x16){}; ob = (f32x16){};
            const unsigned kb = WS_BR + BR_KV + h * 262144 + kh * 4096;
#pragma unroll 1
            for (int i = 0; i < 32; ++i) {
                const unsigned so = kb + i * 8192;
                const bf16x8 a0 = ld16c(rs, vl, so), a1 = ld16c(rs, vl + 1024, so), b0 = ld16c(rs, vl + 2048, so), b1 = ld16c(rs, vl + 3072, so);
#define ONLINE(Q0, Q1, O, M) do { f32x16 s_ = MFMA(a0, Q0, (f32x16){}); s_ = MFMA(a1, asbf(Q1), s_); \
                const float mn = __builtin_fmaxf(M, swap_max(max16(s_))), al = ex2(M - mn); M = mn; \
                _Pragma("unroll") for (int r = 0; r < 16; ++r) { s_[r] = ex2(s_[r] - mn); O[r] *= al; } \
                u32x4 e0, e1; pack16(s_, e0, e1); O = MFMA(b0, asbf(e0), O); O = MFMA(b1, asbf(e1), O); } while (0)
                ONLINE(qa0, qa1, oa, mra); ONLINE(qb0, qb1, ob, mrb);
            }
        }
        {   const unsigned cw_ = lds0 + L_OA + t * 4096 + vl;
#define PUTP(O, M) do { O[13] = __uint_as_float((__float_as_uint(O[13]) & ~mhi) | (__float_as_uint(M) & mhi)); \
            _Pragma("unroll") for (int i = 0; i < 4; ++i) *(LAS f32x4*)(uintptr_t)(cw_ + i * 1024) = (f32x4){O[4 * i], O[4 * i + 1], O[4 * i + 2], O[4 * i + 3]}; } while (0)
            PUTP(ob, mrb);
        }
        BAR_ONLY();
        {   const unsigned cbs = lds0 + L_OA + (t ^ 2) * 4096 + vl;
            f32x16 o1;
#pragma unroll
            for (int i = 3; i >= 0; --i) { const f32x4 e = *(const LAS f32x4*)(uintptr_t)(cbs + i * 1024); o1[4 * i] = e[0]; o1[4 * i + 1] = e[1]; o1[4 * i + 2] = e[2]; o1[4 * i + 3] = e[3]; }
            const float m1 = from_hi(o1[13]);
            const float mm = __builtin_fmaxf(mra, m1), a0 = ex2(mra - mm), a1 = ex2(m1 - mm);
            const float rl = __builtin_amdgcn_rcpf(from_hi(oa[12] * a0 + o1[12] * a1)), f0 = a0 * rl, f1 = a1 * rl;
            f32x16 v;
#pragma unroll
            for (int i = 0; i < 16; ++i) v[i] = oa[i] * f0 + o1[i] * f1;
            u32x4 e0, e1; pack16(v, e0, e1);
            fix_pad(e1, 0u, mhi);
            const int ot = 4 * j + 2 * qp + kh;
            const unsigned so = WS_BR + BR_O + (ot * 16 + h) * 2048;
            st16c(rs, e0, vl, so); st16c(rs, e1, vl + 1024, so);
            asm volatile("s_waitcnt lgkmcnt(0)\n\ts_barrier\n\ts_waitcnt vmcnt(0)" ::: "memory");
            if (lane == 0) set_flag(ws, WS_FOA + (ot * 16 + h) * 8, tag);
        }
    } else {
        const unsigned vx[4] = {vxe, (vxe ^ 64u) + 7168, vxe + 14336, (vxe ^ 64u) + 21504};
        const unsigned kc = (lane & 7) ^ (((lane >> 4) & 1) << 2);
        const unsigned vk0 = (lane >> 3) * 2688 + (kc > 6 ? 6 : kc) * 16, vk1 = vk0 + 8 * 2688;
        const char* wkb = wa + 896 + 112 * h + (size_t)(16 * t) * 2688;
        const unsigned lk = lds0 + t * 2048;
#define DMA_K(ck) do { const char* p_ = wkb + (size_t)CK(ck) * 64 * 2688; GLDS(vk0, p_, lk, KW(ck), 0); GLDS(vk1, p_, lk, KW(ck) + 1024, 0); } while (0)
#define DMA_X(cx) do { const char* p_ = xb + CK(cx) * 128; GLDS(vx[0], p_, lx, xo_of(cx), 0); GLDS(vx[1], p_, lx, xo_of(cx) + 1024, 0); \
        GLDS(vx[2], p_, lx, xo_of(cx) + 2048, 0); GLDS(vx[3], p_, lx, xo_of(cx) + 3072, 0); } while (0)
        asm volatile("global_load_dwordx2 %0, %1, %2 sc1" : "=v"(ep) : "v"(0u), "s"(ws) : "memory");
        DMA_K(0); DMA_X(1); DMA_K(1);
        unsigned vw[3];
#pragma unroll
        for (int n = 0; n < 3; ++n) { const unsigned L = 64 * n + lane, row = (L * 43691u) >> 19, cc = L - row * 12; vw[n] = row * 2688 + (cc > 10 ? 10 : cc) * 16; }
        const unsigned vq = (lane >> 2) * 2688 + (lane & 3) * 16;
        const char* wqb = wa + (56 * h - 8 * (h & 1)) + (size_t)(16 * t) * 2688;
        const char* wbb = wb + (168 * cb - 8 * (cb & 1)) + (size_t)(16 * t) * 2688;
        const unsigned lq = lds0 + t * 1024, lw = lds0 + 4096 + t * 3072;
#define DMA_S(ck) do { GLDS(vq, wqb + (size_t)CK(ck) * 64 * 2688, lq, so_of(ck), 0); const char* p_ = wbb + (size_t)CK(ck) * 64 * 2688; \
        GLDS(vw[0], p_, lw, so_of(ck), 0); GLDS(vw[1], p_, lw, so_of(ck) + 1024, 0); GLDS(vw[2], p_, lw, so_of(ck) + 2048, 0); } while (0)
        WAIT_BAR(6); asm volatile("" :: "v"(dmi)); DMA_X(2); DMA_K(2);
        WAIT_BAR(6); DMA_X(3); DMA_K(3);
        WAIT_BAR(6); DMA_X(4); DMA_K(4);
        WAIT_BAR(6); DMA_X(5); DMA_K(5); DMA_S(0);
        WAIT_BAR(10); DMA_X(6); DMA_K(6); DMA_S(1);
        WAIT_BAR(14);
        WAIT_BAR(4); DMA_S(2);
        asm volatile("" : "+v"(ep)); MKTAG();
        const unsigned fo = WS_FA + ((((lane >> 4) & 1) * 8 + (h >> 1)) * 16 + (lane & 15)) * 8;
        WAIT_BAR(4); DMA_S(3);
        WAIT_BAR(4); DMA_S(4); DMA_S(5);
        WAIT_BAR(8); DMA_S(6);
        unsigned dm0;
        {
            const int li = (t & 1) * 56 + (lane < 56 ? lane : 55), c0 = (h & 3) < 3 ? (h & 3) * 256 : 640;
            asm volatile("global_load_ubyte %0, %1, %2" : "=v"(dm0) : "v"(((j & 7) * 56 + (li >> 1)) * 896 + (li & 1) * 128 + c0), "s"((t & 2) ? wob : woa) : "memory"); }
        f32x16 cbv, pbv;
        {   const unsigned long long M = (0x5555ull << (r32 + 18)) >> (4 * hi);
            int nlo = tile > 0 ? ~(unsigned)M : ~0u, nhi = ~(unsigned)(M >> 32);
#pragma unroll
            for (int r = 0; r < 16; ++r) { const int cc = (r & 3) + 8 * (r >> 2);
                int e0 = nhi << (31 - cc); PIN(e0); cbv[r] = __uint_as_float((unsigned)(e0 >> 8) & 0xFF800000u);
                if (r < 8) pbv[r] = 0.f; else { int e1 = nlo << (31 - cc); PIN(e1); pbv[r] = __uint_as_float((unsigned)(e1 >> 8) & 0xFF800000u); } }
            PIN(cbv); PIN(pbv); }
        WAIT_BAR(9);
        WAIT_BAR(0);
        asm volatile("" :: "v"(dm0));
        u64 pa;
        if (t == 0) { wait_flags_d(ws, fo, tag); lds_arrive(lds0 + L_CNT + 4, lane); } else lds_wait(lds0 + L_CNT + 4, 1);
        const char* kv0 = ws + WS_BR + BR_KV + (size_t)h * 262144 + t * 8192; const unsigned lr = lds0 + t * 8192;
#define KV4(p, LC) do { GLDSC(vl, p, lr, LC, 0); GLDSC(vl, p, lr, LC, 1024); GLDSC(vl, p, lr, LC, 2048); GLDSC(vl, p, lr, LC, 3072); } while (0)
        KV4(kv0, L_RING); KV4(kv0 + 4096, L_RING + 4096); KV4(kv0 + 32768, L_RING + 32768); KV4(kv0 + 36864, L_RING + 36864);
        WAIT_BAR(8);
        const int tt = 4 * j + (h >> 2), cg = h & 3, c = t;
        float glog = *(const LAS float*)(uintptr_t)(lds0 + L_GL + (h >> 2) * 128 + r32 * 4);
        const int col0 = cg < 3 ? cg * 128 : 320;
        const unsigned vwo = (lane >> 4) * 896 + (((lane & 15) ^ ((lane >> 4) << 2)) << 4), lc7 = lds0 + c * 7168;
        const char* wobp = wob + col0 * 2 + (size_t)(c * 7) * 3584; const char* woap = woa + col0 * 2 + (size_t)(c * 7) * 3584;
#define DWP(base, p, LC) do { if ((p) & 1) GLDS(vwo, (base) + (size_t)((p) - 1) * 3584, lc7, (LC) + 4096 + (p) * 1024 - 3584 - 4096, 3584); else GLDS(vwo, (base) + (size_t)(p) * 3584, lc7, (LC) + (p) * 1024, 0); } while (0)
#define DWQ(base, q, LC) do { const char* b_ = (base) + (size_t)(q) * 28 * 3584; DWP(b_, 0, LC); DWP(b_, 1, LC); DWP(b_, 2, LC); DWP(b_, 3, LC); DWP(b_, 4, LC); DWP(b_, 5, LC); DWP(b_, 6, LC); } while (0)
#define DWQ0(base, q) do { const char* b_ = (base) + (size_t)(q) * 28 * 3584; GLDS(vwo, b_, lc7, 0, 0); GLDS(vwo, b_ + 3584, lc7, 1024, 0); GLDS(vwo, b_ + 2 * 3584, lc7, 2048, 0); \
        GLDS(vwo, b_ + 3 * 3584, lc7, 3072, 0); DWP(b_, 4, 0); DWP(b_, 5, 0); DWP(b_, 6, 0); } while (0)
#define KVG(gr) do { const char* p_ = kv0 + (size_t)(gr) * 32768; KV4(p_, L_RING + ((gr) % 3) * 32768); KV4(p_ + 4096, L_RING + ((gr) % 3) * 32768 + 4096); } while (0)
        const int q4 = (lane & 15) >> 2, p4 = lane & 3, ch = (lane >> 4) & 1;
        const unsigned wl = lds0 + (4 * hi + q4) * 256 + (((c ^ q4) << 6) + ch * 32 + p4 * 8);
        unsigned wsa = wl + L_SA, wsk = wsa - (1024u & mhi); PIN(wsa); PIN(wsk);
        unsigned wz = wl, wzk = wl - (1024u & mhi); PIN(wz); PIN(wzk);
#define WFR(B, BK, off, k) wfrag((B) + (off) + ((k) >> 1) * 7168 + ((k) & 1) * 4096, ((k) == 7 ? (BK) : (B)) + (off) + ((k) >> 1) * 7168 + ((k) & 1) * 4096 + 2048)
        bf16x8 war[32];
#define WARQ(q, off) do { _Pragma("unroll") for (int k = 0; k < 8; ++k) war[8 * (q) + k] = WFR(wsa, wsk, off, k); } while (0)
        const unsigned bB = WS_BR + BR_SZ;
        WAIT_BAR(0);
        KVG(2); DWQ(woap, 0, L_SA);
        WAIT_BAR(0);
        if (tid == 256) set_flag(ws, WS_FB + (cb * 16 + j) * 8, tag);
        KVG(3); DWQ(woap, 1, L_SB); WARQ(0, 0);
        const unsigned fbo = WS_FB + (((16 * (lane % 3) + h) / 3) * 16 + ((lane & 4) && j > 0 ? j - 1 : j)) * 8;
        WAIT_BAR(0);
        KVG(4); DWQ(woap, 2, L_SA); WARQ(1, L_SB - L_SA);
        WAIT_BAR(0);
        asm volatile("global_load_dwordx2 %0, %1, %2 sc1" : "=v"(pa) : "v"(fbo), "s"(ws) : "memory");
        KVG(5); WARQ(2, 0);
        asm volatile("s_waitcnt vmcnt(8)" ::: "memory"); SBAR(); asm volatile("" : "+v"(pa) :: "memory");
        if (__builtin_expect(!__all(pa == tag), 0)) wait_flags_d(ws, fbo, tag);
        {   const char* qo = ws + bB + BR_Q + (size_t)(h * 64 + tile) * 2048; const char* ko = ws + bB + BR_KV + (size_t)(h * 64 + tile) * 4096;
            const unsigned lq2 = lds0 + t * 2048, lk2 = lds0 + t * 4096;
            GLDSC(vl, qo, lq2, L_SB + 20480, 0); GLDSC(vl, qo, lq2, L_SB + 20480, 1024);
            GLDSC(vl, ko, lk2, L_SB + 4096, 0); GLDSC(vl, ko, lk2, L_SB + 4096, 1024); GLDSC(vl, ko, lk2, L_SB + 4096, 2048); GLDSC(vl, ko, lk2, L_SB + 4096, 3072);
            if (t == 0) { const char* po = tile > 0 ? ko - 4096 : ko; GLDSC(vl, po, lds0, L_SB, 0); GLDSC(vl, po, lds0, L_SB, 1024); GLDSC(vl, po, lds0, L_SB, 3072); } }
        WAIT_BAR(0);
        {
            const unsigned bo = lds0 + L_SB + t * 4096 + vl, bq = lds0 + L_SB + 20480 + t * 2048 + vl;
            const bf16x8 bq0 = ldsr128(bq), bq1 = ldsr128(bq + 1024), bk0 = ldsr128(bo + 4096), bk1 = ldsr128(bo + 5120), bv0 = ldsr128(bo + 6144), bv1 = ldsr128(bo + 7168);
            const bf16x8 bp0 = ldsr128(bo), bp1 = ldsr128(bo + 1024), bpv = ldsr128(bo + 3072);
            KVG(6); DWQ(woap, 3, L_SA);
            __builtin_amdgcn_s_setprio(1);
            f32x16 sc = MFMA(bk0, bq0, cbv); sc = MFMA(bk1, bq1, sc);
            f32x16 sp = MFMA(bp0, bq0, pbv); sp = MFMA(bp1, bq1, sp);
            float m = max16(sc);
#pragma unroll
            for (int r = 8; r < 16; r += 2) m = __builtin_fmaxf(__builtin_fmaxf(m, sp[r]), sp[r + 1]);
            m = swap_max(m);
#pragma unroll
            for (int r = 0; r < 16; ++r) sc[r] = ex2(sc[r] - m);
#pragma unroll
            for (int r = 8; r < 16; ++r) sp[r] = ex2(sp[r] - m);
            u32x4 c0, c1, p0, p1; pack16(sc, c0, c1); pack16(sp, p0, p1);
            f32x16 ob = MFMA(bv0, asbf(c0), (f32x16){}); ob = MFMA(bv1, asbf(c1), ob); ob = MFMA(bpv, asbf(p1), ob);
            const float rl = __builtin_amdgcn_rcpf(from_hi(ob[12]));
#pragma unroll
            for (int i = 0; i < 16; ++i) ob[i] *= rl;
            u32x4 e0, e1; pack16(ob, e0, e1); fix_pad(e1, 0u, mhi);
            st16c(rs, e0, vl, bB + BR_O + (tile * 16 + h) * 2048); st16c(rs, e1, vl + 1024, bB + BR_O + (tile * 16 + h) * 2048);
            __builtin_amdgcn_s_setprio(0);
        }
        WAIT_BAR(2);
        KVG(7); DWQ(wobp, 0, L_SB); WARQ(3, 0);
        asm volatile("s_waitcnt vmcnt(15)" ::: "memory");
        if (lane == 0) set_flag(ws, WS_FOB + (tile * 16 + h) * 8, tag);
        const unsigned foo = WS_FOB + (tt * 16 + (lane & 15)) * 8;
        WAIT_BAR(1);
        DWQ(wobp, 1, L_SA);
        WAIT_BAR(0);
        asm volatile("global_load_dwordx2 %0, %1, %2 sc1" : "=v"(pa) : "v"(foo), "s"(ws) : "memory");
        DWQ0(wobp, 2);
        asm volatile("s_waitcnt vmcnt(7)" ::: "memory"); SBAR(); asm volatile("" : "+v"(pa) :: "memory");
        if (__builtin_expect(!__all(pa == tag), 0)) wait_flags_d(ws, foo, tag);
        {   const char* ofb = ws + bB + BR_O + (size_t)tt * 32768 + c * 8192; KV4(ofb, L_OF); KV4(ofb + 4096, L_OF + 4096); }
        unsigned oad = lds0 + L_OF + vl; PIN(oad);
        f32x16 accB = {};
        bf16x8 wf[4], of[4];
#define OPB(B, BK, off, kbase) do { \
        _Pragma("unroll") for (int i = 0; i < 4; ++i) { wf[i] = WFR(B, BK, off, i); of[i] = ldsr128(oad + ((kbase) + i) * 1024); } \
        _Pragma("unroll") for (int i = 0; i < 8; ++i) { SBAR(); accB = MFMA(wf[i & 3], of[i & 3], accB); SBAR(); \
            if (i < 4) { wf[i] = WFR(B, BK, off, i + 4); of[i] = ldsr128(oad + ((kbase) + i + 4) * 1024); } } SBAR(); } while (0)
        BAR_ONLY();
        WAIT_BAR(0);
        DWQ(wobp, 3, L_OA);
        OPB(wsa, wsk, L_SB - L_SA, 0); OPB(wsa, wsk, 0, 8); OPB(wz, wzk, 0, 16);
        if (tid == 256) *(LAS unsigned*)(uintptr_t)(lds0 + L_SCR) = 0u;
        WAIT_BAR(0);
        OPB(wz, wzk, L_OA, 24);
        float gate = __builtin_amdgcn_rcpf(1.0f + ex2(-1.4426950408889634f * glog)); const float g1 = 1.0f - gate;
#pragma unroll
        for (int i = 0; i < 16; ++i) accB[i] *= g1;
        unsigned oo = (tt * 32 + r32) * 896 + (col0 + 32 * c) * 2 + hi * 16;
        PIN(accB); PIN(gate); PIN(oo);
        if (t == 0) { wait_flags_d(ws, WS_FOA + (tt * 16 + (lane & 15)) * 8, tag); lds_arrive(lds0 + L_SCR, lane); } else lds_wait(lds0 + L_SCR, 1);
        {
            const char* ofa = ws + WS_BR + BR_O + (size_t)tt * 32768 + c * 4096; const unsigned lp = lds0 + c * 4096;
            GLDSC(vl, ofa, lp, 0, 0); GLDSC(vl, ofa, lp, 0, 1024); GLDSC(vl, ofa, lp, 0, 2048); GLDSC(vl, ofa, lp, 0, 3072);
            GLDSC(vl, ofa + 16384, lp, 16384, 0); GLDSC(vl, ofa + 16384, lp, 16384, 1024); GLDSC(vl, ofa + 16384, lp, 16384, 2048); GLDSC(vl, ofa + 16384, lp, 16384, 3072); }
        WAIT_BAR(4);
        f32x16 acc = {};
        {   unsigned ob_ = lds0 + vl; PIN(ob_);
            bf16x8 og[8];
#pragma unroll
            for (int i = 0; i < 8; ++i) og[i] = ldsr128(ob_ + i * 1024);
#pragma unroll
            for (int i = 0; i < 32; ++i) { if (i == 8) { WAIT_BAR(0); } SBAR(); acc = MFMA(war[i], og[i & 7], acc); SBAR(); if (i < 24) og[i & 7] = ldsr128(ob_ + (i + 8) * 1024); }
            SBAR();
        }
        if (bid == 0 && tid == 256) __hip_atomic_store((GAS u64*)(ws + WS_EPOCH), ep + 1, RLX_AGENT);
        if (cg < 3 || c >= 2) {
#pragma unroll
            for (int i = 0; i < 16; ++i) acc[i] = acc[i] * gate + accB[i];
            u32x4 s0_, s1_; pack16(acc, s0_, s1_);
            auto r0 = __builtin_amdgcn_permlane32_swap(s0_[0], s0_[2], false, false), r1 = __builtin_amdgcn_permlane32_swap(s0_[1], s0_[3], false, false);
            auto r2 = __builtin_amdgcn_permlane32_swap(s1_[0], s1_[2], false, false), r3 = __builtin_amdgcn_permlane32_swap(s1_[1], s1_[3], false, false);
            char* op = out + oo;
            *(GAS u32x4*)op = (u32x4){r0[0], r1[0], r0[1], r1[1]};
            *(GAS u32x4*)(op + 32) = (u32x4){r2[0], r3[0], r2[1], r3[1]};
        }
    }
    asm volatile(".Lfk_end:");
}

extern "C" void kernel_launch(void* const* d_in, const int* in_sizes, int n_in,
                              void* d_out, int out_size, void* d_ws, size_t ws_size,
                              hipStream_t stream) {
    (void)in_sizes; (void)n_in; (void)out_size; (void)ws_size;
    (void)hipFuncSetAttribute((const void*)fused_kernel, hipFuncAttributeMaxDynamicSharedMemorySize, LDS_BYTES);
    fused_kernel<<<256, 512, LDS_BYTES, stream>>>((const char*)d_in[0], (const char*)d_in[1], (const char*)d_in[2], (const char*)d_in[4],
                                                  (const char*)d_in[5], (const char*)d_in[6], (char*)d_ws, (char*)d_out);
}
```

```cpp
#include <hip/hip_runtime.h>
#include <hip/hip_bf16.h>
#include <stdint.h>

typedef short bf16x8 __attribute__((ext_vector_type(8)));
typedef short s16x4 __attribute__((ext_vector_type(4)));
typedef float f32x16 __attribute__((ext_vector_type(16)));
typedef float f32x4 __attribute__((ext_vector_type(4)));
typedef float f32x2 __attribute__((ext_vector_type(2)));
typedef unsigned u32x4 __attribute__((ext_vector_type(4)));
typedef unsigned u32x2 __attribute__((ext_vector_type(2)));
typedef __bf16 bf2_t __attribute__((ext_vector_type(2)));
typedef unsigned long long u64;
#define LAS __attribute__((address_space(3)))
#define GAS __attribute__((address_space(1)))
#define DI __device__ __forceinline__
#define MFMA(a, b, c) __builtin_amdgcn_mfma_f32_32x32x16_bf16((a), (b), (c), 0, 0, 0)
#define SBAR() __builtin_amdgcn_sched_barrier(0)
#define PIN(x) asm volatile("" : "+v"(x))
#define RLX_AGENT __ATOMIC_RELAXED, __HIP_MEMORY_SCOPE_AGENT

constexpr float QSCALE = 0.18898223650461362f * 1.4426950408889634f;

constexpr unsigned WS_EPOCH = 0;
constexpr unsigned WS_FA = 24576;
constexpr unsigned WS_FB = 6144;
constexpr unsigned WS_FOA = WS_FB + 2048;
constexpr unsigned WS_FOB = WS_FOA + 8192;
constexpr unsigned WS_BR = 65536;
constexpr unsigned BR_Q = 0, BR_KV = 2097152, BR_O = 6291456, BR_SZ = 8388608;
constexpr unsigned WS_TOTAL = WS_BR + 2 * BR_SZ;

constexpr unsigned L_XB = 0;
constexpr unsigned L_KW = 98304;
constexpr unsigned L_GT = 155648;
constexpr unsigned L_CNT = 159232;
constexpr unsigned L_GL = 159744;
constexpr unsigned L_Q = 139264;
constexpr unsigned L_RING = 0;
constexpr unsigned L_SA = 98304, L_SB = 126976;
constexpr unsigned L_OF = 65536;
constexpr unsigned L_OA = 32768;
constexpr unsigned L_SCR = 155648;
constexpr unsigned LDS_BYTES = 163840;

DI unsigned cvtpk(float lo, float hi) { f32x2 v = {lo, hi}; return __builtin_bit_cast(unsigned, __builtin_convertvector(v, bf2_t)); }
#define GLDS(voff, sbase, lds_s, LC, OFF) asm volatile("s_add_i32 m0, %2, %3\n\ts_nop 0\n\tglobal_load_lds_dwordx4 %0, %1 offset:%4" \
        :: "v"(voff), "s"(sbase), "s"(__builtin_amdgcn_readfirstlane(lds_s)), "n"((int)(LC)), "n"((int)(OFF)) : "memory", "scc")
#define GLDSC(voff, sbase, lds_s, LC, OFF) asm volatile("s_add_i32 m0, %2, %3\n\ts_nop 0\n\tglobal_load_lds_dwordx4 %0, %1 offset:%4 sc1" \
        :: "v"(voff), "s"(sbase), "s"(__builtin_amdgcn_readfirstlane(lds_s)), "n"((int)(LC)), "n"((int)(OFF)) : "memory", "scc")
#define GLDS_IF(sel, voff, sbase, lds_s, LC, OFF) asm volatile("s_cmp_lg_u32 %5, 0\n\ts_cbranch_scc1 1f\n\ts_add_i32 m0, %2, %3\n\ts_nop 0\n\tglobal_load_lds_dwordx4 %0, %1 offset:%4\n1:" \
        :: "v"(voff), "s"(sbase), "s"(__builtin_amdgcn_readfirstlane(lds_s)), "n"((int)(LC)), "n"((int)(OFF)), "s"(sel) : "memory", "scc")
#define WAIT_BAR(N) asm volatile("s_waitcnt vmcnt(" #N ") lgkmcnt(0)\n\ts_barrier" ::: "memory")
#define BAR_ONLY() asm volatile("s_waitcnt lgkmcnt(0)\n\ts_barrier" ::: "memory")
DI bf16x8 ldsr128(unsigned a) { return *(const LAS bf16x8*)(uintptr_t)a; }
typedef short v4i16_t __attribute__((ext_vector_type(4)));
DI s16x4 ldstr(unsigned a) { return __builtin_bit_cast(s16x4, __builtin_amdgcn_ds_read_tr16_b64_v4i16((LAS v4i16_t*)(uintptr_t)a)); }
DI bf16x8 wfrag(unsigned a0, unsigned a1) { s16x4 lo = ldstr(a0), hi = ldstr(a1); return __builtin_shufflevector(lo, hi, 0, 1, 2, 3, 4, 5, 6, 7); }
DI bf16x8 asbf(u32x4 v) { return __builtin_bit_cast(bf16x8, v); }
DI float ex2(float v) { return __builtin_amdgcn_exp2f(v); }
DI float swap_max(float m) {
    auto rr = __builtin_amdgcn_permlane32_swap(__float_as_uint(m), __float_as_uint(m), false, false);
    return __builtin_fmaxf(__uint_as_float(rr[0]), __uint_as_float(rr[1]));
}
DI float from_hi(float v) {
    auto rr = __builtin_amdgcn_permlane32_swap(__float_as_uint(v), __float_as_uint(v), false, false);
    return __uint_as_float(rr[1]);
}
DI void pack16(const f32x16& a, u32x4& s0, u32x4& s1) {
    s0 = (u32x4){cvtpk(a[0], a[1]), cvtpk(a[2], a[3]), cvtpk(a[4], a[5]), cvtpk(a[6], a[7])};
    s1 = (u32x4){cvtpk(a[8], a[9]), cvtpk(a[10], a[11]), cvtpk(a[12], a[13]), cvtpk(a[14], a[15])};
}
DI float max16(const f32x16& s) {
    float m = __builtin_fmaxf(s[0], s[1]);
#pragma unroll
    for (int r = 2; r < 16; r += 2) m = __builtin_fmaxf(__builtin_fmaxf(m, s[r]), s[r + 1]);
    return m;
}
typedef __amdgpu_buffer_rsrc_t rsrc_t;
DI void st16c(rsrc_t r, u32x4 v, unsigned voff, unsigned soff) { __builtin_amdgcn_raw_buffer_store_b128(v, r, voff, soff, 16); }
DI bf16x8 ld16c(rsrc_t r, unsigned voff, unsigned soff) { return asbf(__builtin_amdgcn_raw_buffer_load_b128(r, voff, soff, 16)); }
DI void wait_flags_d(const char* ws, unsigned off, u64 tag) {
#pragma unroll 1
    for (int it = 0; it < 40000; ++it) {
        u64 a;
        asm volatile("global_load_dwordx2 %0, %1, %2 sc1\n\ts_waitcnt vmcnt(0)" : "=&v"(a) : "v"(off), "s"(ws) : "memory");
        if (__all(a == tag)) break;
        __builtin_amdgcn_s_sleep(4);
    }
}
DI void wait_flags2(const char* ws, unsigned off, u64 tag) {
#pragma unroll 1
    for (int it = 0; it < 40000; ++it) {
        u32x4 a;
        asm volatile("global_load_dwordx4 %0, %1, %2 sc1\n\ts_waitcnt vmcnt(0)" : "=&v"(a) : "v"(off), "s"(ws) : "memory");
        if (__all(a[0] == (unsigned)tag && a[2] == (unsigned)tag && a[1] == (unsigned)(tag >> 32) && a[3] == (unsigned)(tag >> 32))) break;
        __builtin_amdgcn_s_sleep(4);
    }
}
DI void set_flag(char* ws, unsigned off, u64 tag) { __hip_atomic_store((GAS u64*)(ws + off), tag, RLX_AGENT); }


DI void fix_v(u32x4& s0, u32x4& s1, unsigned m28) {
    unsigned keep = (m28 & 1u) - 1u, ones = m28 & 0x3F803F80u; PIN(keep); PIN(ones);
#pragma unroll
    for (int i = 0; i < 4; ++i) { s0[i] = (s0[i] & keep) | ones; s1[i] = (s1[i] & keep) | ones; }
}
DI void lds_arrive(unsigned a, int lane) { if (lane == 0) __hip_atomic_fetch_add((LAS unsigned*)(uintptr_t)a, 1u, __ATOMIC_RELAXED, __HIP_MEMORY_SCOPE_WORKGROUP); }
DI void lds_wait(unsigned a, unsigned n) {
#pragma unroll 1
    for (int it = 0; it < 2000000; ++it) {
        if (__builtin_amdgcn_readfirstlane(__hip_atomic_load((LAS unsigned*)(uintptr_t)a, __ATOMIC_RELAXED, __HIP_MEMORY_SCOPE_WORKGROUP)) >= n) break;
        __builtin_amdgcn_s_sleep(1);
    }
    asm volatile("" ::: "memory");
}
DI void fix_pad(u32x4& s1, unsigned pad, unsigned mhi) { s1[2] = (s1[2] & ~mhi) | (pad & mhi); s1[3] &= ~mhi; }
template <bool V>
DI void pack_unit(f32x16 a, int u, unsigned m28, unsigned mhi, u32x4& s0, u32x4& s1) {
    if (!V) {
        const bool isq = u < 16;
        const float sc = isq ? QSCALE : 1.0f;
#pragma unroll
        for (int i = 0; i < 16; ++i) a[i] *= sc;
        pack16(a, s0, s1);
        fix_pad(s1, isq ? 0u : 0x00003F80u, mhi);
    } else { pack16(a, s0, s1); fix_v(s0, s1, m28); }
}
#define MKTAG() do { u64 z = ((u64)__builtin_amdgcn_readfirstlane((unsigned)(ep >> 32)) << 32) | (unsigned)__builtin_amdgcn_readfirstlane((unsigned)ep); \
        z += 0x9E3779B97F4A7C15ull; z = (z ^ (z >> 30)) * 0xBF58476D1CE4E5B9ull; z = (z ^ (z >> 27)) * 0x94D049BB133111EBull; tag = z ^ (z >> 31); } while (0)
#define XB(i) (L_XB + (i) * 16384)
#define KW(i) (L_KW + (i) * 8192)
#define MM(VV, acc, wf, xfr) do { if (VV) acc = MFMA(xfr, wf, acc); else acc = MFMA(wf, xfr, acc); } while (0)
__device__ constexpr unsigned so_of(int c) { return c == 0 ? KW(0) : (c == 6 ? KW(2) : XB(c)); }
__device__ constexpr unsigned xo_of(int c) { return c == 6 ? XB(0) : XB(c); }

template <bool VK>
DI void stage1(bf16x8 (&xf)[28], const unsigned (&xa)[4], const unsigned (&xh)[4], unsigned ka0, unsigned ka1, rsrc_t rs, int h, int tile, int lane, unsigned m28, unsigned mhi,
               unsigned gv0, unsigned gv1, unsigned dmc, unsigned dmi, u64 ep, u64& tag, unsigned lds0) {
    f32x16 acc0 = {}, acc1 = {};
    bf16x8 w0[2], w1[2];
#define XR(s) xf[s] = ldsr128(xo_of((s) >> 2) >= 65536 ? xh[(s) & 3] + (xo_of((s) >> 2) - 65536) : xa[(s) & 3] + xo_of((s) >> 2))
#define KF(base, s) wfrag((base) + (s) * 2048, (base) + (s) * 2048 + 512)
    XR(0); w0[0] = KF(ka0, 0); w1[0] = KF(ka1, 0); XR(1); w0[1] = KF(ka0, 1); w1[1] = KF(ka1, 1);
#pragma unroll
    for (int s = 0; s < 28; ++s) {
        if ((s & 3) == 2 && s < 26) { BAR_ONLY(); }
        if (s == 20) {
            asm volatile("s_waitcnt vmcnt(0)" ::: "memory"); SBAR();
            asm volatile("" : "+v"(gv0), "+v"(gv1), "+v"(ep) : "v"(dmc), "v"(dmi) : "memory");
            const int tid = threadIdx.x;
            { u32x2 e = {gv0, 0u}; *(LAS u32x2*)(uintptr_t)(lds0 + L_GT + tid * 8) = e; }
            if (tid < 192) { u32x2 e = {gv1, 0u}; *(LAS u32x2*)(uintptr_t)(lds0 + L_GT + 2048 + tid * 8) = e; }
            if (tid == 0) *(LAS u32x2*)(uintptr_t)(lds0 + L_CNT) = (u32x2){0u, 0u};
            MKTAG();
        }
        SBAR();
        MM(VK, acc0, w0[s & 1], xf[s]); SBAR();
        if (s + 2 < 28) { XR(s + 2); w0[s & 1] = KF(ka0, s + 2); }
        SBAR();
        MM(VK, acc1, w1[s & 1], xf[s]); SBAR();
        if (s + 2 < 28) w1[s & 1] = KF(ka1, s + 2);
    }
    SBAR();
    u32x4 s0, s1;
    pack16(acc0, s0, s1); if (!VK) fix_pad(s1, 0x00003F80u, mhi); else fix_v(s0, s1, m28);
    const unsigned off = WS_BR + BR_KV + ((2 * h & 15) * 64 + tile) * 4096 + (VK ? 2048 : 0);
    st16c(rs, s0, lane * 16, off); st16c(rs, s1, lane * 16 + 1024, off);
    pack16(acc1, s0, s1); if (!VK) fix_pad(s1, 0x00003F80u, mhi); else fix_v(s0, s1, m28);
    st16c(rs, s0, lane * 16, off + 262144); st16c(rs, s1, lane * 16 + 1024, off + 262144);
}
template <bool V01, bool V2>
DI void stage2(const bf16x8 (&xf)[28], unsigned ql, unsigned qh, unsigned gl, bool spec, unsigned wlo, unsigned whi, char* ws, rsrc_t rs, unsigned lds0,
               int h, int cb, int j, int t, int lane, u64 tag, u32x4 (&bp)[6], unsigned m28, unsigned mhi) {
    f32x16 aq = {}, acc0 = {}, acc1 = {}, acc2 = {};
    bf16x8 wq[2], w0[2], w1[2], w2[2];
    const unsigned gstride = spec ? 32u : 256u;
    unsigned sent = 0, tmp_; unsigned fao = WS_FA + (h * 64 + 4 * j + t) * 8; u64 tagv = tag; PIN(fao); PIN(tagv);
#define SADDR(lo, hi_, c) (so_of(c) >= 65536 ? (hi_) + (so_of(c) - 65536) : (lo) + so_of(c))
#define WF(s, u) wfrag(SADDR(wlo, whi, (s) >> 2) + ((s) & 3) * 3072 + (u) * 56, SADDR(wlo, whi, (s) >> 2) + ((s) & 3) * 3072 + 768 + (u) * 56)
#define QR(s) do { const unsigned a_ = spec ? gl + 128 * (s) : SADDR(ql, qh, (s) >> 2) + ((s) & 3) * 1024; wq[(s) & 1] = wfrag(a_, a_ + gstride); } while (0)
    QR(0); w0[0] = WF(0, 0); w1[0] = WF(0, 1); w2[0] = WF(0, 2); QR(1); w0[1] = WF(1, 0); w1[1] = WF(1, 1); w2[1] = WF(1, 2);
#pragma unroll
    for (int s = 0; s < 28; ++s) {
        if (s == 6 || s == 10 || s == 14) { asm volatile("s_barrier" ::: "memory"); }
        if (s == 18) { BAR_ONLY(); }
        if (s >= 7 && s <= 15) {
            if (s == 15) asm volatile("s_waitcnt vmcnt(0)" ::: "memory");
            asm volatile("s_getreg_b32 %1, hwreg(HW_REG_IB_STS, 0, 4)\n\ts_or_b32 %1, %1, %0\n\ts_cbranch_scc1 1f\n\ts_mov_b32 %0, 1\n\ts_mov_b64 exec, 1\n\t"
                         "global_store_dwordx2 %2, %3, %4 sc1\n\ts_mov_b64 exec, -1\n1:" : "+s"(sent), "=&s"(tmp_) : "v"(fao), "v"(tagv), "s"(ws) : "memory", "scc");
        }
        SBAR();
        aq = MFMA(wq[s & 1], xf[s], aq); SBAR();
        if (s + 2 < 28) QR(s + 2);
        SBAR();
        MM(V01, acc0, w0[s & 1], xf[s]); SBAR();
        if (s + 2 < 28) w0[s & 1] = WF(s + 2, 0);
        SBAR();
        MM(V01, acc1, w1[s & 1], xf[s]); SBAR();
        if (s + 2 < 28) w1[s & 1] = WF(s + 2, 1);
        SBAR();
        MM(V2, acc2, w2[s & 1], xf[s]); SBAR();
        if (s + 2 < 28) w2[s & 1] = WF(s + 2, 2);
    }
    SBAR();
    {
        if (lane >> 5) *(LAS float*)(uintptr_t)(lds0 + L_GL + t * 128 + (lane & 31) * 4) = aq[12];
#pragma unroll
        for (int i = 0; i < 16; ++i) aq[i] *= QSCALE;
        u32x4 s0, s1; pack16(aq, s0, s1);
        fix_pad(s1, 0u, mhi);
        *(LAS u32x4*)(uintptr_t)(lds0 + L_Q + t * 2048 + lane * 16) = s0; *(LAS u32x4*)(uintptr_t)(lds0 + L_Q + t * 2048 + 1024 + lane * 16) = s1;
    }
    pack_unit<V01>(acc0, 3 * cb, m28, mhi, bp[0], bp[1]); pack_unit<V01>(acc1, 3 * cb + 1, m28, mhi, bp[2], bp[3]); pack_unit<V2>(acc2, 3 * cb + 2, m28, mhi, bp[4], bp[5]);
}

__global__ __launch_bounds__(512, 2) void fused_kernel(const char* __restrict__ x, const char* __restrict__ wa, const char* __restrict__ woa,
                                                       const char* __restrict__ wb, const char* __restrict__ wob, const char* __restrict__ gw,
                                                       char* ws, char* __restrict__ out) {
    extern __shared__ __attribute__((aligned(16))) char smem[];
    const unsigned lds0 = (unsigned)(uintptr_t)(LAS char*)smem;
    const int tid = threadIdx.x, lane = tid & 63, w = __builtin_amdgcn_readfirstlane(tid >> 6), t = w & 3, g = w >> 2;
    const int bid = blockIdx.x, xg = bid & 7, idx = bid >> 3;
    const int h = (xg >> 1) * 4 + (idx & 3), j = (xg & 1) * 8 + (idx >> 2);
    const int r32 = lane & 31, hi = lane >> 5;
    const rsrc_t rs = __builtin_amdgcn_make_buffer_rsrc(ws, 0, WS_TOTAL, 0x00020000);
    const unsigned vl = lane * 16;
    asm volatile("" :: "s"(x), "s"(wa), "s"(woa), "s"(wb), "s"(wob), "s"(gw), "s"(ws), "s"(out));
    u64 pc; unsigned len;
    asm volatile("s_getpc_b64 %0\n.Lfk_ref:\n\ts_mov_b32 %1, .Lfk_end-.Lfk_ref" : "=s"(pc), "=s"(len));
    const int cb = (int)((0xCFEDBA8967542103ull >> (4 * h)) & 15);
    const int tile = 4 * j + t;
    const int rot = (j + 2 * (h & 3)) % 7;
#define CK(p) ((p) + rot >= 7 ? (p) + rot - 7 : (p) + rot)
    const unsigned vxe = (lane >> 3) * 896 + (((lane & 7) ^ (lane >> 4)) << 4), lx = lds0 + t * 4096;
    const char* xb = x + (size_t)(j * 128 + t * 32) * 896;
    unsigned dmi;
    {
        const unsigned co = (unsigned)((idx * 8 + w) * 2 + (lane & 1)) * 128u;
        asm volatile("global_load_ubyte %0, %1, %2" : "=v"(dmi) : "v"(co < len ? co : 0u), "s"(pc) : "memory"); }
    {
        const char* p_ = xb + rot * 128;
        GLDS_IF(g, vxe, p_, lx, XB(0), 0); GLDS_IF(g, (vxe ^ 64u) + 7168, p_, lx, XB(0) + 1024, 0); GLDS_IF(g, vxe + 14336, p_, lx, XB(0) + 2048, 0); GLDS_IF(g, (vxe ^ 64u) + 21504, p_, lx, XB(0) + 3072, 0); }
    u64 ep, tag;
    unsigned m28 = r32 == 28 ? ~0u : (r32 > 28 ? 1u : 0u), mhi = hi ? ~0u : 0u; PIN(m28); PIN(mhi);
    if (g == 0) {
        unsigned gv0, gv1, dmc;
        {
            const int pl = lane & 15;
            const char* tp = pl < 9 ? ws + (size_t)pl * 2097152 + 32768 : (pl == 9 ? (const char*)out : (pl == 10 ? woa : (pl == 11 ? wob : wb)));
            asm volatile("global_load_ubyte %0, %1, off sc1" : "=v"(dmc) : "v"(tp) : "memory");
        }
        asm volatile("global_load_ushort %0, %1, %2" : "=v"(gv0) : "v"((CK(t) * 64 + lane) * 2), "s"(gw) : "memory");
        asm volatile("global_load_ushort %0, %1, %2" : "=v"(gv1) : "v"((CK(t < 3 ? 4 + t : 6) * 64 + lane) * 2), "s"(gw) : "memory");
        asm volatile("global_load_dwordx2 %0, %1, %2 sc1" : "=v"(ep) : "v"(0u), "s"(ws) : "memory");
        const int fr = (r32 >> 1) & 7;
        unsigned xa[4], xh[4];
#pragma unroll
        for (int ks = 0; ks < 4; ++ks) { xa[ks] = lds0 + (4 * t + (r32 >> 3)) * 1024 + (r32 & 7) * 128 + (((2 * ks + hi) ^ fr) << 4); xh[ks] = xa[ks] + 65536; PIN(xa[ks]); PIN(xh[ks]); }
        const int q4 = (lane & 15) >> 2, p4 = lane & 3, ch = (lane >> 4) & 1;
        const unsigned o0 = ch * 32 + p4 * 8, o1 = o0 + 56, sw = (q4 >> 1) << 6;
        unsigned ka0 = lds0 + L_KW + (8 * hi + q4) * 128 + (o0 ^ sw), ka1 = lds0 + L_KW + (8 * hi + q4) * 128 + (o1 ^ sw); PIN(ka0); PIN(ka1);
        unsigned ql = lds0 + (8 * hi + q4) * 64 + ch * 32 + p4 * 8 + (h & 1) * 8, qh = ql + 65536; PIN(ql); PIN(qh);
        unsigned wlo = lds0 + 4096 + (8 * hi + q4) * 192 + ch * 32 + p4 * 8 + (cb & 1) * 8, whi = wlo + 65536; PIN(wlo); PIN(whi);
        const bool spec = ch == 1 && p4 == 3;
        const unsigned gl = lds0 + L_GT + 64 * hi + 8 * q4;
        bf16x8 xf[28]; u32x4 bp[6];
        WAIT_BAR(4);
        if (h < 8) stage1<false>(xf, xa, xh, ka0, ka1, rs, h, tile, lane, m28, mhi, gv0, gv1, dmc, dmi, ep, tag, lds0); else stage1<true>(xf, xa, xh, ka0, ka1, rs, h, tile, lane, m28, mhi, gv0, gv1, dmc, dmi, ep, tag, lds0);
        BAR_ONLY();
        if (cb <= 9) stage2<false, false>(xf, ql, qh, gl, spec, wlo, whi, ws, rs, lds0, h, cb, j, t, lane, tag, bp, m28, mhi);
        else if (cb == 10) stage2<false, true>(xf, ql, qh, gl, spec, wlo, whi, ws, rs, lds0, h, cb, j, t, lane, tag, bp, m28, mhi);
        else stage2<true, true>(xf, ql, qh, gl, spec, wlo, whi, ws, rs, lds0, h, cb, j, t, lane, tag, bp, m28, mhi);
        BAR_ONLY();
#pragma unroll
        for (int i = 0; i < 3; ++i) { const int u = 3 * cb + i;
            const unsigned off = WS_BR + BR_SZ + (u < 16 ? BR_Q + (u * 64 + tile) * 2048 : BR_KV + ((u & 15) * 64 + tile) * 4096 + (u >= 32 ? 2048 : 0));
            st16c(rs, bp[2 * i], vl, off); st16c(rs, bp[2 * i + 1], vl + 1024, off); }
        const int qp = t & 1, kh = t >> 1;
        const unsigned qad = lds0 + L_Q + qp * 4096 + kh * 2048 + vl, qbd = qad ^ 2048u;
        const bf16x8 qa0 = ldsr128(qad), qb0 = ldsr128(qbd);
        u32x4 qa1 = __builtin_bit_cast(u32x4, ldsr128(qad + 1024)), qb1 = __builtin_bit_cast(u32x4, ldsr128(qbd + 1024));
        unsigned ac = lds0 + L_RING + kh * 4096 + vl, an = ac + 32768, af = ac + 65536;
        f32x16 oa = {}, ob = {}, sA, sB; u32x4 pA0 = {}, pA1 = {}, pB0 = {}, pB1 = {};
        bf16x8 k0 = ldsr128(ac), k1 = ldsr128(ac + 1024), v0 = {}, v1 = {}, kn0, kn1;
        float mra, mrb;
        {
            sA = MFMA(k0, qa0, (f32x16){}); sB = MFMA(k0, qb0, (f32x16){});
            f32x16 ta = MFMA(k1, asbf(qa1), sA); sB = MFMA(k1, asbf(qb1), sB);
            const unsigned na = cvtpk(-swap_max(max16(ta)), 0.f), nb = cvtpk(-swap_max(max16(sB)), 0.f);
            mra = -__uint_as_float(na << 16); mrb = -__uint_as_float(nb << 16);
            qa1[2] = (qa1[2] & ~mhi) | (na & mhi); qb1[2] = (qb1[2] & ~mhi) | (nb & mhi);
            sA = MFMA(k1, asbf(qa1), sA);
        }
#define GRP(S, b, SP, bp, PP, jp) do { PP[jp] = cvtpk(SP[bp], SP[bp + 1]); PP[jp + 1] = cvtpk(SP[bp + 2], SP[bp + 3]); SBAR(); \
        S[b] = ex2(S[b]); S[b + 1] = ex2(S[b + 1]); S[b + 2] = ex2(S[b + 2]); S[b + 3] = ex2(S[b + 3]); PIN(PP); PIN(S); SBAR(); } while (0)
#define STEP2(KC0, KC1, KN0, KN1, VP1, VC1, ka, va) do { \
        KN0 = ldsr128(ka); ob = MFMA(v0, asbf(pB0), ob); SBAR(); GRP(sA, 0, sB, 12, pB1, 2); KN1 = ldsr128((ka) + 1024); \
        sB = MFMA(KC0, qb0, (f32x16){}); SBAR(); VC1 = ldsr128((va) + 3072); GRP(sA, 4, sA, 0, pA0, 0); v0 = ldsr128((va) + 2048); \
        sB = MFMA(KC1, asbf(qb1), sB); SBAR(); GRP(sA, 8, sA, 4, pA0, 2); \
        ob = MFMA(VP1, asbf(pB1), ob); SBAR(); GRP(sA, 12, sA, 8, pA1, 0); \
        oa = MFMA(v0, asbf(pA0), oa); SBAR(); GRP(sB, 0, sA, 12, pA1, 2); \
        sA = MFMA(KN0, qa0, (f32x16){}); SBAR(); GRP(sB, 4, sB, 0, pB0, 0); \
        sA = MFMA(KN1, asbf(qa1), sA); SBAR(); GRP(sB, 8, sB, 4, pB0, 2); \
        oa = MFMA(VC1, asbf(pA1), oa); SBAR(); GRP(sB, 12, sB, 8, pB1, 0); \
    } while (0)
        sB = (f32x16){};
        bf16x8 v1n;
#pragma unroll 1
        for (int gr = 0; gr < 8; ++gr) {
            STEP2(k0, k1, kn0, kn1, v1, v1n, ac + 8192, ac);
            STEP2(kn0, kn1, k0, k1, v1n, v1, ac + 16384, ac + 8192);
            if (gr == 0) asm volatile("s_barrier" ::: "memory"); else asm volatile("s_waitcnt vmcnt(0)\n\ts_barrier" ::: "memory");
            SBAR();
            STEP2(k0, k1, kn0, kn1, v1, v1n, ac + 24576, ac + 16384);
            STEP2(kn0, kn1, k0, k1, v1n, v1, an, ac + 24576);
            const unsigned tmp = ac; ac = an; an = af; af = tmp;
        }
        pB1[2] = cvtpk(sB[12], sB[13]); pB1[3] = cvtpk(sB[14], sB[15]);
        ob = MFMA(v0, asbf(pB0), ob); ob = MFMA(v1, asbf(pB1), ob);
        if (__builtin_expect(__any(hi && !(oa[12] < 1.0e27f && ob[12] < 1.0e27f && oa[12] > 1.0e-27f && ob[12] > 1.0e-27f)), 0)) {
            qa1[2] &= ~mhi; qb1[2] &= ~mhi;
            mra = -__builtin_inff(); mrb = mra; oa = (f32x16){}; ob = (f32x16){};
            const unsigned kb = WS_BR + BR_KV + h * 262144 + kh * 4096;
#pragma unroll 1
            for (int i = 0; i < 32; ++i) {
                const unsigned so = kb + i * 8192;
                const bf16x8 a0 = ld16c(rs, vl, so), a1 = ld16c(rs, vl + 1024, so), b0 = ld16c(rs, vl + 2048, so), b1 = ld16c(rs, vl + 3072, so);
#define ONLINE(Q0, Q1, O, M) do { f32x16 s_ = MFMA(a0, Q0, (f32x16){}); s_ = MFMA(a1, asbf(Q1), s_); \
                const float mn = __builtin_fmaxf(M, swap_max(max16(s_))), al = ex2(M - mn); M = mn; \
                _Pragma("unroll") for (int r = 0; r < 16; ++r) { s_[r] = ex2(s_[r] - mn); O[r] *= al; } \
                u32x4 e0, e1; pack16(s_, e0, e1); O = MFMA(b0, asbf(e0), O); O = MFMA(b1, asbf(e1), O); } while (0)
                ONLINE(qa0, qa1, oa, mra); ONLINE(qb0, qb1, ob, mrb);
            }
        }
        {   const unsigned cw_ = lds0 + L_OA + t * 4096 + vl;
#define PUTP(O, M) do { O[13] = __uint_as_float((__float_as_uint(O[13]) & ~mhi) | (__float_as_uint(M) & mhi)); \
            _Pragma("unroll") for (int i = 0; i < 4; ++i) *(LAS f32x4*)(uintptr_t)(cw_ + i * 1024) = (f32x4){O[4 * i], O[4 * i + 1], O[4 * i + 2], O[4 * i + 3]}; } while (0)
            PUTP(ob, mrb);
        }
        BAR_ONLY();
        {   const unsigned cbs = lds0 + L_OA + (t ^ 2) * 4096 + vl;
            f32x16 o1;
#pragma unroll
            for (int i = 3; i >= 0; --i) { const f32x4 e = *(const LAS f32x4*)(uintptr_t)(cbs + i * 1024); o1[4 * i] = e[0]; o1[4 * i + 1] = e[1]; o1[4 * i + 2] = e[2]; o1[4 * i + 3] = e[3]; }
            const float m1 = from_hi(o1[13]);
            const float mm = __builtin_fmaxf(mra, m1), a0 = ex2(mra - mm), a1 = ex2(m1 - mm);
            const float rl = __builtin_amdgcn_rcpf(from_hi(oa[12] * a0 + o1[12] * a1)), f0 = a0 * rl, f1 = a1 * rl;
            f32x16 v;
#pragma unroll
            for (int i = 0; i < 16; ++i) v[i] = oa[i] * f0 + o1[i] * f1;
            u32x4 e0, e1; pack16(v, e0, e1);
            fix_pad(e1, 0u, mhi);
            const int ot = 4 * j + 2 * qp + kh;
            const unsigned so = WS_BR + BR_O + (ot * 16 + h) * 2048;
            st16c(rs, e0, vl, so); st16c(rs, e1, vl + 1024, so);
            asm volatile("s_waitcnt lgkmcnt(0)\n\ts_barrier\n\ts_waitcnt vmcnt(0)" ::: "memory");
            if (lane == 0) set_flag(ws, WS_FOA + (ot * 16 + h) * 8, tag);
        }
    } else {
        const unsigned vx[4] = {vxe, (vxe ^ 64u) + 7168, vxe + 14336, (vxe ^ 64u) + 21504};
        const unsigned kc = (lane & 7) ^ (((lane >> 4) & 1) << 2);
        const unsigned vk0 = (lane >> 3) * 2688 + (kc > 6 ? 6 : kc) * 16, vk1 = vk0 + 8 * 2688;
        const char* wkb = wa + 896 + 112 * h + (size_t)(16 * t) * 2688;
        const unsigned lk = lds0 + t * 2048;
#define DMA_K(ck) do { const char* p_ = wkb + (size_t)CK(ck) * 64 * 2688; GLDS(vk0, p_, lk, KW(ck), 0); GLDS(vk1, p_, lk, KW(ck) + 1024, 0); } while (0)
#define DMA_X(cx) do { const char* p_ = xb + CK(cx) * 128; GLDS(vx[0], p_, lx, xo_of(cx), 0); GLDS(vx[1], p_, lx, xo_of(cx) + 1024, 0); \
        GLDS(vx[2], p_, lx, xo_of(cx) + 2048, 0); GLDS(vx[3], p_, lx, xo_of(cx) + 3072, 0); } while (0)
        asm volatile("global_load_dwordx2 %0, %1, %2 sc1" : "=v"(ep) : "v"(0u), "s"(ws) : "memory");
        DMA_K(0); DMA_X(1); DMA_K(1);
        unsigned vw[3];
#pragma unroll
        for (int n = 0; n < 3; ++n) { const unsigned L = 64 * n + lane, row = (L * 43691u) >> 19, cc = L - row * 12; vw[n] = row * 2688 + (cc > 10 ? 10 : cc) * 16; }
        const unsigned vq = (lane >> 2) * 2688 + (lane & 3) * 16;
        const char* wqb = wa + (56 * h - 8 * (h & 1)) + (size_t)(16 * t) * 2688;
        const char* wbb = wb + (168 * cb - 8 * (cb & 1)) + (size_t)(16 * t) * 2688;
        const unsigned lq = lds0 + t * 1024, lw = lds0 + 4096 + t * 3072;
#define DMA_S(ck) do { GLDS(vq, wqb + (size_t)CK(ck) * 64 * 2688, lq, so_of(ck), 0); const char* p_ = wbb + (size_t)CK(ck) * 64 * 2688; \
        GLDS(vw[0], p_, lw, so_of(ck), 0); GLDS(vw[1], p_, lw, so_of(ck) + 1024, 0); GLDS(vw[2], p_, lw, so_of(ck) + 2048, 0); } while (0)
        WAIT_BAR(6); asm volatile("" :: "v"(dmi)); DMA_X(2); DMA_K(2);
        WAIT_BAR(6); DMA_X(3); DMA_K(3);
        WAIT_BAR(6); DMA_X(4); DMA_K(4);
        WAIT_BAR(6); DMA_X(5); DMA_K(5); DMA_S(0);
        WAIT_BAR(10); DMA_X(6); DMA_K(6); DMA_S(1);
        WAIT_BAR(14);
        WAIT_BAR(4); DMA_S(2);
        asm volatile("" : "+v"(ep)); MKTAG();
        const unsigned fo = WS_FA + ((lane >> 5) * 8 + (h >> 1)) * 512 + (lane & 31) * 16;
        WAIT_BAR(4); DMA_S(3);
        WAIT_BAR(4); DMA_S(4); DMA_S(5);
        WAIT_BAR(8); DMA_S(6);
        unsigned dm0;
        {
            const int li = (t & 1) * 56 + (lane < 56 ? lane : 55), c0 = (h & 3) < 3 ? (h & 3) * 256 : 640;
            asm volatile("global_load_ubyte %0, %1, %2" : "=v"(dm0) : "v"(((j & 7) * 56 + (li >> 1)) * 896 + (li & 1) * 128 + c0), "s"((t & 2) ? wob : woa) : "memory"); }
        f32x16 cbv, pbv;
        {   const unsigned long long M = (0x5555ull << (r32 + 18)) >> (4 * hi);
            int nlo = tile > 0 ? ~(unsigned)M : ~0u, nhi = ~(unsigned)(M >> 32);
#pragma unroll
            for (int r = 0; r < 16; ++r) { const int cc = (r & 3) + 8 * (r >> 2);
                int e0 = nhi << (31 - cc); PIN(e0); cbv[r] = __uint_as_float((unsigned)(e0 >> 8) & 0xFF800000u);
                if (r < 8) pbv[r] = 0.f; else { int e1 = nlo << (31 - cc); PIN(e1); pbv[r] = __uint_as_float((unsigned)(e1 >> 8) & 0xFF800000u); } }
            PIN(cbv); PIN(pbv); }
        WAIT_BAR(9);
        WAIT_BAR(0);
        asm volatile("" :: "v"(dm0));
        u64 pa;
        if (t == 0) { wait_flags2(ws, fo, tag); lds_arrive(lds0 + L_CNT + 4, lane); } else lds_wait(lds0 + L_CNT + 4, 1);
        const char* kv0 = ws + WS_BR + BR_KV + (size_t)h * 262144 + t * 8192; const unsigned lr = lds0 + t * 8192;
#define KV4(p, LC) do { GLDSC(vl, p, lr, LC, 0); GLDSC(vl, p, lr, LC, 1024); GLDSC(vl, p, lr, LC, 2048); GLDSC(vl, p, lr, LC, 3072); } while (0)
        KV4(kv0, L_RING); KV4(kv0 + 4096, L_RING + 4096); KV4(kv0 + 32768, L_RING + 32768); KV4(kv0 + 36864, L_RING + 36864);
        WAIT_BAR(8);
        const int tt = 4 * j + (h >> 2), cg = h & 3, c = t;
        float glog = *(const LAS float*)(uintptr_t)(lds0 + L_GL + (h >> 2) * 128 + r32 * 4);
        const int col0 = cg < 3 ? cg * 128 : 320;
        const unsigned vwo = (lane >> 4) * 896 + (((lane & 15) ^ ((lane >> 4) << 2)) << 4), lc7 = lds0 + c * 7168;
        const char* wobp = wob + col0 * 2 + (size_t)(c * 7) * 3584; const char* woap = woa + col0 * 2 + (size_t)(c * 7) * 3584;
#define DWP(base, p, LC) do { if ((p) & 1) GLDS(vwo, (base) + (size_t)((p) - 1) * 3584, lc7, (LC) + 4096 + (p) * 1024 - 3584 - 4096, 3584); else GLDS(vwo, (base) + (size_t)(p) * 3584, lc7, (LC) + (p) * 1024, 0); } while (0)
#define DWQ(base, q, LC) do { const char* b_ = (base) + (size_t)(q) * 28 * 3584; DWP(b_, 0, LC); DWP(b_, 1, LC); DWP(b_, 2, LC); DWP(b_, 3, LC); DWP(b_, 4, LC); DWP(b_, 5, LC); DWP(b_, 6, LC); } while (0)
#define DWQ0(base, q) do { const char* b_ = (base) + (size_t)(q) * 28 * 3584; GLDS(vwo, b_, lc7, 0, 0); GLDS(vwo, b_ + 3584, lc7, 1024, 0); GLDS(vwo, b_ + 2 * 3584, lc7, 2048, 0); \
        GLDS(vwo, b_ + 3 * 3584, lc7, 3072, 0); DWP(b_, 4, 0); DWP(b_, 5, 0); DWP(b_, 6, 0); } while (0)
#define KVG(gr) do { const char* p_ = kv0 + (size_t)(gr) * 32768; KV4(p_, L_RING + ((gr) % 3) * 32768); KV4(p_ + 4096, L_RING + ((gr) % 3) * 32768 + 4096); } while (0)
        const int q4 = (lane & 15) >> 2, p4 = lane & 3, ch = (lane >> 4) & 1;
        const unsigned wl = lds0 + (4 * hi + q4) * 256 + (((c ^ q4) << 6) + ch * 32 + p4 * 8);
        unsigned wsa = wl + L_SA, wsk = wsa - (1024u & mhi); PIN(wsa); PIN(wsk);
        unsigned wz = wl, wzk = wl - (1024u & mhi); PIN(wz); PIN(wzk);
#define WFR(B, BK, off, k) wfrag((B) + (off) + ((k) >> 1) * 7168 + ((k) & 1) * 4096, ((k) == 7 ? (BK) : (B)) + (off) + ((k) >> 1) * 7168 + ((k) & 1) * 4096 + 2048)
        bf16x8 war[32];
#define WARQ(q, off) do { _Pragma("unroll") for (int k = 0; k < 8; ++k) war[8 * (q) + k] = WFR(wsa, wsk, off, k); } while (0)
        const unsigned bB = WS_BR + BR_SZ;
        WAIT_BAR(0);
        KVG(2); DWQ(woap, 0, L_SA);
        WAIT_BAR(0);
        if (tid == 256) set_flag(ws, WS_FB + (cb * 16 + j) * 8, tag);
        KVG(3); DWQ(woap, 1, L_SB); WARQ(0, 0);
        const unsigned fbo = WS_FB + (((16 * (lane % 3) + h) / 3) * 16 + ((lane & 4) && j > 0 ? j - 1 : j)) * 8;
        WAIT_BAR(0);
        KVG(4); DWQ(woap, 2, L_SA); WARQ(1, L_SB - L_SA);
        WAIT_BAR(0);
        asm volatile("global_load_dwordx2 %0, %1, %2 sc1" : "=v"(pa) : "v"(fbo), "s"(ws) : "memory");
        KVG(5); WARQ(2, 0);
        asm volatile("s_waitcnt vmcnt(8)" ::: "memory"); SBAR(); asm volatile("" : "+v"(pa) :: "memory");
        if (__builtin_expect(!__all(pa == tag), 0)) wait_flags_d(ws, fbo, tag);
        {   const char* qo = ws + bB + BR_Q + (size_t)(h * 64 + tile) * 2048; const char* ko = ws + bB + BR_KV + (size_t)(h * 64 + tile) * 4096;
            const unsigned lq2 = lds0 + t * 2048, lk2 = lds0 + t * 4096;
            GLDSC(vl, qo, lq2, L_SB + 20480, 0); GLDSC(vl, qo, lq2, L_SB + 20480, 1024);
            GLDSC(vl, ko, lk2, L_SB + 4096, 0); GLDSC(vl, ko, lk2, L_SB + 4096, 1024); GLDSC(vl, ko, lk2, L_SB + 4096, 2048); GLDSC(vl, ko, lk2, L_SB + 4096, 3072);
            if (t == 0) { const char* po = tile > 0 ? ko - 4096 : ko; GLDSC(vl, po, lds0, L_SB, 0); GLDSC(vl, po, lds0, L_SB, 1024); GLDSC(vl, po, lds0, L_SB, 3072); } }
        WAIT_BAR(0);
        {
            const unsigned bo = lds0 + L_SB + t * 4096 + vl, bq = lds0 + L_SB + 20480 + t * 2048 + vl;
            const bf16x8 bq0 = ldsr128(bq), bq1 = ldsr128(bq + 1024), bk0 = ldsr128(bo + 4096), bk1 = ldsr128(bo + 5120), bv0 = ldsr128(bo + 6144), bv1 = ldsr128(bo + 7168);
            const bf16x8 bp0 = ldsr128(bo), bp1 = ldsr128(bo + 1024), bpv = ldsr128(bo + 3072);
            KVG(6); DWQ(woap, 3, L_SA);
            __builtin_amdgcn_s_setprio(1);
            f32x16 sc = MFMA(bk0, bq0, cbv); sc = MFMA(bk1, bq1, sc);
            f32x16 sp = MFMA(bp0, bq0, pbv); sp = MFMA(bp1, bq1, sp);
            float m = max16(sc);
#pragma unroll
            for (int r = 8; r < 16; r += 2) m = __builtin_fmaxf(__builtin_fmaxf(m, sp[r]), sp[r + 1]);
            m = swap_max(m);
#pragma unroll
            for (int r = 0; r < 16; ++r) sc[r] = ex2(sc[r] - m);
#pragma unroll
            for (int r = 8; r < 16; ++r) sp[r] = ex2(sp[r] - m);
            u32x4 c0, c1, p0, p1; pack16(sc, c0, c1); pack16(sp, p0, p1);
            f32x16 ob = MFMA(bv0, asbf(c0), (f32x16){}); ob = MFMA(bv1, asbf(c1), ob); ob = MFMA(bpv, asbf(p1), ob);
            const float rl = __builtin_amdgcn_rcpf(from_hi(ob[12]));
#pragma unroll
            for (int i = 0; i < 16; ++i) ob[i] *= rl;
            u32x4 e0, e1; pack16(ob, e0, e1); fix_pad(e1, 0u, mhi);
            st16c(rs, e0, vl, bB + BR_O + (tile * 16 + h) * 2048); st16c(rs, e1, vl + 1024, bB + BR_O + (tile * 16 + h) * 2048);
            __builtin_amdgcn_s_setprio(0);
        }
        WAIT_BAR(2);
        KVG(7); DWQ(wobp, 0, L_SB); WARQ(3, 0);
        asm volatile("s_waitcnt vmcnt(15)" ::: "memory");
        if (lane == 0) set_flag(ws, WS_FOB + (tile * 16 + h) * 8, tag);
        const unsigned foo = WS_FOB + (tt * 16 + (lane & 15)) * 8;
        WAIT_BAR(1);
        DWQ(wobp, 1, L_SA);
        WAIT_BAR(0);
        asm volatile("global_load_dwordx2 %0, %1, %2 sc1" : "=v"(pa) : "v"(foo), "s"(ws) : "memory");
        DWQ0(wobp, 2);
        asm volatile("s_waitcnt vmcnt(7)" ::: "memory"); SBAR(); asm volatile("" : "+v"(pa) :: "memory");
        if (__builtin_expect(!__all(pa == tag), 0)) wait_flags_d(ws, foo, tag);
        {   const char* ofb = ws + bB + BR_O + (size_t)tt * 32768 + c * 8192; KV4(ofb, L_OF); KV4(ofb + 4096, L_OF + 4096); }
        unsigned oad = lds0 + L_OF + vl; PIN(oad);
        f32x16 accB = {};
        bf16x8 wf[4], of[4];
#define OPB(B, BK, off, kbase) do { \
        _Pragma("unroll") for (int i = 0; i < 4; ++i) { wf[i] = WFR(B, BK, off, i); of[i] = ldsr128(oad + ((kbase) + i) * 1024); } \
        _Pragma("unroll") for (int i = 0; i < 8; ++i) { SBAR(); accB = MFMA(wf[i & 3], of[i & 3], accB); SBAR(); \
            if (i < 4) { wf[i] = WFR(B, BK, off, i + 4); of[i] = ldsr128(oad + ((kbase) + i + 4) * 1024); } } SBAR(); } while (0)
        BAR_ONLY();
        WAIT_BAR(0);
        DWQ(wobp, 3, L_OA);
        OPB(wsa, wsk, L_SB - L_SA, 0); OPB(wsa, wsk, 0, 8); OPB(wz, wzk, 0, 16);
        if (tid == 256) *(LAS unsigned*)(uintptr_t)(lds0 + L_SCR) = 0u;
        WAIT_BAR(0);
        OPB(wz, wzk, L_OA, 24);
        float gate = __builtin_amdgcn_rcpf(1.0f + ex2(-1.4426950408889634f * glog)); const float g1 = 1.0f - gate;
#pragma unroll
        for (int i = 0; i < 16; ++i) accB[i] *= g1;
        unsigned oo = (tt * 32 + r32) * 896 + (col0 + 32 * c) * 2 + hi * 16;
        PIN(accB); PIN(gate); PIN(oo);
        if (t == 0) { wait_flags_d(ws, WS_FOA + (tt * 16 + (lane & 15)) * 8, tag); lds_arrive(lds0 + L_SCR, lane); } else lds_wait(lds0 + L_SCR, 1);
        {
            const char* ofa = ws + WS_BR + BR_O + (size_t)tt * 32768 + c * 4096; const unsigned lp = lds0 + c * 4096;
            GLDSC(vl, ofa, lp, 0, 0); GLDSC(vl, ofa, lp, 0, 1024); GLDSC(vl, ofa, lp, 0, 2048); GLDSC(vl, ofa, lp, 0, 3072);
            GLDSC(vl, ofa + 16384, lp, 16384, 0); GLDSC(vl, ofa + 16384, lp, 16384, 1024); GLDSC(vl, ofa + 16384, lp, 16384, 2048); GLDSC(vl, ofa + 16384, lp, 16384, 3072); }
        WAIT_BAR(4);
        f32x16 acc = {};
        {   unsigned ob_ = lds0 + vl; PIN(ob_);
            bf16x8 og[8];
#pragma unroll
            for (int i = 0; i < 8; ++i) og[i] = ldsr128(ob_ + i * 1024);
#pragma unroll
            for (int i = 0; i < 32; ++i) { if (i == 8) { WAIT_BAR(0); } SBAR(); acc = MFMA(war[i], og[i & 7], acc); SBAR(); if (i < 24) og[i & 7] = ldsr128(ob_ + (i + 8) * 1024); }
            SBAR();
        }
        if (bid == 0 && tid == 256) __hip_atomic_store((GAS u64*)(ws + WS_EPOCH), ep + 1, RLX_AGENT);
        if (cg < 3 || c >= 2) {
#pragma unroll
            for (int i = 0; i < 16; ++i) acc[i] = acc[i] * gate + accB[i];
            u32x4 s0_, s1_; pack16(acc, s0_, s1_);
            auto r0 = __builtin_amdgcn_permlane32_swap(s0_[0], s0_[2], false, false), r1 = __builtin_amdgcn_permlane32_swap(s0_[1], s0_[3], false, false);
            auto r2 = __builtin_amdgcn_permlane32_swap(s1_[0], s1_[2], false, false), r3 = __builtin_amdgcn_permlane32_swap(s1_[1], s1_[3], false, false);
            char* op = out + oo;
            *(GAS u32x4*)op = (u32x4){r0[0], r1[0], r0[1], r1[1]};
            *(GAS u32x4*)(op + 32) = (u32x4){r2[0], r3[0], r2[1], r3[1]};
        }
    }
    asm volatile(".Lfk_end:");
}

extern "C" void kernel_launch(void* const* d_in, const int* in_sizes, int n_in,
                              void* d_out, int out_size, void* d_ws, size_t ws_size,
                              hipStream_t stream) {
    (void)in_sizes; (void)n_in; (void)out_size; (void)ws_size;
    (void)hipFuncSetAttribute((const void*)fused_kernel, hipFuncAttributeMaxDynamicSharedMemorySize, LDS_BYTES);
    fused_kernel<<<256, 512, LDS_BYTES, stream>>>((const char*)d_in[0], (const char*)d_in[1], (const char*)d_in[2], (const char*)d_in[4],
                                                  (const char*)d_in[5], (const char*)d_in[6], (char*)d_ws, (char*)d_out);
}
```

```cpp
#include <hip/hip_runtime.h>
#include <hip/hip_bf16.h>
#include <stdint.h>

typedef short bf16x8 __attribute__((ext_vector_type(8)));
typedef short s16x4 __attribute__((ext_vector_type(4)));
typedef float f32x16 __attribute__((ext_vector_type(16)));
typedef float f32x4 __attribute__((ext_vector_type(4)));
typedef float f32x2 __attribute__((ext_vector_type(2)));
typedef unsigned u32x4 __attribute__((ext_vector_type(4)));
typedef unsigned u32x2 __attribute__((ext_vector_type(2)));
typedef __bf16 bf2_t __attribute__((ext_vector_type(2)));
typedef unsigned long long u64;
#define LAS __attribute__((address_space(3)))
#define GAS __attribute__((address_space(1)))
#define DI __device__ __forceinline__
#define MFMA(a, b, c) __builtin_amdgcn_mfma_f32_32x32x16_bf16((a), (b), (c), 0, 0, 0)
#define SBAR() __builtin_amdgcn_sched_barrier(0)
#define PIN(x) asm volatile("" : "+v"(x))
#define RLX_AGENT __ATOMIC_RELAXED, __HIP_MEMORY_SCOPE_AGENT

#define POLL_SLEEP 4
template <int N> struct IC { static constexpr int value = N; };
constexpr float QSCALE = 0.18898223650461362f * 1.4426950408889634f;

constexpr unsigned WS_EPOCH = 0;
constexpr unsigned WS_FA = 4096;
constexpr unsigned WS_FB = WS_FA + 2048;
constexpr unsigned WS_FOA = WS_FB + 2048;
constexpr unsigned WS_FOB = WS_FOA + 8192;
constexpr unsigned WS_BR = 65536;
constexpr unsigned BR_Q = 0, BR_KV = 2097152, BR_O = 6291456, BR_SZ = 8388608;
constexpr unsigned WS_TOTAL = WS_BR + 2 * BR_SZ;

constexpr unsigned L_XB = 0;
constexpr unsigned L_KW = 98304;
constexpr unsigned L_GT = 155648;
constexpr unsigned L_CNT = 159232;
constexpr unsigned L_GL = 159744;
constexpr unsigned L_Q = 139264;
constexpr unsigned L_RING = 0;
constexpr unsigned L_SA = 98304, L_SB = 126976;
constexpr unsigned L_OF = 65536;
constexpr unsigned L_OA = 32768;
constexpr unsigned L_SCR = 155648;
constexpr unsigned LDS_BYTES = 163840;

DI unsigned cvtpk(float lo, float hi) { f32x2 v = {lo, hi}; return __builtin_bit_cast(unsigned, __builtin_convertvector(v, bf2_t)); }
#define GLDS(voff, sbase, lds_s, LC, OFF) asm volatile("s_add_i32 m0, %2, %3\n\ts_nop 0\n\tglobal_load_lds_dwordx4 %0, %1 offset:%4" \
        :: "v"(voff), "s"(sbase), "s"(__builtin_amdgcn_readfirstlane(lds_s)), "n"((int)(LC)), "n"((int)(OFF)) : "memory", "scc")
#define GLDSC(voff, sbase, lds_s, LC, OFF) asm volatile("s_add_i32 m0, %2, %3\n\ts_nop 0\n\tglobal_load_lds_dwordx4 %0, %1 offset:%4 sc1" \
        :: "v"(voff), "s"(sbase), "s"(__builtin_amdgcn_readfirstlane(lds_s)), "n"((int)(LC)), "n"((int)(OFF)) : "memory", "scc")
#define GLDS_IF(sel, voff, sbase, lds_s, LC, OFF) asm volatile("s_cmp_lg_u32 %5, 0\n\ts_cbranch_scc1 1f\n\ts_add_i32 m0, %2, %3\n\ts_nop 0\n\tglobal_load_lds_dwordx4 %0, %1 offset:%4\n1:" \
        :: "v"(voff), "s"(sbase), "s"(__builtin_amdgcn_readfirstlane(lds_s)), "n"((int)(LC)), "n"((int)(OFF)), "s"(sel) : "memory", "scc")
#define GLDSC_IF(sel, voff, sbase, lds_s, LC, OFF) asm volatile("s_cmp_lg_u32 %5, 0\n\ts_cbranch_scc1 1f\n\ts_add_i32 m0, %2, %3\n\ts_nop 0\n\tglobal_load_lds_dwordx4 %0, %1 offset:%4 sc1\n1:" \
        :: "v"(voff), "s"(sbase), "s"(__builtin_amdgcn_readfirstlane(lds_s)), "n"((int)(LC)), "n"((int)(OFF)), "s"(sel) : "memory", "scc")
#define WAIT_BAR(N) asm volatile("s_waitcnt vmcnt(" #N ") lgkmcnt(0)\n\ts_barrier" ::: "memory")
#define BAR_ONLY() asm volatile("s_waitcnt lgkmcnt(0)\n\ts_barrier" ::: "memory")
DI bf16x8 ldsr128(unsigned a) { return *(const LAS bf16x8*)(uintptr_t)a; }
typedef short v4i16_t __attribute__((ext_vector_type(4)));
DI s16x4 ldstr(unsigned a) { return __builtin_bit_cast(s16x4, __builtin_amdgcn_ds_read_tr16_b64_v4i16((LAS v4i16_t*)(uintptr_t)a)); }
DI bf16x8 wfrag(unsigned a0, unsigned a1) { s16x4 lo = ldstr(a0), hi = ldstr(a1); return __builtin_shufflevector(lo, hi, 0, 1, 2, 3, 4, 5, 6, 7); }
DI bf16x8 asbf(u32x4 v) { return __builtin_bit_cast(bf16x8, v); }
DI float ex2(float v) { return __builtin_amdgcn_exp2f(v); }
DI float swap_max(float m) {
    auto rr = __builtin_amdgcn_permlane32_swap(__float_as_uint(m), __float_as_uint(m), false, false);
    return __builtin_fmaxf(__uint_as_float(rr[0]), __uint_as_float(rr[1]));
}
DI float from_hi(float v) {
    auto rr = __builtin_amdgcn_permlane32_swap(__float_as_uint(v), __float_as_uint(v), false, false);
    return __uint_as_float(rr[1]);
}
DI void pack16(const f32x16& a, u32x4& s0, u32x4& s1) {
    s0 = (u32x4){cvtpk(a[0], a[1]), cvtpk(a[2], a[3]), cvtpk(a[4], a[5]), cvtpk(a[6], a[7])};
    s1 = (u32x4){cvtpk(a[8], a[9]), cvtpk(a[10], a[11]), cvtpk(a[12], a[13]), cvtpk(a[14], a[15])};
}
DI float max16(const f32x16& s) {
    float m = __builtin_fmaxf(s[0], s[1]);
#pragma unroll
    for (int r = 2; r < 16; r += 2) m = __builtin_fmaxf(__builtin_fmaxf(m, s[r]), s[r + 1]);
    return m;
}
typedef __amdgpu_buffer_rsrc_t rsrc_t;
DI void st16c(rsrc_t r, u32x4 v, unsigned voff, unsigned soff) { __builtin_amdgcn_raw_buffer_store_b128(v, r, voff, soff, 16); }
DI bf16x8 ld16c(rsrc_t r, unsigned voff, unsigned soff) { return asbf(__builtin_amdgcn_raw_buffer_load_b128(r, voff, soff, 16)); }
DI void wait_flags_d(const char* ws, unsigned off, u64 tag) {
#pragma unroll 1
    for (int it = 0; it < 40000; ++it) {
        u64 a;
        asm volatile("global_load_dwordx2 %0, %1, %2 sc1\n\ts_waitcnt vmcnt(0)" : "=&v"(a) : "v"(off), "s"(ws) : "memory");
        if (__all(a == tag)) break;
        __builtin_amdgcn_s_sleep(POLL_SLEEP);
    }
}
DI void set_flag(char* ws, unsigned off, u64 tag) { __hip_atomic_store((GAS u64*)(ws + off), tag, RLX_AGENT); }

#define STAMP(i) do {} while (0)
#define STAMPF(i) do {} while (0)
#define STARGS
#define STPASS

DI void fix_v(u32x4& s0, u32x4& s1, unsigned m28) {
    unsigned keep = (m28 & 1u) - 1u, ones = m28 & 0x3F803F80u; PIN(keep); PIN(ones);
#pragma unroll
    for (int i = 0; i < 4; ++i) { s0[i] = (s0[i] & keep) | ones; s1[i] = (s1[i] & keep) | ones; }
}
DI void lds_arrive(unsigned a, int lane) { if (lane == 0) __hip_atomic_fetch_add((LAS unsigned*)(uintptr_t)a, 1u, __ATOMIC_RELAXED, __HIP_MEMORY_SCOPE_WORKGROUP); }
DI void lds_wait(unsigned a, unsigned n) {
#pragma unroll 1
    for (int it = 0; it < 2000000; ++it) {
        if (__builtin_amdgcn_readfirstlane(__hip_atomic_load((LAS unsigned*)(uintptr_t)a, __ATOMIC_RELAXED, __HIP_MEMORY_SCOPE_WORKGROUP)) >= n) break;
        __builtin_amdgcn_s_sleep(1);
    }
    asm volatile("" ::: "memory");
}
DI void fix_pad(u32x4& s1, unsigned pad, unsigned mhi) { s1[2] = (s1[2] & ~mhi) | (pad & mhi); s1[3] &= ~mhi; }
template <bool V>
DI void pack_unit(f32x16 a, int u, unsigned m28, unsigned mhi, u32x4& s0, u32x4& s1) {
    if (!V) {
        const bool isq = u < 16;
        const float sc = isq ? QSCALE : 1.0f;
#pragma unroll
        for (int i = 0; i < 16; ++i) a[i] *= sc;
        pack16(a, s0, s1);
        fix_pad(s1, isq ? 0u : 0x00003F80u, mhi);
    } else { pack16(a, s0, s1); fix_v(s0, s1, m28); }
}
#define MKTAG() do { u64 z = ((u64)__builtin_amdgcn_readfirstlane((unsigned)(ep >> 32)) << 32) | (unsigned)__builtin_amdgcn_readfirstlane((unsigned)ep); \
        z += 0x9E3779B97F4A7C15ull; z = (z ^ (z >> 30)) * 0xBF58476D1CE4E5B9ull; z = (z ^ (z >> 27)) * 0x94D049BB133111EBull; tag = z ^ (z >> 31); } while (0)
#define XB(i) (L_XB + (i) * 16384)
#define KW(i) (L_KW + (i) * 8192)
#define MM(VV, acc, wf, xfr) do { if (VV) acc = MFMA(xfr, wf, acc); else acc = MFMA(wf, xfr, acc); } while (0)
__device__ constexpr unsigned so_of(int c) { return c == 0 ? KW(0) : (c == 6 ? KW(2) : XB(c)); }
__device__ constexpr unsigned xo_of(int c) { return c == 6 ? XB(0) : XB(c); }

template <bool VK>
DI void stage1(bf16x8 (&xf)[28], const unsigned (&xa)[4], const unsigned (&xh)[4], unsigned ka0, unsigned ka1, rsrc_t rs, int h, int tile, int lane, unsigned m28, unsigned mhi,
               unsigned gv0, unsigned gv1, unsigned dmc, unsigned dmi, u64 ep, u64& tag, unsigned lds0 STARGS) {
    f32x16 acc0 = {}, acc1 = {};
    bf16x8 w0[2], w1[2];
#define XR(s) xf[s] = ldsr128(xo_of((s) >> 2) >= 65536 ? xh[(s) & 3] + (xo_of((s) >> 2) - 65536) : xa[(s) & 3] + xo_of((s) >> 2))
#define KF(base, s) wfrag((base) + (s) * 2048, (base) + (s) * 2048 + 512)
    XR(0); w0[0] = KF(ka0, 0); w1[0] = KF(ka1, 0); XR(1); w0[1] = KF(ka0, 1); w1[1] = KF(ka1, 1);
#pragma unroll
    for (int s = 0; s < 28; ++s) {
        if ((s & 3) == 2 && s < 26) { BAR_ONLY(); if (s == 10) STAMP(3); if (s == 22) STAMP(4); }
        if (s == 20) {
            asm volatile("s_waitcnt vmcnt(0)" : "+v"(gv0), "+v"(gv1), "+v"(ep) : "v"(dmc), "v"(dmi) : "memory");
            const int tid = threadIdx.x;
            { u32x2 e = {gv0, 0u}; *(LAS u32x2*)(uintptr_t)(lds0 + L_GT + tid * 8) = e; }
            if (tid < 192) { u32x2 e = {gv1, 0u}; *(LAS u32x2*)(uintptr_t)(lds0 + L_GT + 2048 + tid * 8) = e; }
            if (tid == 0) *(LAS u32x2*)(uintptr_t)(lds0 + L_CNT) = (u32x2){0u, 0u};
            MKTAG();
        }
        SBAR();
        MM(VK, acc0, w0[s & 1], xf[s]); SBAR();
        if (s + 2 < 28) { XR(s + 2); w0[s & 1] = KF(ka0, s + 2); }
        SBAR();
        MM(VK, acc1, w1[s & 1], xf[s]); SBAR();
        if (s + 2 < 28) w1[s & 1] = KF(ka1, s + 2);
    }
    SBAR();
    u32x4 s0, s1;
    pack16(acc0, s0, s1); if (!VK) fix_pad(s1, 0x00003F80u, mhi); else fix_v(s0, s1, m28);
    const unsigned off = WS_BR + BR_KV + ((2 * h & 15) * 64 + tile) * 4096 + (VK ? 2048 : 0);
    st16c(rs, s0, lane * 16, off); st16c(rs, s1, lane * 16 + 1024, off);
    pack16(acc1, s0, s1); if (!VK) fix_pad(s1, 0x00003F80u, mhi); else fix_v(s0, s1, m28);
    st16c(rs, s0, lane * 16, off + 262144); st16c(rs, s1, lane * 16 + 1024, off + 262144);
}
template <bool V01, bool V2>
DI void stage2(const bf16x8 (&xf)[28], unsigned ql, unsigned qh, unsigned gl, bool spec, unsigned wlo, unsigned whi, char* ws, rsrc_t rs, unsigned lds0,
               int h, int cb, int j, int t, int lane, u64 tag, u32x4 (&bp)[6], unsigned m28, unsigned mhi STARGS) {
    f32x16 aq = {}, acc0 = {}, acc1 = {}, acc2 = {};
    bf16x8 wq[2], w0[2], w1[2], w2[2];
    const unsigned gstride = spec ? 32u : 256u;
    bool sent = false;
#define SADDR(lo, hi_, c) (so_of(c) >= 65536 ? (hi_) + (so_of(c) - 65536) : (lo) + so_of(c))
#define WF(s, u) wfrag(SADDR(wlo, whi, (s) >> 2) + ((s) & 3) * 3072 + (u) * 56, SADDR(wlo, whi, (s) >> 2) + ((s) & 3) * 3072 + 768 + (u) * 56)
#define QR(s) do { const unsigned a_ = spec ? gl + 128 * (s) : SADDR(ql, qh, (s) >> 2) + ((s) & 3) * 1024; wq[(s) & 1] = wfrag(a_, a_ + gstride); } while (0)
    QR(0); w0[0] = WF(0, 0); w1[0] = WF(0, 1); w2[0] = WF(0, 2); QR(1); w0[1] = WF(1, 0); w1[1] = WF(1, 1); w2[1] = WF(1, 2);
#pragma unroll
    for (int s = 0; s < 28; ++s) {
        if (s == 6 || s == 10 || s == 14 || s == 18) { BAR_ONLY(); if (s == 10) STAMP(6); if (s == 18) STAMP(7); }
        if (s >= 7 && s <= 15) {
            if (s == 15) asm volatile("s_waitcnt vmcnt(0)" ::: "memory");
            if (!sent && (s == 15 || (__builtin_amdgcn_s_getreg(7 | (3 << 11)) & 15) == 0)) {
                sent = true; asm volatile("" ::: "memory");
                if (lane == 0) { if (__hip_atomic_fetch_add((LAS unsigned*)(uintptr_t)(lds0 + L_CNT), 1u, __ATOMIC_RELAXED, __HIP_MEMORY_SCOPE_WORKGROUP) == 3u) set_flag(ws, WS_FA + (h * 16 + j) * 8, tag); }
                STAMP(8);
            }
        }
        SBAR();
        aq = MFMA(wq[s & 1], xf[s], aq); SBAR();
        if (s + 2 < 28) QR(s + 2);
        SBAR();
        MM(V01, acc0, w0[s & 1], xf[s]); SBAR();
        if (s + 2 < 28) w0[s & 1] = WF(s + 2, 0);
        SBAR();
        MM(V01, acc1, w1[s & 1], xf[s]); SBAR();
        if (s + 2 < 28) w1[s & 1] = WF(s + 2, 1);
        SBAR();
        MM(V2, acc2, w2[s & 1], xf[s]); SBAR();
        if (s + 2 < 28) w2[s & 1] = WF(s + 2, 2);
    }
    SBAR();
    {
        if (lane >> 5) *(LAS float*)(uintptr_t)(lds0 + L_GL + t * 128 + (lane & 31) * 4) = aq[12];
#pragma unroll
        for (int i = 0; i < 16; ++i) aq[i] *= QSCALE;
        u32x4 s0, s1; pack16(aq, s0, s1);
        fix_pad(s1, 0u, mhi);
        *(LAS u32x4*)(uintptr_t)(lds0 + L_Q + t * 2048 + lane * 16) = s0; *(LAS u32x4*)(uintptr_t)(lds0 + L_Q + t * 2048 + 1024 + lane * 16) = s1;
    }
    pack_unit<V01>(acc0, 3 * cb, m28, mhi, bp[0], bp[1]); pack_unit<V01>(acc1, 3 * cb + 1, m28, mhi, bp[2], bp[3]); pack_unit<V2>(acc2, 3 * cb + 2, m28, mhi, bp[4], bp[5]);
}

__global__ __launch_bounds__(512, 2) void fused_kernel(const char* __restrict__ x, const char* __restrict__ wa, const char* __restrict__ woa,
                                                       const char* __restrict__ wb, const char* __restrict__ wob, const char* __restrict__ gw,
                                                       char* ws, char* __restrict__ out) {
    extern __shared__ __attribute__((aligned(16))) char smem[];
    const unsigned lds0 = (unsigned)(uintptr_t)(LAS char*)smem;
    const int tid = threadIdx.x, lane = tid & 63, w = __builtin_amdgcn_readfirstlane(tid >> 6), t = w & 3, g = w >> 2;
    const int bid = blockIdx.x, xg = bid & 7, idx = bid >> 3;
    const int h = (xg >> 1) * 4 + (idx & 3), j = (xg & 1) * 8 + (idx >> 2);
    const int r32 = lane & 31, hi = lane >> 5;
    const rsrc_t rs = __builtin_amdgcn_make_buffer_rsrc(ws, 0, WS_TOTAL, 0x00020000);
    const unsigned vl = lane * 16;
    asm volatile("" :: "s"(x), "s"(wa), "s"(woa), "s"(wb), "s"(wob), "s"(gw), "s"(ws), "s"(out));
    u64 pc; unsigned len;
    asm volatile("s_getpc_b64 %0\n.Lfk_ref:\n\ts_mov_b32 %1, .Lfk_end-.Lfk_ref" : "=s"(pc), "=s"(len));
    STAMPF(0);
    const int cb = (int)((0xCFEDBA8967542103ull >> (4 * h)) & 15);
    const int tile = 4 * j + t;
    const int rot = (j + 2 * (h & 3)) % 7;
#define CK(p) ((p) + rot >= 7 ? (p) + rot - 7 : (p) + rot)
    const unsigned vxe = (lane >> 3) * 896 + (((lane & 7) ^ (lane >> 4)) << 4), lx = lds0 + t * 4096;
    const char* xb = x + (size_t)(j * 128 + t * 32) * 896;
    unsigned dmi;
    {
        const unsigned co = (unsigned)((idx * 8 + w) * 2 + (lane & 1)) * 128u;
        asm volatile("global_load_ubyte %0, %1, %2" : "=v"(dmi) : "v"(co < len ? co : 0u), "s"(pc) : "memory"); }
    {
        const char* p_ = xb + rot * 128;
        GLDS_IF(g, vxe, p_, lx, XB(0), 0); GLDS_IF(g, (vxe ^ 64u) + 7168, p_, lx, XB(0) + 1024, 0); GLDS_IF(g, vxe + 14336, p_, lx, XB(0) + 2048, 0); GLDS_IF(g, (vxe ^ 64u) + 21504, p_, lx, XB(0) + 3072, 0); }
    u64 ep, tag;
    unsigned m28 = r32 == 28 ? ~0u : (r32 > 28 ? 1u : 0u), mhi = hi ? ~0u : 0u; PIN(m28); PIN(mhi);
    if (g == 0) {
        unsigned gv0, gv1, dmc;
        {
            const int pl = lane & 15;
            const char* tp = pl < 9 ? ws + (size_t)pl * 2097152 + 32768 : (pl == 9 ? (const char*)out : (pl == 10 ? woa : (pl == 11 ? wob : wb)));
            asm volatile("global_load_ubyte %0, %1, off sc1" : "=v"(dmc) : "v"(tp) : "memory");
        }
        asm volatile("global_load_ushort %0, %1, %2" : "=v"(gv0) : "v"((CK(t) * 64 + lane) * 2), "s"(gw) : "memory");
        asm volatile("global_load_ushort %0, %1, %2" : "=v"(gv1) : "v"((CK(t < 3 ? 4 + t : 6) * 64 + lane) * 2), "s"(gw) : "memory");
        asm volatile("global_load_dwordx2 %0, %1, %2 sc1" : "=v"(ep) : "v"(0u), "s"(ws) : "memory");
        const int fr = (r32 >> 1) & 7;
        unsigned xa[4], xh[4];
#pragma unroll
        for (int ks = 0; ks < 4; ++ks) { xa[ks] = lds0 + (4 * t + (r32 >> 3)) * 1024 + (r32 & 7) * 128 + (((2 * ks + hi) ^ fr) << 4); xh[ks] = xa[ks] + 65536; PIN(xa[ks]); PIN(xh[ks]); }
        const int q4 = (lane & 15) >> 2, p4 = lane & 3, ch = (lane >> 4) & 1;
        const unsigned o0 = ch * 32 + p4 * 8, o1 = o0 + 56, sw = (q4 >> 1) << 6;
        unsigned ka0 = lds0 + L_KW + (8 * hi + q4) * 128 + (o0 ^ sw), ka1 = lds0 + L_KW + (8 * hi + q4) * 128 + (o1 ^ sw); PIN(ka0); PIN(ka1);
        unsigned ql = lds0 + (8 * hi + q4) * 64 + ch * 32 + p4 * 8 + (h & 1) * 8, qh = ql + 65536; PIN(ql); PIN(qh);
        unsigned wlo = lds0 + 4096 + (8 * hi + q4) * 192 + ch * 32 + p4 * 8 + (cb & 1) * 8, whi = wlo + 65536; PIN(wlo); PIN(whi);
        const bool spec = ch == 1 && p4 == 3;
        const unsigned gl = lds0 + L_GT + 64 * hi + 8 * q4;
        bf16x8 xf[28]; u32x4 bp[6];
        STAMP(1);
        WAIT_BAR(4); STAMP(2);
        if (h < 8) stage1<false>(xf, xa, xh, ka0, ka1, rs, h, tile, lane, m28, mhi, gv0, gv1, dmc, dmi, ep, tag, lds0 STPASS); else stage1<true>(xf, xa, xh, ka0, ka1, rs, h, tile, lane, m28, mhi, gv0, gv1, dmc, dmi, ep, tag, lds0 STPASS);
        BAR_ONLY(); STAMP(5);
        if (cb <= 9) stage2<false, false>(xf, ql, qh, gl, spec, wlo, whi, ws, rs, lds0, h, cb, j, t, lane, tag, bp, m28, mhi STPASS);
        else if (cb == 10) stage2<false, true>(xf, ql, qh, gl, spec, wlo, whi, ws, rs, lds0, h, cb, j, t, lane, tag, bp, m28, mhi STPASS);
        else stage2<true, true>(xf, ql, qh, gl, spec, wlo, whi, ws, rs, lds0, h, cb, j, t, lane, tag, bp, m28, mhi STPASS);
        STAMP(9);
        BAR_ONLY();
#pragma unroll
        for (int i = 0; i < 3; ++i) { const int u = 3 * cb + i;
            const unsigned off = WS_BR + BR_SZ + (u < 16 ? BR_Q + (u * 64 + tile) * 2048 : BR_KV + ((u & 15) * 64 + tile) * 4096 + (u >= 32 ? 2048 : 0));
            st16c(rs, bp[2 * i], vl, off); st16c(rs, bp[2 * i + 1], vl + 1024, off); }
        STAMP(10);
        const int qp = t & 1, kh = t >> 1;
        const unsigned qad = lds0 + L_Q + qp * 4096 + kh * 2048 + vl, qbd = qad ^ 2048u;
        const bf16x8 qa0 = ldsr128(qad), qb0 = ldsr128(qbd);
        u32x4 qa1 = __builtin_bit_cast(u32x4, ldsr128(qad + 1024)), qb1 = __builtin_bit_cast(u32x4, ldsr128(qbd + 1024));
        unsigned ac = lds0 + L_RING + kh * 4096 + vl, an = ac + 32768, af = ac + 65536;
        f32x16 oa = {}, ob = {}, sA, sB; u32x4 pA0 = {}, pA1 = {}, pB0 = {}, pB1 = {};
        bf16x8 k0 = ldsr128(ac), k1 = ldsr128(ac + 1024), v0 = {}, v1 = {}, kn0, kn1;
        float mra, mrb;
        {
            sA = MFMA(k0, qa0, (f32x16){}); sB = MFMA(k0, qb0, (f32x16){});
            f32x16 ta = MFMA(k1, asbf(qa1), sA); sB = MFMA(k1, asbf(qb1), sB);
            const unsigned na = cvtpk(-swap_max(max16(ta)), 0.f), nb = cvtpk(-swap_max(max16(sB)), 0.f);
            mra = -__uint_as_float(na << 16); mrb = -__uint_as_float(nb << 16);
            qa1[2] = (qa1[2] & ~mhi) | (na & mhi); qb1[2] = (qb1[2] & ~mhi) | (nb & mhi);
            sA = MFMA(k1, asbf(qa1), sA);
        }
#define GRP(S, b, SP, bp, PP, jp) do { PP[jp] = cvtpk(SP[bp], SP[bp + 1]); PP[jp + 1] = cvtpk(SP[bp + 2], SP[bp + 3]); SBAR(); \
        S[b] = ex2(S[b]); S[b + 1] = ex2(S[b + 1]); S[b + 2] = ex2(S[b + 2]); S[b + 3] = ex2(S[b + 3]); PIN(PP); PIN(S); SBAR(); } while (0)
#define STEP2(KC0, KC1, KN0, KN1, VP1, VC1, ka, va) do { \
        KN0 = ldsr128(ka); ob = MFMA(v0, asbf(pB0), ob); SBAR(); GRP(sA, 0, sB, 12, pB1, 2); KN1 = ldsr128((ka) + 1024); \
        sB = MFMA(KC0, qb0, (f32x16){}); SBAR(); VC1 = ldsr128((va) + 3072); GRP(sA, 4, sA, 0, pA0, 0); v0 = ldsr128((va) + 2048); \
        sB = MFMA(KC1, asbf(qb1), sB); SBAR(); GRP(sA, 8, sA, 4, pA0, 2); \
        ob = MFMA(VP1, asbf(pB1), ob); SBAR(); GRP(sA, 12, sA, 8, pA1, 0); \
        oa = MFMA(v0, asbf(pA0), oa); SBAR(); GRP(sB, 0, sA, 12, pA1, 2); \
        sA = MFMA(KN0, qa0, (f32x16){}); SBAR(); GRP(sB, 4, sB, 0, pB0, 0); \
        sA = MFMA(KN1, asbf(qa1), sA); SBAR(); GRP(sB, 8, sB, 4, pB0, 2); \
        oa = MFMA(VC1, asbf(pA1), oa); SBAR(); GRP(sB, 12, sB, 8, pB1, 0); \
    } while (0)
        sB = (f32x16){};
        bf16x8 v1n;
#pragma unroll 1
        for (int gr = 0; gr < 8; ++gr) {
            STEP2(k0, k1, kn0, kn1, v1, v1n, ac + 8192, ac);
            STEP2(kn0, kn1, k0, k1, v1n, v1, ac + 16384, ac + 8192);
            if (gr == 0) asm volatile("s_barrier" ::: "memory"); else asm volatile("s_waitcnt vmcnt(0)\n\ts_barrier" ::: "memory");
            STAMP(11 + gr);
            SBAR();
            STEP2(k0, k1, kn0, kn1, v1, v1n, ac + 24576, ac + 16384);
            STEP2(kn0, kn1, k0, k1, v1n, v1, an, ac + 24576);
            const unsigned tmp = ac; ac = an; an = af; af = tmp;
        }
        pB1[2] = cvtpk(sB[12], sB[13]); pB1[3] = cvtpk(sB[14], sB[15]);
        ob = MFMA(v0, asbf(pB0), ob); ob = MFMA(v1, asbf(pB1), ob);
        STAMP(20);
#define SAFE_LIMIT 1.0e18f
        if (__builtin_expect(__any(hi && !(oa[12] < SAFE_LIMIT && ob[12] < SAFE_LIMIT)), 0)) {
            qa1[2] &= ~mhi; qb1[2] &= ~mhi;
            mra = -__builtin_inff(); mrb = mra; oa = (f32x16){}; ob = (f32x16){};
            const unsigned kb = WS_BR + BR_KV + h * 262144 + kh * 4096;
#pragma unroll 1
            for (int i = 0; i < 32; ++i) {
                const unsigned so = kb + i * 8192;
                const bf16x8 a0 = ld16c(rs, vl, so), a1 = ld16c(rs, vl + 1024, so), b0 = ld16c(rs, vl + 2048, so), b1 = ld16c(rs, vl + 3072, so);
#define ONLINE(Q0, Q1, O, M) do { f32x16 s_ = MFMA(a0, Q0, (f32x16){}); s_ = MFMA(a1, asbf(Q1), s_); \
                const float mn = __builtin_fmaxf(M, swap_max(max16(s_))), al = ex2(M - mn); M = mn; \
                _Pragma("unroll") for (int r = 0; r < 16; ++r) { s_[r] = ex2(s_[r] - mn); O[r] *= al; } \
                u32x4 e0, e1; pack16(s_, e0, e1); O = MFMA(b0, asbf(e0), O); O = MFMA(b1, asbf(e1), O); } while (0)
                ONLINE(qa0, qa1, oa, mra); ONLINE(qb0, qb1, ob, mrb);
            }
        }
        {   const unsigned cw_ = lds0 + L_OA + t * 4096 + vl;
#define PUTP(O, M) do { O[13] = __uint_as_float((__float_as_uint(O[13]) & ~mhi) | (__float_as_uint(M) & mhi)); \
            _Pragma("unroll") for (int i = 0; i < 4; ++i) *(LAS f32x4*)(uintptr_t)(cw_ + i * 1024) = (f32x4){O[4 * i], O[4 * i + 1], O[4 * i + 2], O[4 * i + 3]}; } while (0)
            PUTP(ob, mrb);
        }
        STAMP(29);
        BAR_ONLY();
        STAMP(30);
        {   const unsigned cbs = lds0 + L_OA + (t ^ 2) * 4096 + vl;
            f32x16 o1;
#pragma unroll
            for (int i = 3; i >= 0; --i) { const f32x4 e = *(const LAS f32x4*)(uintptr_t)(cbs + i * 1024); o1[4 * i] = e[0]; o1[4 * i + 1] = e[1]; o1[4 * i + 2] = e[2]; o1[4 * i + 3] = e[3]; }
            const float m1 = from_hi(o1[13]);
            const float mm = __builtin_fmaxf(mra, m1), a0 = ex2(mra - mm), a1 = ex2(m1 - mm);
            const float rl = __builtin_amdgcn_rcpf(from_hi(oa[12] * a0 + o1[12] * a1)), f0 = a0 * rl, f1 = a1 * rl;
            f32x16 v;
#pragma unroll
            for (int i = 0; i < 16; ++i) v[i] = oa[i] * f0 + o1[i] * f1;
            u32x4 e0, e1; pack16(v, e0, e1);
            fix_pad(e1, 0u, mhi);
            const int ot = 4 * j + 2 * qp + kh;
            const unsigned so = WS_BR + BR_O + (ot * 16 + h) * 2048;
            st16c(rs, e0, vl, so); st16c(rs, e1, vl + 1024, so);
            STAMP(21);
            asm volatile("s_waitcnt lgkmcnt(0)\n\ts_barrier\n\ts_waitcnt vmcnt(0)" ::: "memory");
            if (lane == 0) set_flag(ws, WS_FOA + (ot * 16 + h) * 8, tag);
            STAMP(22);
        }
    } else {
        const unsigned vx[4] = {vxe, (vxe ^ 64u) + 7168, vxe + 14336, (vxe ^ 64u) + 21504};
        const unsigned kc = (lane & 7) ^ (((lane >> 4) & 1) << 2);
        const unsigned vk0 = (lane >> 3) * 2688 + (kc > 6 ? 6 : kc) * 16, vk1 = vk0 + 8 * 2688;
        const char* wkb = wa + 896 + 112 * h + (size_t)(16 * t) * 2688;
        const unsigned lk = lds0 + t * 2048;
#define DMA_K(ck) do { const char* p_ = wkb + (size_t)CK(ck) * 64 * 2688; GLDS(vk0, p_, lk, KW(ck), 0); GLDS(vk1, p_, lk, KW(ck) + 1024, 0); } while (0)
#define DMA_X(cx) do { const char* p_ = xb + CK(cx) * 128; GLDS(vx[0], p_, lx, xo_of(cx), 0); GLDS(vx[1], p_, lx, xo_of(cx) + 1024, 0); \
        GLDS(vx[2], p_, lx, xo_of(cx) + 2048, 0); GLDS(vx[3], p_, lx, xo_of(cx) + 3072, 0); } while (0)
        asm volatile("global_load_dwordx2 %0, %1, %2 sc1" : "=v"(ep) : "v"(0u), "s"(ws) : "memory");
        DMA_K(0); DMA_X(1); DMA_K(1);
        unsigned vw[3];
#pragma unroll
        for (int n = 0; n < 3; ++n) { const unsigned L = 64 * n + lane, row = (L * 43691u) >> 19, cc = L - row * 12; vw[n] = row * 2688 + (cc > 10 ? 10 : cc) * 16; }
        const unsigned vq = (lane >> 2) * 2688 + (lane & 3) * 16;
        const char* wqb = wa + (56 * h - 8 * (h & 1)) + (size_t)(16 * t) * 2688;
        const char* wbb = wb + (168 * cb - 8 * (cb & 1)) + (size_t)(16 * t) * 2688;
        const unsigned lq = lds0 + t * 1024, lw = lds0 + 4096 + t * 3072;
#define DMA_S(ck) do { GLDS(vq, wqb + (size_t)CK(ck) * 64 * 2688, lq, so_of(ck), 0); const char* p_ = wbb + (size_t)CK(ck) * 64 * 2688; \
        GLDS(vw[0], p_, lw, so_of(ck), 0); GLDS(vw[1], p_, lw, so_of(ck) + 1024, 0); GLDS(vw[2], p_, lw, so_of(ck) + 2048, 0); } while (0)
        STAMP(1);
        WAIT_BAR(6); asm volatile("" :: "v"(dmi)); STAMP(2); DMA_X(2); DMA_K(2);
        WAIT_BAR(6); DMA_X(3); DMA_K(3);
        WAIT_BAR(6); DMA_X(4); DMA_K(4);
        WAIT_BAR(6); STAMP(3); DMA_X(5); DMA_K(5); DMA_S(0);
        WAIT_BAR(10); DMA_X(6); DMA_K(6); DMA_S(1);
        WAIT_BAR(14);
        WAIT_BAR(4); STAMP(4); DMA_S(2);
        asm volatile("" : "+v"(ep)); MKTAG();
        const unsigned fo = WS_FA + ((((lane >> 4) & 1) * 8 + (h >> 1)) * 16 + (lane & 15)) * 8;
        WAIT_BAR(4); STAMP(5); DMA_S(3);
        WAIT_BAR(4); DMA_S(4); DMA_S(5);
        WAIT_BAR(8); STAMP(6); DMA_S(6);
        unsigned dm0;
        {
            const int li = (t & 1) * 56 + (lane < 56 ? lane : 55), c0 = (h & 3) < 3 ? (h & 3) * 256 : 640;
            asm volatile("global_load_ubyte %0, %1, %2" : "=v"(dm0) : "v"(((j & 7) * 56 + (li >> 1)) * 896 + (li & 1) * 128 + c0), "s"((t & 2) ? wob : woa) : "memory"); }
        f32x16 cbv, pbv;
        {   const unsigned long long M = (0x5555ull << (r32 + 18)) >> (4 * hi);
            int nlo = tile > 0 ? ~(unsigned)M : ~0u, nhi = ~(unsigned)(M >> 32);
#pragma unroll
            for (int r = 0; r < 16; ++r) { const int cc = (r & 3) + 8 * (r >> 2);
                int e0 = nhi << (31 - cc); PIN(e0); cbv[r] = __uint_as_float((unsigned)(e0 >> 8) & 0xFF800000u);
                if (r < 8) pbv[r] = 0.f; else { int e1 = nlo << (31 - cc); PIN(e1); pbv[r] = __uint_as_float((unsigned)(e1 >> 8) & 0xFF800000u); } }
            PIN(cbv); PIN(pbv); }
        WAIT_BAR(9);
        WAIT_BAR(0);
        asm volatile("" :: "v"(dm0));
        STAMP(7);
        u64 pa;
        if (t == 0) { wait_flags_d(ws, fo, tag); lds_arrive(lds0 + L_CNT + 4, lane); } else lds_wait(lds0 + L_CNT + 4, 1);
        STAMP(8);
        const char* kv0 = ws + WS_BR + BR_KV + (size_t)h * 262144 + t * 8192; const unsigned lr = lds0 + t * 8192;
#define KV4(p, LC) do { GLDSC(vl, p, lr, LC, 0); GLDSC(vl, p, lr, LC, 1024); GLDSC(vl, p, lr, LC, 2048); GLDSC(vl, p, lr, LC, 3072); } while (0)
        KV4(kv0, L_RING); KV4(kv0 + 4096, L_RING + 4096); KV4(kv0 + 32768, L_RING + 32768); KV4(kv0 + 36864, L_RING + 36864);
        STAMP(9);
        WAIT_BAR(8);
        STAMP(10);
        const int tt = 4 * j + (h >> 2), cg = h & 3, c = t;
        float glog = *(const LAS float*)(uintptr_t)(lds0 + L_GL + (h >> 2) * 128 + r32 * 4);
        const int col0 = cg < 3 ? cg * 128 : 320;
        const unsigned vwo = (lane >> 4) * 896 + (((lane & 15) ^ ((lane >> 4) << 2)) << 4), lc7 = lds0 + c * 7168;
        const char* wobp = wob + col0 * 2 + (size_t)(c * 7) * 3584; const char* woap = woa + col0 * 2 + (size_t)(c * 7) * 3584;
#define DWP(base, p, LC) do { if ((p) & 1) GLDS(vwo, (base) + (size_t)((p) - 1) * 3584, lc7, (LC) + 4096 + (p) * 1024 - 3584 - 4096, 3584); else GLDS(vwo, (base) + (size_t)(p) * 3584, lc7, (LC) + (p) * 1024, 0); } while (0)
#define DWQ(base, q, LC) do { const char* b_ = (base) + (size_t)(q) * 28 * 3584; DWP(b_, 0, LC); DWP(b_, 1, LC); DWP(b_, 2, LC); DWP(b_, 3, LC); DWP(b_, 4, LC); DWP(b_, 5, LC); DWP(b_, 6, LC); } while (0)
#define DWQ0(base, q) do { const char* b_ = (base) + (size_t)(q) * 28 * 3584; GLDS(vwo, b_, lc7, 0, 0); GLDS(vwo, b_ + 3584, lc7, 1024, 0); GLDS(vwo, b_ + 2 * 3584, lc7, 2048, 0); \
        GLDS(vwo, b_ + 3 * 3584, lc7, 3072, 0); DWP(b_, 4, 0); DWP(b_, 5, 0); DWP(b_, 6, 0); } while (0)
#define KVG(gr) do { const char* p_ = kv0 + (size_t)(gr) * 32768; KV4(p_, L_RING + ((gr) % 3) * 32768); KV4(p_ + 4096, L_RING + ((gr) % 3) * 32768 + 4096); } while (0)
        const int q4 = (lane & 15) >> 2, p4 = lane & 3, ch = (lane >> 4) & 1;
        const unsigned wl = lds0 + (4 * hi + q4) * 256 + (((c ^ q4) << 6) + ch * 32 + p4 * 8);
        unsigned wsa = wl + L_SA, wsk = wsa - (1024u & mhi); PIN(wsa); PIN(wsk);
        unsigned wz = wl, wzk = wl - (1024u & mhi); PIN(wz); PIN(wzk);
#define WFR(B, BK, off, k) wfrag((B) + (off) + ((k) >> 1) * 7168 + ((k) & 1) * 4096, ((k) == 7 ? (BK) : (B)) + (off) + ((k) >> 1) * 7168 + ((k) & 1) * 4096 + 2048)
        bf16x8 war[32];
#define WARQ(q, off) do { _Pragma("unroll") for (int k = 0; k < 8; ++k) war[8 * (q) + k] = WFR(wsa, wsk, off, k); } while (0)
        const unsigned bB = WS_BR + BR_SZ;
        WAIT_BAR(0);
        STAMP(11);
        KVG(2); DWQ(woap, 0, L_SA);
        WAIT_BAR(0);
        STAMP(12);
        if (tid == 256) set_flag(ws, WS_FB + (cb * 16 + j) * 8, tag);
        KVG(3); DWQ(woap, 1, L_SB); WARQ(0, 0);
        const unsigned fbo = WS_FB + (((16 * (lane % 3) + h) / 3) * 16 + ((lane & 4) && j > 0 ? j - 1 : j)) * 8;
        WAIT_BAR(0);
        STAMP(13);
        KVG(4); DWQ(woap, 2, L_SA); WARQ(1, L_SB - L_SA);
        WAIT_BAR(0);
        STAMP(14);
        asm volatile("global_load_dwordx2 %0, %1, %2 sc1" : "=v"(pa) : "v"(fbo), "s"(ws) : "memory");
        KVG(5); WARQ(2, 0);
        asm volatile("s_waitcnt vmcnt(8)" : "+v"(pa) :: "memory");
        if (__builtin_expect(!__all(pa == tag), 0)) wait_flags_d(ws, fbo, tag);
        {   const char* qo = ws + bB + BR_Q + (size_t)(h * 64 + tile) * 2048; const char* ko = ws + bB + BR_KV + (size_t)(h * 64 + tile) * 4096;
            const unsigned lq2 = lds0 + t * 2048, lk2 = lds0 + t * 4096;
            GLDSC(vl, qo, lq2, L_SB + 20480, 0); GLDSC(vl, qo, lq2, L_SB + 20480, 1024);
            GLDSC(vl, ko, lk2, L_SB + 4096, 0); GLDSC(vl, ko, lk2, L_SB + 4096, 1024); GLDSC(vl, ko, lk2, L_SB + 4096, 2048); GLDSC(vl, ko, lk2, L_SB + 4096, 3072);
            if (t == 0) { const char* po = tile > 0 ? ko - 4096 : ko; GLDSC(vl, po, lds0, L_SB, 0); GLDSC(vl, po, lds0, L_SB, 1024); GLDSC(vl, po, lds0, L_SB, 3072); } }
        WAIT_BAR(0);
        STAMP(15);
        {
            const unsigned bo = lds0 + L_SB + t * 4096 + vl, bq = lds0 + L_SB + 20480 + t * 2048 + vl;
            const bf16x8 bq0 = ldsr128(bq), bq1 = ldsr128(bq + 1024), bk0 = ldsr128(bo + 4096), bk1 = ldsr128(bo + 5120), bv0 = ldsr128(bo + 6144), bv1 = ldsr128(bo + 7168);
            const bf16x8 bp0 = ldsr128(bo), bp1 = ldsr128(bo + 1024), bpv = ldsr128(bo + 3072);
            KVG(6); DWQ(woap, 3, L_SA);
            __builtin_amdgcn_s_setprio(1);
            f32x16 sc = MFMA(bk0, bq0, cbv); sc = MFMA(bk1, bq1, sc);
            f32x16 sp = MFMA(bp0, bq0, pbv); sp = MFMA(bp1, bq1, sp);
            float m = max16(sc);
#pragma unroll
            for (int r = 8; r < 16; r += 2) m = __builtin_fmaxf(__builtin_fmaxf(m, sp[r]), sp[r + 1]);
            m = swap_max(m);
#pragma unroll
            for (int r = 0; r < 16; ++r) sc[r] = ex2(sc[r] - m);
#pragma unroll
            for (int r = 8; r < 16; ++r) sp[r] = ex2(sp[r] - m);
            u32x4 c0, c1, p0, p1; pack16(sc, c0, c1); pack16(sp, p0, p1);
            f32x16 ob = MFMA(bv0, asbf(c0), (f32x16){}); ob = MFMA(bv1, asbf(c1), ob); ob = MFMA(bpv, asbf(p1), ob);
            const float rl = __builtin_amdgcn_rcpf(from_hi(ob[12]));
#pragma unroll
            for (int i = 0; i < 16; ++i) ob[i] *= rl;
            u32x4 e0, e1; pack16(ob, e0, e1); fix_pad(e1, 0u, mhi);
            st16c(rs, e0, vl, bB + BR_O + (tile * 16 + h) * 2048); st16c(rs, e1, vl + 1024, bB + BR_O + (tile * 16 + h) * 2048);
            __builtin_amdgcn_s_setprio(0);
        }
        WAIT_BAR(2);
        STAMP(16);
        KVG(7); DWQ(wobp, 0, L_SB); WARQ(3, 0);
        asm volatile("s_waitcnt vmcnt(15)" ::: "memory");
        if (lane == 0) set_flag(ws, WS_FOB + (tile * 16 + h) * 8, tag);
        const unsigned foo = WS_FOB + (tt * 16 + (lane & 15)) * 8;
        WAIT_BAR(1);
        STAMP(17);
        DWQ(wobp, 1, L_SA);
        WAIT_BAR(0);
        STAMP(18);
        asm volatile("global_load_dwordx2 %0, %1, %2 sc1" : "=v"(pa) : "v"(foo), "s"(ws) : "memory");
        DWQ0(wobp, 2);
        asm volatile("s_waitcnt vmcnt(7)" : "+v"(pa) :: "memory");
        if (__builtin_expect(!__all(pa == tag), 0)) wait_flags_d(ws, foo, tag);
        {   const char* ofb = ws + bB + BR_O + (size_t)tt * 32768 + c * 8192; KV4(ofb, L_OF); KV4(ofb + 4096, L_OF + 4096); }
        unsigned oad = lds0 + L_OF + vl; PIN(oad);
        f32x16 accB = {};
        bf16x8 wf[4], of[4];
#define OPB(B, BK, off, kbase) do { \
        _Pragma("unroll") for (int i = 0; i < 4; ++i) { wf[i] = WFR(B, BK, off, i); of[i] = ldsr128(oad + ((kbase) + i) * 1024); } \
        _Pragma("unroll") for (int i = 0; i < 8; ++i) { SBAR(); accB = MFMA(wf[i & 3], of[i & 3], accB); SBAR(); \
            if (i < 4) { wf[i] = WFR(B, BK, off, i + 4); of[i] = ldsr128(oad + ((kbase) + i + 4) * 1024); } } SBAR(); } while (0)
        BAR_ONLY();
        STAMP(30);
        WAIT_BAR(0);
        STAMP(23);
        DWQ(wobp, 3, L_OA);
        OPB(wsa, wsk, L_SB - L_SA, 0); OPB(wsa, wsk, 0, 8); OPB(wz, wzk, 0, 16);
        if (tid == 256) *(LAS unsigned*)(uintptr_t)(lds0 + L_SCR) = 0u;
        WAIT_BAR(0);
        OPB(wz, wzk, L_OA, 24);
        float gate = __builtin_amdgcn_rcpf(1.0f + ex2(-1.4426950408889634f * glog)); const float g1 = 1.0f - gate;
#pragma unroll
        for (int i = 0; i < 16; ++i) accB[i] *= g1;
        unsigned oo = (tt * 32 + r32) * 896 + (col0 + 32 * c) * 2 + hi * 16;
        PIN(accB); PIN(gate); PIN(oo);
        STAMP(24);
        if (t == 0) { wait_flags_d(ws, WS_FOA + (tt * 16 + (lane & 15)) * 8, tag); lds_arrive(lds0 + L_SCR, lane); } else lds_wait(lds0 + L_SCR, 1);
        STAMP(25);
        {
            const char* ofa = ws + WS_BR + BR_O + (size_t)tt * 32768 + c * 4096; const unsigned lp = lds0 + c * 4096;
            GLDSC(vl, ofa, lp, 0, 0); GLDSC(vl, ofa, lp, 0, 1024); GLDSC(vl, ofa, lp, 0, 2048); GLDSC(vl, ofa, lp, 0, 3072);
            GLDSC(vl, ofa + 16384, lp, 16384, 0); GLDSC(vl, ofa + 16384, lp, 16384, 1024); GLDSC(vl, ofa + 16384, lp, 16384, 2048); GLDSC(vl, ofa + 16384, lp, 16384, 3072); }
        WAIT_BAR(4);
        STAMP(26);
        f32x16 acc = {};
        {   unsigned ob_ = lds0 + vl; PIN(ob_);
            bf16x8 og[8];
#pragma unroll
            for (int i = 0; i < 8; ++i) og[i] = ldsr128(ob_ + i * 1024);
#pragma unroll
            for (int i = 0; i < 32; ++i) { if (i == 8) { WAIT_BAR(0); } SBAR(); acc = MFMA(war[i], og[i & 7], acc); SBAR(); if (i < 24) og[i & 7] = ldsr128(ob_ + (i + 8) * 1024); }
            SBAR();
        }
        if (bid == 0 && tid == 256) __hip_atomic_store((GAS u64*)(ws + WS_EPOCH), ep + 1, RLX_AGENT);
        if (cg < 3 || c >= 2) {
#pragma unroll
            for (int i = 0; i < 16; ++i) acc[i] = acc[i] * gate + accB[i];
            u32x4 s0_, s1_; pack16(acc, s0_, s1_);
            auto r0 = __builtin_amdgcn_permlane32_swap(s0_[0], s0_[2], false, false), r1 = __builtin_amdgcn_permlane32_swap(s0_[1], s0_[3], false, false);
            auto r2 = __builtin_amdgcn_permlane32_swap(s1_[0], s1_[2], false, false), r3 = __builtin_amdgcn_permlane32_swap(s1_[1], s1_[3], false, false);
            char* op = out + oo;
            *(GAS u32x4*)op = (u32x4){r0[0], r1[0], r0[1], r1[1]};
            *(GAS u32x4*)(op + 32) = (u32x4){r2[0], r3[0], r2[1], r3[1]};
        }
    }
    STAMPF(27);
    asm volatile(".Lfk_end:");
}

extern "C" void kernel_launch(void* const* d_in, const int* in_sizes, int n_in,
                              void* d_out, int out_size, void* d_ws, size_t ws_size,
                              hipStream_t stream) {
    (void)in_sizes; (void)n_in; (void)out_size; (void)ws_size;
    (void)hipFuncSetAttribute((const void*)fused_kernel, hipFuncAttributeMaxDynamicSharedMemorySize, LDS_BYTES);
    fused_kernel<<<256, 512, LDS_BYTES, stream>>>((const char*)d_in[0], (const char*)d_in[1], (const char*)d_in[2], (const char*)d_in[4],
                                                  (const char*)d_in[5], (const char*)d_in[6], (char*)d_ws, (char*)d_out);
}
```

```cpp
#include <hip/hip_runtime.h>
#include <hip/hip_bf16.h>
#include <stdint.h>

typedef short bf16x8 __attribute__((ext_vector_type(8)));
typedef short s16x4 __attribute__((ext_vector_type(4)));
typedef float f32x16 __attribute__((ext_vector_type(16)));
typedef float f32x4 __attribute__((ext_vector_type(4)));
typedef float f32x2 __attribute__((ext_vector_type(2)));
typedef unsigned u32x4 __attribute__((ext_vector_type(4)));
typedef unsigned u32x2 __attribute__((ext_vector_type(2)));
typedef __bf16 bf2_t __attribute__((ext_vector_type(2)));
typedef unsigned long long u64;
#define LAS __attribute__((address_space(3)))
#define GAS __attribute__((address_space(1)))
#define DI __device__ __forceinline__
#define MFMA(a, b, c) __builtin_amdgcn_mfma_f32_32x32x16_bf16((a), (b), (c), 0, 0, 0)
#define SBAR() __builtin_amdgcn_sched_barrier(0)
#define PIN(x) asm volatile("" : "+v"(x))
#define RLX_AGENT __ATOMIC_RELAXED, __HIP_MEMORY_SCOPE_AGENT

#define POLL_SLEEP 4
template <int N> struct IC { static constexpr int value = N; };
constexpr float QSCALE = 0.18898223650461362f * 1.4426950408889634f;

constexpr unsigned WS_EPOCH = 0;
constexpr unsigned WS_FA = 4096;
constexpr unsigned WS_FB = WS_FA + 2048;
constexpr unsigned WS_FOA = WS_FB + 2048;
constexpr unsigned WS_FOB = WS_FOA + 8192;
constexpr unsigned WS_BR = 65536;
constexpr unsigned BR_Q = 0, BR_KV = 2097152, BR_O = 6291456, BR_SZ = 8388608;
constexpr unsigned WS_TOTAL = WS_BR + 2 * BR_SZ;

constexpr unsigned L_XB = 0;
constexpr unsigned L_KW = 98304;
constexpr unsigned L_GT = 155648;
constexpr unsigned L_CNT = 159232;
constexpr unsigned L_GL = 159744;
constexpr unsigned L_Q = 139264;
constexpr unsigned L_RING = 0;
constexpr unsigned L_SA = 98304, L_SB = 126976;
constexpr unsigned L_OF = 65536;
constexpr unsigned L_OA = 32768;
constexpr unsigned L_SCR = 155648;
constexpr unsigned LDS_BYTES = 163840;

DI unsigned cvtpk(float lo, float hi) { f32x2 v = {lo, hi}; return __builtin_bit_cast(unsigned, __builtin_convertvector(v, bf2_t)); }
#define GLDS(voff, sbase, lds_s, LC, OFF) asm volatile("s_add_i32 m0, %2, %3\n\ts_nop 0\n\tglobal_load_lds_dwordx4 %0, %1 offset:%4" \
        :: "v"(voff), "s"(sbase), "s"(__builtin_amdgcn_readfirstlane(lds_s)), "n"((int)(LC)), "n"((int)(OFF)) : "memory", "scc")
#define GLDSC(voff, sbase, lds_s, LC, OFF) asm volatile("s_add_i32 m0, %2, %3\n\ts_nop 0\n\tglobal_load_lds_dwordx4 %0, %1 offset:%4 sc1" \
        :: "v"(voff), "s"(sbase), "s"(__builtin_amdgcn_readfirstlane(lds_s)), "n"((int)(LC)), "n"((int)(OFF)) : "memory", "scc")
#define GLDS_IF(sel, voff, sbase, lds_s, LC, OFF) asm volatile("s_cmp_lg_u32 %5, 0\n\ts_cbranch_scc1 1f\n\ts_add_i32 m0, %2, %3\n\ts_nop 0\n\tglobal_load_lds_dwordx4 %0, %1 offset:%4\n1:" \
        :: "v"(voff), "s"(sbase), "s"(__builtin_amdgcn_readfirstlane(lds_s)), "n"((int)(LC)), "n"((int)(OFF)), "s"(sel) : "memory", "scc")
#define GLDSC_IF(sel, voff, sbase, lds_s, LC, OFF) asm volatile("s_cmp_lg_u32 %5, 0\n\ts_cbranch_scc1 1f\n\ts_add_i32 m0, %2, %3\n\ts_nop 0\n\tglobal_load_lds_dwordx4 %0, %1 offset:%4 sc1\n1:" \
        :: "v"(voff), "s"(sbase), "s"(__builtin_amdgcn_readfirstlane(lds_s)), "n"((int)(LC)), "n"((int)(OFF)), "s"(sel) : "memory", "scc")
#define WAIT_BAR(N) asm volatile("s_waitcnt vmcnt(" #N ") lgkmcnt(0)\n\ts_barrier" ::: "memory")
#define BAR_ONLY() asm volatile("s_waitcnt lgkmcnt(0)\n\ts_barrier" ::: "memory")
DI bf16x8 ldsr128(unsigned a) { return *(const LAS bf16x8*)(uintptr_t)a; }
typedef short v4i16_t __attribute__((ext_vector_type(4)));
DI s16x4 ldstr(unsigned a) { return __builtin_bit_cast(s16x4, __builtin_amdgcn_ds_read_tr16_b64_v4i16((LAS v4i16_t*)(uintptr_t)a)); }
DI bf16x8 wfrag(unsigned a0, unsigned a1) { s16x4 lo = ldstr(a0), hi = ldstr(a1); return __builtin_shufflevector(lo, hi, 0, 1, 2, 3, 4, 5, 6, 7); }
DI bf16x8 asbf(u32x4 v) { return __builtin_bit_cast(bf16x8, v); }
DI float ex2(float v) { return __builtin_amdgcn_exp2f(v); }
DI float swap_max(float m) {
    auto rr = __builtin_amdgcn_permlane32_swap(__float_as_uint(m), __float_as_uint(m), false, false);
    return __builtin_fmaxf(__uint_as_float(rr[0]), __uint_as_float(rr[1]));
}
DI float from_hi(float v) {
    auto rr = __builtin_amdgcn_permlane32_swap(__float_as_uint(v), __float_as_uint(v), false, false);
    return __uint_as_float(rr[1]);
}
DI void pack16(const f32x16& a, u32x4& s0, u32x4& s1) {
    s0 = (u32x4){cvtpk(a[0], a[1]), cvtpk(a[2], a[3]), cvtpk(a[4], a[5]), cvtpk(a[6], a[7])};
    s1 = (u32x4){cvtpk(a[8], a[9]), cvtpk(a[10], a[11]), cvtpk(a[12], a[13]), cvtpk(a[14], a[15])};
}
DI float max16(const f32x16& s) {
    float m = __builtin_fmaxf(s[0], s[1]);
#pragma unroll
    for (int r = 2; r < 16; r += 2) m = __builtin_fmaxf(__builtin_fmaxf(m, s[r]), s[r + 1]);
    return m;
}
typedef __amdgpu_buffer_rsrc_t rsrc_t;
DI void st16c(rsrc_t r, u32x4 v, unsigned voff, unsigned soff) { __builtin_amdgcn_raw_buffer_store_b128(v, r, voff, soff, 16); }
DI bf16x8 ld16c(rsrc_t r, unsigned voff, unsigned soff) { return asbf(__builtin_amdgcn_raw_buffer_load_b128(r, voff, soff, 16)); }
DI void wait_flags_d(const char* ws, unsigned off, u64 tag) {
#pragma unroll 1
    for (int it = 0; it < 40000; ++it) {
        u64 a;
        asm volatile("global_load_dwordx2 %0, %1, %2 sc1\n\ts_waitcnt vmcnt(0)" : "=&v"(a) : "v"(off), "s"(ws) : "memory");
        if (__all(a == tag)) break;
        __builtin_amdgcn_s_sleep(POLL_SLEEP);
    }
}
DI void set_flag(char* ws, unsigned off, u64 tag) { __hip_atomic_store((GAS u64*)(ws + off), tag, RLX_AGENT); }

#define STAMP(i) do {} while (0)
#define STAMPF(i) do {} while (0)
#define STARGS
#define STPASS

DI void fix_v(u32x4& s0, u32x4& s1, unsigned m28) {
    unsigned keep = (m28 & 1u) - 1u, ones = m28 & 0x3F803F80u; PIN(keep); PIN(ones);
#pragma unroll
    for (int i = 0; i < 4; ++i) { s0[i] = (s0[i] & keep) | ones; s1[i] = (s1[i] & keep) | ones; }
}
DI void lds_arrive(unsigned a, int lane) { if (lane == 0) __hip_atomic_fetch_add((LAS unsigned*)(uintptr_t)a, 1u, __ATOMIC_RELAXED, __HIP_MEMORY_SCOPE_WORKGROUP); }
DI void lds_wait(unsigned a, unsigned n) {
#pragma unroll 1
    for (int it = 0; it < 2000000; ++it) {
        if (__builtin_amdgcn_readfirstlane(__hip_atomic_load((LAS unsigned*)(uintptr_t)a, __ATOMIC_RELAXED, __HIP_MEMORY_SCOPE_WORKGROUP)) >= n) break;
        __builtin_amdgcn_s_sleep(1);
    }
    asm volatile("" ::: "memory");
}
DI void fix_pad(u32x4& s1, unsigned pad, unsigned mhi) { s1[2] = (s1[2] & ~mhi) | (pad & mhi); s1[3] &= ~mhi; }
template <bool V>
DI void pack_unit(f32x16 a, int u, unsigned m28, unsigned mhi, u32x4& s0, u32x4& s1) {
    if (!V) {
        const bool isq = u < 16;
        const float sc = isq ? QSCALE : 1.0f;
#pragma unroll
        for (int i = 0; i < 16; ++i) a[i] *= sc;
        pack16(a, s0, s1);
        fix_pad(s1, isq ? 0u : 0x00003F80u, mhi);
    } else { pack16(a, s0, s1); fix_v(s0, s1, m28); }
}
#define MKTAG() do { u64 z = ((u64)__builtin_amdgcn_readfirstlane((unsigned)(ep >> 32)) << 32) | (unsigned)__builtin_amdgcn_readfirstlane((unsigned)ep); \
        z += 0x9E3779B97F4A7C15ull; z = (z ^ (z >> 30)) * 0xBF58476D1CE4E5B9ull; z = (z ^ (z >> 27)) * 0x94D049BB133111EBull; tag = z ^ (z >> 31); } while (0)
#define XB(i) (L_XB + (i) * 16384)
#define KW(i) (L_KW + (i) * 8192)
#define MM(VV, acc, wf, xfr) do { if (VV) acc = MFMA(xfr, wf, acc); else acc = MFMA(wf, xfr, acc); } while (0)
__device__ constexpr unsigned so_of(int c) { return c == 0 ? KW(0) : (c == 6 ? KW(2) : XB(c)); }
__device__ constexpr unsigned xo_of(int c) { return c == 6 ? XB(0) : XB(c); }

template <bool VK>
DI void stage1(bf16x8 (&xf)[28], const unsigned (&xa)[4], const unsigned (&xh)[4], unsigned ka0, unsigned ka1, rsrc_t rs, int h, int tile, int lane, unsigned m28, unsigned mhi,
               unsigned gv0, unsigned gv1, unsigned dmc, unsigned dmi, u64 ep, u64& tag, unsigned lds0 STARGS) {
    f32x16 acc0 = {}, acc1 = {};
    bf16x8 w0[2], w1[2];
#define XR(s) xf[s] = ldsr128(xo_of((s) >> 2) >= 65536 ? xh[(s) & 3] + (xo_of((s) >> 2) - 65536) : xa[(s) & 3] + xo_of((s) >> 2))
#define KF(base, s) wfrag((base) + (s) * 2048, (base) + (s) * 2048 + 512)
    XR(0); w0[0] = KF(ka0, 0); w1[0] = KF(ka1, 0); XR(1); w0[1] = KF(ka0, 1); w1[1] = KF(ka1, 1);
#pragma unroll
    for (int s = 0; s < 28; ++s) {
        if ((s & 3) == 2 && s < 26) { BAR_ONLY(); if (s == 10) STAMP(3); if (s == 22) STAMP(4); }
        if (s == 20) {
            asm volatile("s_waitcnt vmcnt(0)" ::: "memory"); SBAR();
            asm volatile("" : "+v"(gv0), "+v"(gv1), "+v"(ep) : "v"(dmc), "v"(dmi) : "memory");
            const int tid = threadIdx.x;
            { u32x2 e = {gv0, 0u}; *(LAS u32x2*)(uintptr_t)(lds0 + L_GT + tid * 8) = e; }
            if (tid < 192) { u32x2 e = {gv1, 0u}; *(LAS u32x2*)(uintptr_t)(lds0 + L_GT + 2048 + tid * 8) = e; }
            if (tid == 0) *(LAS u32x2*)(uintptr_t)(lds0 + L_CNT) = (u32x2){0u, 0u};
            MKTAG();
        }
        SBAR();
        MM(VK, acc0, w0[s & 1], xf[s]); SBAR();
        if (s + 2 < 28) { XR(s + 2); w0[s & 1] = KF(ka0, s + 2); }
        SBAR();
        MM(VK, acc1, w1[s & 1], xf[s]); SBAR();
        if (s + 2 < 28) w1[s & 1] = KF(ka1, s + 2);
    }
    SBAR();
    u32x4 s0, s1;
    pack16(acc0, s0, s1); if (!VK) fix_pad(s1, 0x00003F80u, mhi); else fix_v(s0, s1, m28);
    const unsigned off = WS_BR + BR_KV + ((2 * h & 15) * 64 + tile) * 4096 + (VK ? 2048 : 0);
    st16c(rs, s0, lane * 16, off); st16c(rs, s1, lane * 16 + 1024, off);
    pack16(acc1, s0, s1); if (!VK) fix_pad(s1, 0x00003F80u, mhi); else fix_v(s0, s1, m28);
    st16c(rs, s0, lane * 16, off + 262144); st16c(rs, s1, lane * 16 + 1024, off + 262144);
}
template <bool V01, bool V2>
DI void stage2(const bf16x8 (&xf)[28], unsigned ql, unsigned qh, unsigned gl, bool spec, unsigned wlo, unsigned whi, char* ws, rsrc_t rs, unsigned lds0,
               int h, int cb, int j, int t, int lane, u64 tag, u32x4 (&bp)[6], unsigned m28, unsigned mhi STARGS) {
    f32x16 aq = {}, acc0 = {}, acc1 = {}, acc2 = {};
    bf16x8 wq[2], w0[2], w1[2], w2[2];
    const unsigned gstride = spec ? 32u : 256u;
    bool sent = false;
#define SADDR(lo, hi_, c) (so_of(c) >= 65536 ? (hi_) + (so_of(c) - 65536) : (lo) + so_of(c))
#define WF(s, u) wfrag(SADDR(wlo, whi, (s) >> 2) + ((s) & 3) * 3072 + (u) * 56, SADDR(wlo, whi, (s) >> 2) + ((s) & 3) * 3072 + 768 + (u) * 56)
#define QR(s) do { const unsigned a_ = spec ? gl + 128 * (s) : SADDR(ql, qh, (s) >> 2) + ((s) & 3) * 1024; wq[(s) & 1] = wfrag(a_, a_ + gstride); } while (0)
    QR(0); w0[0] = WF(0, 0); w1[0] = WF(0, 1); w2[0] = WF(0, 2); QR(1); w0[1] = WF(1, 0); w1[1] = WF(1, 1); w2[1] = WF(1, 2);
#pragma unroll
    for (int s = 0; s < 28; ++s) {
        if (s == 6 || s == 10 || s == 14 || s == 18) { BAR_ONLY(); if (s == 10) STAMP(6); if (s == 18) STAMP(7); }
        if (s >= 7 && s <= 15) {
            if (s == 15) asm volatile("s_waitcnt vmcnt(0)" ::: "memory");
            if (!sent && (s == 15 || (__builtin_amdgcn_s_getreg(7 | (3 << 11)) & 15) == 0)) {
                sent = true; asm volatile("" ::: "memory");
                if (lane == 0) { if (__hip_atomic_fetch_add((LAS unsigned*)(uintptr_t)(lds0 + L_CNT), 1u, __ATOMIC_RELAXED, __HIP_MEMORY_SCOPE_WORKGROUP) == 3u) set_flag(ws, WS_FA + (h * 16 + j) * 8, tag); }
                STAMP(8);
            }
        }
        SBAR();
        aq = MFMA(wq[s & 1], xf[s], aq); SBAR();
        if (s + 2 < 28) QR(s + 2);
        SBAR();
        MM(V01, acc0, w0[s & 1], xf[s]); SBAR();
        if (s + 2 < 28) w0[s & 1] = WF(s + 2, 0);
        SBAR();
        MM(V01, acc1, w1[s & 1], xf[s]); SBAR();
        if (s + 2 < 28) w1[s & 1] = WF(s + 2, 1);
        SBAR();
        MM(V2, acc2, w2[s & 1], xf[s]); SBAR();
        if (s + 2 < 28) w2[s & 1] = WF(s + 2, 2);
    }
    SBAR();
    {
        if (lane >> 5) *(LAS float*)(uintptr_t)(lds0 + L_GL + t * 128 + (lane & 31) * 4) = aq[12];
#pragma unroll
        for (int i = 0; i < 16; ++i) aq[i] *= QSCALE;
        u32x4 s0, s1; pack16(aq, s0, s1);
        fix_pad(s1, 0u, mhi);
        *(LAS u32x4*)(uintptr_t)(lds0 + L_Q + t * 2048 + lane * 16) = s0; *(LAS u32x4*)(uintptr_t)(lds0 + L_Q + t * 2048 + 1024 + lane * 16) = s1;
    }
    pack_unit<V01>(acc0, 3 * cb, m28, mhi, bp[0], bp[1]); pack_unit<V01>(acc1, 3 * cb + 1, m28, mhi, bp[2], bp[3]); pack_unit<V2>(acc2, 3 * cb + 2, m28, mhi, bp[4], bp[5]);
}

__global__ __launch_bounds__(512, 2) void fused_kernel(const char* __restrict__ x, const char* __restrict__ wa, const char* __restrict__ woa,
                                                       const char* __restrict__ wb, const char* __restrict__ wob, const char* __restrict__ gw,
                                                       char* ws, char* __restrict__ out) {
    extern __shared__ __attribute__((aligned(16))) char smem[];
    const unsigned lds0 = (unsigned)(uintptr_t)(LAS char*)smem;
    const int tid = threadIdx.x, lane = tid & 63, w = __builtin_amdgcn_readfirstlane(tid >> 6), t = w & 3, g = w >> 2;
    const int bid = blockIdx.x, xg = bid & 7, idx = bid >> 3;
    const int h = (xg >> 1) * 4 + (idx & 3), j = (xg & 1) * 8 + (idx >> 2);
    const int r32 = lane & 31, hi = lane >> 5;
    const rsrc_t rs = __builtin_amdgcn_make_buffer_rsrc(ws, 0, WS_TOTAL, 0x00020000);
    const unsigned vl = lane * 16;
    asm volatile("" :: "s"(x), "s"(wa), "s"(woa), "s"(wb), "s"(wob), "s"(gw), "s"(ws), "s"(out));
    u64 pc; unsigned len;
    asm volatile("s_getpc_b64 %0\n.Lfk_ref:\n\ts_mov_b32 %1, .Lfk_end-.Lfk_ref" : "=s"(pc), "=s"(len));
    STAMPF(0);
    const int cb = (int)((0xCFEDBA8967542103ull >> (4 * h)) & 15);
    const int tile = 4 * j + t;
    const int rot = (j + 2 * (h & 3)) % 7;
#define CK(p) ((p) + rot >= 7 ? (p) + rot - 7 : (p) + rot)
    const unsigned vxe = (lane >> 3) * 896 + (((lane & 7) ^ (lane >> 4)) << 4), lx = lds0 + t * 4096;
    const char* xb = x + (size_t)(j * 128 + t * 32) * 896;
    unsigned dmi;
    {
        const unsigned co = (unsigned)((idx * 8 + w) * 2 + (lane & 1)) * 128u;
        asm volatile("global_load_ubyte %0, %1, %2" : "=v"(dmi) : "v"(co < len ? co : 0u), "s"(pc) : "memory"); }
    {
        const char* p_ = xb + rot * 128;
        GLDS_IF(g, vxe, p_, lx, XB(0), 0); GLDS_IF(g, (vxe ^ 64u) + 7168, p_, lx, XB(0) + 1024, 0); GLDS_IF(g, vxe + 14336, p_, lx, XB(0) + 2048, 0); GLDS_IF(g, (vxe ^ 64u) + 21504, p_, lx, XB(0) + 3072, 0); }
    u64 ep, tag;
    unsigned m28 = r32 == 28 ? ~0u : (r32 > 28 ? 1u : 0u), mhi = hi ? ~0u : 0u; PIN(m28); PIN(mhi);
    if (g == 0) {
        unsigned gv0, gv1, dmc;
        {
            const int pl = lane & 15;
            const char* tp = pl < 9 ? ws + (size_t)pl * 2097152 + 32768 : (pl == 9 ? (const char*)out : (pl == 10 ? woa : (pl == 11 ? wob : wb)));
            asm volatile("global_load_ubyte %0, %1, off sc1" : "=v"(dmc) : "v"(tp) : "memory");
        }
        asm volatile("global_load_ushort %0, %1, %2" : "=v"(gv0) : "v"((CK(t) * 64 + lane) * 2), "s"(gw) : "memory");
        asm volatile("global_load_ushort %0, %1, %2" : "=v"(gv1) : "v"((CK(t < 3 ? 4 + t : 6) * 64 + lane) * 2), "s"(gw) : "memory");
        asm volatile("global_load_dwordx2 %0, %1, %2 sc1" : "=v"(ep) : "v"(0u), "s"(ws) : "memory");
        const int fr = (r32 >> 1) & 7;
        unsigned xa[4], xh[4];
#pragma unroll
        for (int ks = 0; ks < 4; ++ks) { xa[ks] = lds0 + (4 * t + (r32 >> 3)) * 1024 + (r32 & 7) * 128 + (((2 * ks + hi) ^ fr) << 4); xh[ks] = xa[ks] + 65536; PIN(xa[ks]); PIN(xh[ks]); }
        const int q4 = (lane & 15) >> 2, p4 = lane & 3, ch = (lane >> 4) & 1;
        const unsigned o0 = ch * 32 + p4 * 8, o1 = o0 + 56, sw = (q4 >> 1) << 6;
        unsigned ka0 = lds0 + L_KW + (8 * hi + q4) * 128 + (o0 ^ sw), ka1 = lds0 + L_KW + (8 * hi + q4) * 128 + (o1 ^ sw); PIN(ka0); PIN(ka1);
        unsigned ql = lds0 + (8 * hi + q4) * 64 + ch * 32 + p4 * 8 + (h & 1) * 8, qh = ql + 65536; PIN(ql); PIN(qh);
        unsigned wlo = lds0 + 4096 + (8 * hi + q4) * 192 + ch * 32 + p4 * 8 + (cb & 1) * 8, whi = wlo + 65536; PIN(wlo); PIN(whi);
        const bool spec = ch == 1 && p4 == 3;
        const unsigned gl = lds0 + L_GT + 64 * hi + 8 * q4;
        bf16x8 xf[28]; u32x4 bp[6];
        STAMP(1);
        WAIT_BAR(4); STAMP(2);
        if (h < 8) stage1<false>(xf, xa, xh, ka0, ka1, rs, h, tile, lane, m28, mhi, gv0, gv1, dmc, dmi, ep, tag, lds0 STPASS); else stage1<true>(xf, xa, xh, ka0, ka1, rs, h, tile, lane, m28, mhi, gv0, gv1, dmc, dmi, ep, tag, lds0 STPASS);
        BAR_ONLY(); STAMP(5);
        if (cb <= 9) stage2<false, false>(xf, ql, qh, gl, spec, wlo, whi, ws, rs, lds0, h, cb, j, t, lane, tag, bp, m28, mhi STPASS);
        else if (cb == 10) stage2<false, true>(xf, ql, qh, gl, spec, wlo, whi, ws, rs, lds0, h, cb, j, t, lane, tag, bp, m28, mhi STPASS);
        else stage2<true, true>(xf, ql, qh, gl, spec, wlo, whi, ws, rs, lds0, h, cb, j, t, lane, tag, bp, m28, mhi STPASS);
        STAMP(9);
        BAR_ONLY();
#pragma unroll
        for (int i = 0; i < 3; ++i) { const int u = 3 * cb + i;
            const unsigned off = WS_BR + BR_SZ + (u < 16 ? BR_Q + (u * 64 + tile) * 2048 : BR_KV + ((u & 15) * 64 + tile) * 4096 + (u >= 32 ? 2048 : 0));
            st16c(rs, bp[2 * i], vl, off); st16c(rs, bp[2 * i + 1], vl + 1024, off); }
        STAMP(10);
        const int qp = t & 1, kh = t >> 1;
        const unsigned qad = lds0 + L_Q + qp * 4096 + kh * 2048 + vl, qbd = qad ^ 2048u;
        const bf16x8 qa0 = ldsr128(qad), qb0 = ldsr128(qbd);
        u32x4 qa1 = __builtin_bit_cast(u32x4, ldsr128(qad + 1024)), qb1 = __builtin_bit_cast(u32x4, ldsr128(qbd + 1024));
        unsigned ac = lds0 + L_RING + kh * 4096 + vl, an = ac + 32768, af = ac + 65536;
        f32x16 oa = {}, ob = {}, sA, sB; u32x4 pA0 = {}, pA1 = {}, pB0 = {}, pB1 = {};
        bf16x8 k0 = ldsr128(ac), k1 = ldsr128(ac + 1024), v0 = {}, v1 = {}, kn0, kn1;
        float mra, mrb;
        {
            sA = MFMA(k0, qa0, (f32x16){}); sB = MFMA(k0, qb0, (f32x16){});
            f32x16 ta = MFMA(k1, asbf(qa1), sA); sB = MFMA(k1, asbf(qb1), sB);
            const unsigned na = cvtpk(-swap_max(max16(ta)), 0.f), nb = cvtpk(-swap_max(max16(sB)), 0.f);
            mra = -__uint_as_float(na << 16); mrb = -__uint_as_float(nb << 16);
            qa1[2] = (qa1[2] & ~mhi) | (na & mhi); qb1[2] = (qb1[2] & ~mhi) | (nb & mhi);
            sA = MFMA(k1, asbf(qa1), sA);
        }
#define GRP(S, b, SP, bp, PP, jp) do { PP[jp] = cvtpk(SP[bp], SP[bp + 1]); PP[jp + 1] = cvtpk(SP[bp + 2], SP[bp + 3]); SBAR(); \
        S[b] = ex2(S[b]); S[b + 1] = ex2(S[b + 1]); S[b + 2] = ex2(S[b + 2]); S[b + 3] = ex2(S[b + 3]); PIN(PP); PIN(S); SBAR(); } while (0)
#define STEP2(KC0, KC1, KN0, KN1, VP1, VC1, ka, va) do { \
        KN0 = ldsr128(ka); ob = MFMA(v0, asbf(pB0), ob); SBAR(); GRP(sA, 0, sB, 12, pB1, 2); KN1 = ldsr128((ka) + 1024); \
        sB = MFMA(KC0, qb0, (f32x16){}); SBAR(); VC1 = ldsr128((va) + 3072); GRP(sA, 4, sA, 0, pA0, 0); v0 = ldsr128((va) + 2048); \
        sB = MFMA(KC1, asbf(qb1), sB); SBAR(); GRP(sA, 8, sA, 4, pA0, 2); \
        ob = MFMA(VP1, asbf(pB1), ob); SBAR(); GRP(sA, 12, sA, 8, pA1, 0); \
        oa = MFMA(v0, asbf(pA0), oa); SBAR(); GRP(sB, 0, sA, 12, pA1, 2); \
        sA = MFMA(KN0, qa0, (f32x16){}); SBAR(); GRP(sB, 4, sB, 0, pB0, 0); \
        sA = MFMA(KN1, asbf(qa1), sA); SBAR(); GRP(sB, 8, sB, 4, pB0, 2); \
        oa = MFMA(VC1, asbf(pA1), oa); SBAR(); GRP(sB, 12, sB, 8, pB1, 0); \
    } while (0)
        sB = (f32x16){};
        bf16x8 v1n;
#pragma unroll 1
        for (int gr = 0; gr < 8; ++gr) {
            STEP2(k0, k1, kn0, kn1, v1, v1n, ac + 8192, ac);
            STEP2(kn0, kn1, k0, k1, v1n, v1, ac + 16384, ac + 8192);
            if (gr == 0) asm volatile("s_barrier" ::: "memory"); else asm volatile("s_waitcnt vmcnt(0)\n\ts_barrier" ::: "memory");
            STAMP(11 + gr);
            SBAR();
            STEP2(k0, k1, kn0, kn1, v1, v1n, ac + 24576, ac + 16384);
            STEP2(kn0, kn1, k0, k1, v1n, v1, an, ac + 24576);
            const unsigned tmp = ac; ac = an; an = af; af = tmp;
        }
        pB1[2] = cvtpk(sB[12], sB[13]); pB1[3] = cvtpk(sB[14], sB[15]);
        ob = MFMA(v0, asbf(pB0), ob); ob = MFMA(v1, asbf(pB1), ob);
        STAMP(20);
#define SAFE_LIMIT 1.0e27f
        if (__builtin_expect(__any(hi && !(oa[12] < SAFE_LIMIT && ob[12] < SAFE_LIMIT && oa[12] > 1.0e-27f && ob[12] > 1.0e-27f)), 0)) {
            qa1[2] &= ~mhi; qb1[2] &= ~mhi;
            mra = -__builtin_inff(); mrb = mra; oa = (f32x16){}; ob = (f32x16){};
            const unsigned kb = WS_BR + BR_KV + h * 262144 + kh * 4096;
#pragma unroll 1
            for (int i = 0; i < 32; ++i) {
                const unsigned so = kb + i * 8192;
                const bf16x8 a0 = ld16c(rs, vl, so), a1 = ld16c(rs, vl + 1024, so), b0 = ld16c(rs, vl + 2048, so), b1 = ld16c(rs, vl + 3072, so);
#define ONLINE(Q0, Q1, O, M) do { f32x16 s_ = MFMA(a0, Q0, (f32x16){}); s_ = MFMA(a1, asbf(Q1), s_); \
                const float mn = __builtin_fmaxf(M, swap_max(max16(s_))), al = ex2(M - mn); M = mn; \
                _Pragma("unroll") for (int r = 0; r < 16; ++r) { s_[r] = ex2(s_[r] - mn); O[r] *= al; } \
                u32x4 e0, e1; pack16(s_, e0, e1); O = MFMA(b0, asbf(e0), O); O = MFMA(b1, asbf(e1), O); } while (0)
                ONLINE(qa0, qa1, oa, mra); ONLINE(qb0, qb1, ob, mrb);
            }
        }
        {   const unsigned cw_ = lds0 + L_OA + t * 4096 + vl;
#define PUTP(O, M) do { O[13] = __uint_as_float((__float_as_uint(O[13]) & ~mhi) | (__float_as_uint(M) & mhi)); \
            _Pragma("unroll") for (int i = 0; i < 4; ++i) *(LAS f32x4*)(uintptr_t)(cw_ + i * 1024) = (f32x4){O[4 * i], O[4 * i + 1], O[4 * i + 2], O[4 * i + 3]}; } while (0)
            PUTP(ob, mrb);
        }
        STAMP(29);
        BAR_ONLY();
        STAMP(30);
        {   const unsigned cbs = lds0 + L_OA + (t ^ 2) * 4096 + vl;
            f32x16 o1;
#pragma unroll
            for (int i = 3; i >= 0; --i) { const f32x4 e = *(const LAS f32x4*)(uintptr_t)(cbs + i * 1024); o1[4 * i] = e[0]; o1[4 * i + 1] = e[1]; o1[4 * i + 2] = e[2]; o1[4 * i + 3] = e[3]; }
            const float m1 = from_hi(o1[13]);
            const float mm = __builtin_fmaxf(mra, m1), a0 = ex2(mra - mm), a1 = ex2(m1 - mm);
            const float rl = __builtin_amdgcn_rcpf(from_hi(oa[12] * a0 + o1[12] * a1)), f0 = a0 * rl, f1 = a1 * rl;
            f32x16 v;
#pragma unroll
            for (int i = 0; i < 16; ++i) v[i] = oa[i] * f0 + o1[i] * f1;
            u32x4 e0, e1; pack16(v, e0, e1);
            fix_pad(e1, 0u, mhi);
            const int ot = 4 * j + 2 * qp + kh;
            const unsigned so = WS_BR + BR_O + (ot * 16 + h) * 2048;
            st16c(rs, e0, vl, so); st16c(rs, e1, vl + 1024, so);
            STAMP(21);
            asm volatile("s_waitcnt lgkmcnt(0)\n\ts_barrier\n\ts_waitcnt vmcnt(0)" ::: "memory");
            if (lane == 0) set_flag(ws, WS_FOA + (ot * 16 + h) * 8, tag);
            STAMP(22);
        }
    } else {
        const unsigned vx[4] = {vxe, (vxe ^ 64u) + 7168, vxe + 14336, (vxe ^ 64u) + 21504};
        const unsigned kc = (lane & 7) ^ (((lane >> 4) & 1) << 2);
        const unsigned vk0 = (lane >> 3) * 2688 + (kc > 6 ? 6 : kc) * 16, vk1 = vk0 + 8 * 2688;
        const char* wkb = wa + 896 + 112 * h + (size_t)(16 * t) * 2688;
        const unsigned lk = lds0 + t * 2048;
#define DMA_K(ck) do { const char* p_ = wkb + (size_t)CK(ck) * 64 * 2688; GLDS(vk0, p_, lk, KW(ck), 0); GLDS(vk1, p_, lk, KW(ck) + 1024, 0); } while (0)
#define DMA_X(cx) do { const char* p_ = xb + CK(cx) * 128; GLDS(vx[0], p_, lx, xo_of(cx), 0); GLDS(vx[1], p_, lx, xo_of(cx) + 1024, 0); \
        GLDS(vx[2], p_, lx, xo_of(cx) + 2048, 0); GLDS(vx[3], p_, lx, xo_of(cx) + 3072, 0); } while (0)
        asm volatile("global_load_dwordx2 %0, %1, %2 sc1" : "=v"(ep) : "v"(0u), "s"(ws) : "memory");
        DMA_K(0); DMA_X(1); DMA_K(1); DMA_X(2); DMA_K(2);
        unsigned vw[3];
#pragma unroll
        for (int n = 0; n < 3; ++n) { const unsigned L = 64 * n + lane, row = (L * 43691u) >> 19, cc = L - row * 12; vw[n] = row * 2688 + (cc > 10 ? 10 : cc) * 16; }
        const unsigned vq = (lane >> 2) * 2688 + (lane & 3) * 16;
        const char* wqb = wa + (56 * h - 8 * (h & 1)) + (size_t)(16 * t) * 2688;
        const char* wbb = wb + (168 * cb - 8 * (cb & 1)) + (size_t)(16 * t) * 2688;
        const unsigned lq = lds0 + t * 1024, lw = lds0 + 4096 + t * 3072;
#define DMA_S(ck) do { GLDS(vq, wqb + (size_t)CK(ck) * 64 * 2688, lq, so_of(ck), 0); const char* p_ = wbb + (size_t)CK(ck) * 64 * 2688; \
        GLDS(vw[0], p_, lw, so_of(ck), 0); GLDS(vw[1], p_, lw, so_of(ck) + 1024, 0); GLDS(vw[2], p_, lw, so_of(ck) + 2048, 0); } while (0)
        STAMP(1);
        WAIT_BAR(12); asm volatile("" :: "v"(dmi)); STAMP(2); DMA_X(3); DMA_K(3);
        WAIT_BAR(12); DMA_X(4); DMA_K(4);
        WAIT_BAR(12); DMA_X(5); DMA_K(5);
        WAIT_BAR(12); STAMP(3); DMA_X(6); DMA_K(6); DMA_S(0);
        WAIT_BAR(16); DMA_S(1);
        WAIT_BAR(14);
        WAIT_BAR(4); STAMP(4); DMA_S(2);
        asm volatile("" : "+v"(ep)); MKTAG();
        const unsigned fo = WS_FA + ((((lane >> 4) & 1) * 8 + (h >> 1)) * 16 + (lane & 15)) * 8;
        WAIT_BAR(4); STAMP(5); DMA_S(3);
        WAIT_BAR(4); DMA_S(4); DMA_S(5);
        WAIT_BAR(8); STAMP(6); DMA_S(6);
        unsigned dm0;
        {
            const int li = (t & 1) * 56 + (lane < 56 ? lane : 55), c0 = (h & 3) < 3 ? (h & 3) * 256 : 640;
            asm volatile("global_load_ubyte %0, %1, %2" : "=v"(dm0) : "v"(((j & 7) * 56 + (li >> 1)) * 896 + (li & 1) * 128 + c0), "s"((t & 2) ? wob : woa) : "memory"); }
        f32x16 cbv, pbv;
        {   const unsigned long long M = (0x5555ull << (r32 + 18)) >> (4 * hi);
            int nlo = tile > 0 ? ~(unsigned)M : ~0u, nhi = ~(unsigned)(M >> 32);
#pragma unroll
            for (int r = 0; r < 16; ++r) { const int cc = (r & 3) + 8 * (r >> 2);
                int e0 = nhi << (31 - cc); PIN(e0); cbv[r] = __uint_as_float((unsigned)(e0 >> 8) & 0xFF800000u);
                if (r < 8) pbv[r] = 0.f; else { int e1 = nlo << (31 - cc); PIN(e1); pbv[r] = __uint_as_float((unsigned)(e1 >> 8) & 0xFF800000u); } }
            PIN(cbv); PIN(pbv); }
        WAIT_BAR(9);
        WAIT_BAR(0);
        asm volatile("" :: "v"(dm0));
        STAMP(7);
        u64 pa;
        if (t == 0) { wait_flags_d(ws, fo, tag); lds_arrive(lds0 + L_CNT + 4, lane); } else lds_wait(lds0 + L_CNT + 4, 1);
        STAMP(8);
        const char* kv0 = ws + WS_BR + BR_KV + (size_t)h * 262144 + t * 8192; const unsigned lr = lds0 + t * 8192;
#define KV4(p, LC) do { GLDSC(vl, p, lr, LC, 0); GLDSC(vl, p, lr, LC, 1024); GLDSC(vl, p, lr, LC, 2048); GLDSC(vl, p, lr, LC, 3072); } while (0)
        KV4(kv0, L_RING); KV4(kv0 + 4096, L_RING + 4096); KV4(kv0 + 32768, L_RING + 32768); KV4(kv0 + 36864, L_RING + 36864);
        STAMP(9);
        WAIT_BAR(8);
        STAMP(10);
        const int tt = 4 * j + (h >> 2), cg = h & 3, c = t;
        float glog = *(const LAS float*)(uintptr_t)(lds0 + L_GL + (h >> 2) * 128 + r32 * 4);
        const int col0 = cg < 3 ? cg * 128 : 320;
        const unsigned vwo = (lane >> 4) * 896 + (((lane & 15) ^ ((lane >> 4) << 2)) << 4), lc7 = lds0 + c * 7168;
        const char* wobp = wob + col0 * 2 + (size_t)(c * 7) * 3584; const char* woap = woa + col0 * 2 + (size_t)(c * 7) * 3584;
#define DWP(base, p, LC) do { if ((p) & 1) GLDS(vwo, (base) + (size_t)((p) - 1) * 3584, lc7, (LC) + 4096 + (p) * 1024 - 3584 - 4096, 3584); else GLDS(vwo, (base) + (size_t)(p) * 3584, lc7, (LC) + (p) * 1024, 0); } while (0)
#define DWQ(base, q, LC) do { const char* b_ = (base) + (size_t)(q) * 28 * 3584; DWP(b_, 0, LC); DWP(b_, 1, LC); DWP(b_, 2, LC); DWP(b_, 3, LC); DWP(b_, 4, LC); DWP(b_, 5, LC); DWP(b_, 6, LC); } while (0)
#define DWQ0(base, q) do { const char* b_ = (base) + (size_t)(q) * 28 * 3584; GLDS(vwo, b_, lc7, 0, 0); GLDS(vwo, b_ + 3584, lc7, 1024, 0); GLDS(vwo, b_ + 2 * 3584, lc7, 2048, 0); \
        GLDS(vwo, b_ + 3 * 3584, lc7, 3072, 0); DWP(b_, 4, 0); DWP(b_, 5, 0); DWP(b_, 6, 0); } while (0)
#define KVG(gr) do { const char* p_ = kv0 + (size_t)(gr) * 32768; KV4(p_, L_RING + ((gr) % 3) * 32768); KV4(p_ + 4096, L_RING + ((gr) % 3) * 32768 + 4096); } while (0)
        const int q4 = (lane & 15) >> 2, p4 = lane & 3, ch = (lane >> 4) & 1;
        const unsigned wl = lds0 + (4 * hi + q4) * 256 + (((c ^ q4) << 6) + ch * 32 + p4 * 8);
        unsigned wsa = wl + L_SA, wsk = wsa - (1024u & mhi); PIN(wsa); PIN(wsk);
        unsigned wz = wl, wzk = wl - (1024u & mhi); PIN(wz); PIN(wzk);
#define WFR(B, BK, off, k) wfrag((B) + (off) + ((k) >> 1) * 7168 + ((k) & 1) * 4096, ((k) == 7 ? (BK) : (B)) + (off) + ((k) >> 1) * 7168 + ((k) & 1) * 4096 + 2048)
        bf16x8 war[32];
#define WARQ(q, off) do { _Pragma("unroll") for (int k = 0; k < 8; ++k) war[8 * (q) + k] = WFR(wsa, wsk, off, k); } while (0)
        const unsigned bB = WS_BR + BR_SZ;
        WAIT_BAR(0);
        STAMP(11);
        KVG(2); DWQ(woap, 0, L_SA);
        WAIT_BAR(0);
        STAMP(12);
        if (tid == 256) set_flag(ws, WS_FB + (cb * 16 + j) * 8, tag);
        KVG(3); DWQ(woap, 1, L_SB); WARQ(0, 0);
        const unsigned fbo = WS_FB + (((16 * (lane % 3) + h) / 3) * 16 + ((lane & 4) && j > 0 ? j - 1 : j)) * 8;
        WAIT_BAR(0);
        STAMP(13);
        KVG(4); DWQ(woap, 2, L_SA); WARQ(1, L_SB - L_SA);
        WAIT_BAR(0);
        STAMP(14);
        asm volatile("global_load_dwordx2 %0, %1, %2 sc1" : "=v"(pa) : "v"(fbo), "s"(ws) : "memory");
        KVG(5); WARQ(2, 0);
        asm volatile("s_waitcnt vmcnt(8)" ::: "memory"); SBAR(); asm volatile("" : "+v"(pa) :: "memory");
        if (__builtin_expect(!__all(pa == tag), 0)) wait_flags_d(ws, fbo, tag);
        {   const char* qo = ws + bB + BR_Q + (size_t)(h * 64 + tile) * 2048; const char* ko = ws + bB + BR_KV + (size_t)(h * 64 + tile) * 4096;
            const unsigned lq2 = lds0 + t * 2048, lk2 = lds0 + t * 4096;
            GLDSC(vl, qo, lq2, L_SB + 20480, 0); GLDSC(vl, qo, lq2, L_SB + 20480, 1024);
            GLDSC(vl, ko, lk2, L_SB + 4096, 0); GLDSC(vl, ko, lk2, L_SB + 4096, 1024); GLDSC(vl, ko, lk2, L_SB + 4096, 2048); GLDSC(vl, ko, lk2, L_SB + 4096, 3072);
            if (t == 0) { const char* po = tile > 0 ? ko - 4096 : ko; GLDSC(vl, po, lds0, L_SB, 0); GLDSC(vl, po, lds0, L_SB, 1024); GLDSC(vl, po, lds0, L_SB, 3072); } }
        WAIT_BAR(0);
        STAMP(15);
        {
            const unsigned bo = lds0 + L_SB + t * 4096 + vl, bq = lds0 + L_SB + 20480 + t * 2048 + vl;
            const bf16x8 bq0 = ldsr128(bq), bq1 = ldsr128(bq + 1024), bk0 = ldsr128(bo + 4096), bk1 = ldsr128(bo + 5120), bv0 = ldsr128(bo + 6144), bv1 = ldsr128(bo + 7168);
            const bf16x8 bp0 = ldsr128(bo), bp1 = ldsr128(bo + 1024), bpv = ldsr128(bo + 3072);
            KVG(6); DWQ(woap, 3, L_SA);
            __builtin_amdgcn_s_setprio(1);
            f32x16 sc = MFMA(bk0, bq0, cbv); sc = MFMA(bk1, bq1, sc);
            f32x16 sp = MFMA(bp0, bq0, pbv); sp = MFMA(bp1, bq1, sp);
            float m = max16(sc);
#pragma unroll
            for (int r = 8; r < 16; r += 2) m = __builtin_fmaxf(__builtin_fmaxf(m, sp[r]), sp[r + 1]);
            m = swap_max(m);
#pragma unroll
            for (int r = 0; r < 16; ++r) sc[r] = ex2(sc[r] - m);
#pragma unroll
            for (int r = 8; r < 16; ++r) sp[r] = ex2(sp[r] - m);
            u32x4 c0, c1, p0, p1; pack16(sc, c0, c1); pack16(sp, p0, p1);
            f32x16 ob = MFMA(bv0, asbf(c0), (f32x16){}); ob = MFMA(bv1, asbf(c1), ob); ob = MFMA(bpv, asbf(p1), ob);
            const float rl = __builtin_amdgcn_rcpf(from_hi(ob[12]));
#pragma unroll
            for (int i = 0; i < 16; ++i) ob[i] *= rl;
            u32x4 e0, e1; pack16(ob, e0, e1); fix_pad(e1, 0u, mhi);
            st16c(rs, e0, vl, bB + BR_O + (tile * 16 + h) * 2048); st16c(rs, e1, vl + 1024, bB + BR_O + (tile * 16 + h) * 2048);
            __builtin_amdgcn_s_setprio(0);
        }
        WAIT_BAR(2);
        STAMP(16);
        KVG(7); DWQ(wobp, 0, L_SB); WARQ(3, 0);
        asm volatile("s_waitcnt vmcnt(15)" ::: "memory");
        if (lane == 0) set_flag(ws, WS_FOB + (tile * 16 + h) * 8, tag);
        const unsigned foo = WS_FOB + (tt * 16 + (lane & 15)) * 8;
        WAIT_BAR(1);
        STAMP(17);
        DWQ(wobp, 1, L_SA);
        WAIT_BAR(0);
        STAMP(18);
        asm volatile("global_load_dwordx2 %0, %1, %2 sc1" : "=v"(pa) : "v"(foo), "s"(ws) : "memory");
        DWQ0(wobp, 2);
        asm volatile("s_waitcnt vmcnt(7)" ::: "memory"); SBAR(); asm volatile("" : "+v"(pa) :: "memory");
        if (__builtin_expect(!__all(pa == tag), 0)) wait_flags_d(ws, foo, tag);
        {   const char* ofb = ws + bB + BR_O + (size_t)tt * 32768 + c * 8192; KV4(ofb, L_OF); KV4(ofb + 4096, L_OF + 4096); }
        unsigned oad = lds0 + L_OF + vl; PIN(oad);
        f32x16 accB = {};
        bf16x8 wf[4], of[4];
#define OPB(B, BK, off, kbase) do { \
        _Pragma("unroll") for (int i = 0; i < 4; ++i) { wf[i] = WFR(B, BK, off, i); of[i] = ldsr128(oad + ((kbase) + i) * 1024); } \
        _Pragma("unroll") for (int i = 0; i < 8; ++i) { SBAR(); accB = MFMA(wf[i & 3], of[i & 3], accB); SBAR(); \
            if (i < 4) { wf[i] = WFR(B, BK, off, i + 4); of[i] = ldsr128(oad + ((kbase) + i + 4) * 1024); } } SBAR(); } while (0)
        BAR_ONLY();
        STAMP(30);
        WAIT_BAR(0);
        STAMP(23);
        DWQ(wobp, 3, L_OA);
        OPB(wsa, wsk, L_SB - L_SA, 0); OPB(wsa, wsk, 0, 8); OPB(wz, wzk, 0, 16);
        if (tid == 256) *(LAS unsigned*)(uintptr_t)(lds0 + L_SCR) = 0u;
        WAIT_BAR(0);
        OPB(wz, wzk, L_OA, 24);
        float gate = __builtin_amdgcn_rcpf(1.0f + ex2(-1.4426950408889634f * glog)); const float g1 = 1.0f - gate;
#pragma unroll
        for (int i = 0; i < 16; ++i) accB[i] *= g1;
        unsigned oo = (tt * 32 + r32) * 896 + (col0 + 32 * c) * 2 + hi * 16;
        PIN(accB); PIN(gate); PIN(oo);
        STAMP(24);
        if (t == 0) { wait_flags_d(ws, WS_FOA + (tt * 16 + (lane & 15)) * 8, tag); lds_arrive(lds0 + L_SCR, lane); } else lds_wait(lds0 + L_SCR, 1);
        STAMP(25);
        {
            const char* ofa = ws + WS_BR + BR_O + (size_t)tt * 32768 + c * 4096; const unsigned lp = lds0 + c * 4096;
            GLDSC(vl, ofa, lp, 0, 0); GLDSC(vl, ofa, lp, 0, 1024); GLDSC(vl, ofa, lp, 0, 2048); GLDSC(vl, ofa, lp, 0, 3072);
            GLDSC(vl, ofa + 16384, lp, 16384, 0); GLDSC(vl, ofa + 16384, lp, 16384, 1024); GLDSC(vl, ofa + 16384, lp, 16384, 2048); GLDSC(vl, ofa + 16384, lp, 16384, 3072); }
        WAIT_BAR(4);
        STAMP(26);
        f32x16 acc = {};
        {   unsigned ob_ = lds0 + vl; PIN(ob_);
            bf16x8 og[8];
#pragma unroll
            for (int i = 0; i < 8; ++i) og[i] = ldsr128(ob_ + i * 1024);
#pragma unroll
            for (int i = 0; i < 32; ++i) { if (i == 8) { WAIT_BAR(0); } SBAR(); acc = MFMA(war[i], og[i & 7], acc); SBAR(); if (i < 24) og[i & 7] = ldsr128(ob_ + (i + 8) * 1024); }
            SBAR();
        }
        if (bid == 0 && tid == 256) __hip_atomic_store((GAS u64*)(ws + WS_EPOCH), ep + 1, RLX_AGENT);
        if (cg < 3 || c >= 2) {
#pragma unroll
            for (int i = 0; i < 16; ++i) acc[i] = acc[i] * gate + accB[i];
            u32x4 s0_, s1_; pack16(acc, s0_, s1_);
            auto r0 = __builtin_amdgcn_permlane32_swap(s0_[0], s0_[2], false, false), r1 = __builtin_amdgcn_permlane32_swap(s0_[1], s0_[3], false, false);
            auto r2 = __builtin_amdgcn_permlane32_swap(s1_[0], s1_[2], false, false), r3 = __builtin_amdgcn_permlane32_swap(s1_[1], s1_[3], false, false);
            char* op = out + oo;
            *(GAS u32x4*)op = (u32x4){r0[0], r1[0], r0[1], r1[1]};
            *(GAS u32x4*)(op + 32) = (u32x4){r2[0], r3[0], r2[1], r3[1]};
        }
    }
    STAMPF(27);
    asm volatile(".Lfk_end:");
}

extern "C" void kernel_launch(void* const* d_in, const int* in_sizes, int n_in,
                              void* d_out, int out_size, void* d_ws, size_t ws_size,
                              hipStream_t stream) {
    (void)in_sizes; (void)n_in; (void)out_size; (void)ws_size;
    (void)hipFuncSetAttribute((const void*)fused_kernel, hipFuncAttributeMaxDynamicSharedMemorySize, LDS_BYTES);
    fused_kernel<<<256, 512, LDS_BYTES, stream>>>((const char*)d_in[0], (const char*)d_in[1], (const char*)d_in[2], (const char*)d_in[4],
                                                  (const char*)d_in[5], (const char*)d_in[6], (char*)d_ws, (char*)d_out);
}
```

```cpp
#include <hip/hip_runtime.h>
#include <hip/hip_bf16.h>
#include <stdint.h>

typedef short bf16x8 __attribute__((ext_vector_type(8)));
typedef short s16x4 __attribute__((ext_vector_type(4)));
typedef float f32x16 __attribute__((ext_vector_type(16)));
typedef float f32x4 __attribute__((ext_vector_type(4)));
typedef float f32x2 __attribute__((ext_vector_type(2)));
typedef unsigned u32x4 __attribute__((ext_vector_type(4)));
typedef unsigned u32x2 __attribute__((ext_vector_type(2)));
typedef __bf16 bf2_t __attribute__((ext_vector_type(2)));
typedef unsigned long long u64;
#define LAS __attribute__((address_space(3)))
#define GAS __attribute__((address_space(1)))
#define DI __device__ __forceinline__
#define MFMA(a, b, c) __builtin_amdgcn_mfma_f32_32x32x16_bf16((a), (b), (c), 0, 0, 0)
#define SBAR() __builtin_amdgcn_sched_barrier(0)
#define PIN(x) asm volatile("" : "+v"(x))
#define RLX_AGENT __ATOMIC_RELAXED, __HIP_MEMORY_SCOPE_AGENT

constexpr float QSCALE = 0.18898223650461362f * 1.4426950408889634f;

constexpr unsigned WS_EPOCH = 0;
constexpr unsigned WS_FA = 4096;
constexpr unsigned WS_FB = WS_FA + 2048;
constexpr unsigned WS_FOA = WS_FB + 2048;
constexpr unsigned WS_FOB = WS_FOA + 8192;
constexpr unsigned WS_BR = 65536;
constexpr unsigned BR_Q = 0, BR_KV = 2097152, BR_O = 6291456, BR_SZ = 8388608;
constexpr unsigned WS_TOTAL = WS_BR + 2 * BR_SZ;

constexpr unsigned L_XB = 0;
constexpr unsigned L_KW = 98304;
constexpr unsigned L_GT = 155648;
constexpr unsigned L_CNT = 159232;
constexpr unsigned L_GL = 159744;
constexpr unsigned L_Q = 139264;
constexpr unsigned L_RING = 0;
constexpr unsigned L_SA = 98304, L_SB = 126976;
constexpr unsigned L_OF = 65536;
constexpr unsigned L_OA = 32768;
constexpr unsigned L_SCR = 155648;
constexpr unsigned LDS_BYTES = 163840;

DI unsigned cvtpk(float lo, float hi) { f32x2 v = {lo, hi}; return __builtin_bit_cast(unsigned, __builtin_convertvector(v, bf2_t)); }
#define GLDS(voff, sbase, lds_s, LC, OFF) asm volatile("s_add_i32 m0, %2, %3\n\ts_nop 0\n\tglobal_load_lds_dwordx4 %0, %1 offset:%4" \
        :: "v"(voff), "s"(sbase), "s"(__builtin_amdgcn_readfirstlane(lds_s)), "n"((int)(LC)), "n"((int)(OFF)) : "memory", "scc")
#define GLDSC(voff, sbase, lds_s, LC, OFF) asm volatile("s_add_i32 m0, %2, %3\n\ts_nop 0\n\tglobal_load_lds_dwordx4 %0, %1 offset:%4 sc1" \
        :: "v"(voff), "s"(sbase), "s"(__builtin_amdgcn_readfirstlane(lds_s)), "n"((int)(LC)), "n"((int)(OFF)) : "memory", "scc")
#define GLDS_IF(sel, voff, sbase, lds_s, LC, OFF) asm volatile("s_cmp_lg_u32 %5, 0\n\ts_cbranch_scc1 1f\n\ts_add_i32 m0, %2, %3\n\ts_nop 0\n\tglobal_load_lds_dwordx4 %0, %1 offset:%4\n1:" \
        :: "v"(voff), "s"(sbase), "s"(__builtin_amdgcn_readfirstlane(lds_s)), "n"((int)(LC)), "n"((int)(OFF)), "s"(sel) : "memory", "scc")
#define WAIT_BAR(N) asm volatile("s_waitcnt vmcnt(" #N ") lgkmcnt(0)\n\ts_barrier" ::: "memory")
#define BAR_ONLY() asm volatile("s_waitcnt lgkmcnt(0)\n\ts_barrier" ::: "memory")
DI bf16x8 ldsr128(unsigned a) { return *(const LAS bf16x8*)(uintptr_t)a; }
typedef short v4i16_t __attribute__((ext_vector_type(4)));
DI s16x4 ldstr(unsigned a) { return __builtin_bit_cast(s16x4, __builtin_amdgcn_ds_read_tr16_b64_v4i16((LAS v4i16_t*)(uintptr_t)a)); }
DI bf16x8 wfrag(unsigned a0, unsigned a1) { s16x4 lo = ldstr(a0), hi = ldstr(a1); return __builtin_shufflevector(lo, hi, 0, 1, 2, 3, 4, 5, 6, 7); }
DI bf16x8 asbf(u32x4 v) { return __builtin_bit_cast(bf16x8, v); }
DI float ex2(float v) { return __builtin_amdgcn_exp2f(v); }
DI float swap_max(float m) {
    auto rr = __builtin_amdgcn_permlane32_swap(__float_as_uint(m), __float_as_uint(m), false, false);
    return __builtin_fmaxf(__uint_as_float(rr[0]), __uint_as_float(rr[1]));
}
DI float from_hi(float v) {
    auto rr = __builtin_amdgcn_permlane32_swap(__float_as_uint(v), __float_as_uint(v), false, false);
    return __uint_as_float(rr[1]);
}
DI void pack16(const f32x16& a, u32x4& s0, u32x4& s1) {
    s0 = (u32x4){cvtpk(a[0], a[1]), cvtpk(a[2], a[3]), cvtpk(a[4], a[5]), cvtpk(a[6], a[7])};
    s1 = (u32x4){cvtpk(a[8], a[9]), cvtpk(a[10], a[11]), cvtpk(a[12], a[13]), cvtpk(a[14], a[15])};
}
DI float max16(const f32x16& s) {
    float m = __builtin_fmaxf(s[0], s[1]);
#pragma unroll
    for (int r = 2; r < 16; r += 2) m = __builtin_fmaxf(__builtin_fmaxf(m, s[r]), s[r + 1]);
    return m;
}
typedef __amdgpu_buffer_rsrc_t rsrc_t;
DI void st16c(rsrc_t r, u32x4 v, unsigned voff, unsigned soff) { __builtin_amdgcn_raw_buffer_store_b128(v, r, voff, soff, 16); }
DI bf16x8 ld16c(rsrc_t r, unsigned voff, unsigned soff) { return asbf(__builtin_amdgcn_raw_buffer_load_b128(r, voff, soff, 16)); }
DI void wait_flags_d(const char* ws, unsigned off, u64 tag) {
#pragma unroll 1
    for (int it = 0; it < 40000; ++it) {
        u64 a;
        asm volatile("global_load_dwordx2 %0, %1, %2 sc1\n\ts_waitcnt vmcnt(0)" : "=&v"(a) : "v"(off), "s"(ws) : "memory");
        if (__all(a == tag)) break;
        __builtin_amdgcn_s_sleep(4);
    }
}
DI void set_flag(char* ws, unsigned off, u64 tag) { __hip_atomic_store((GAS u64*)(ws + off), tag, RLX_AGENT); }


DI void fix_v(u32x4& s0, u32x4& s1, unsigned m28) {
    unsigned keep = (m28 & 1u) - 1u, ones = m28 & 0x3F803F80u; PIN(keep); PIN(ones);
#pragma unroll
    for (int i = 0; i < 4; ++i) { s0[i] = (s0[i] & keep) | ones; s1[i] = (s1[i] & keep) | ones; }
}
DI void lds_arrive(unsigned a, int lane) { if (lane == 0) __hip_atomic_fetch_add((LAS unsigned*)(uintptr_t)a, 1u, __ATOMIC_RELAXED, __HIP_MEMORY_SCOPE_WORKGROUP); }
DI void lds_wait(unsigned a, unsigned n) {
#pragma unroll 1
    for (int it = 0; it < 2000000; ++it) {
        if (__builtin_amdgcn_readfirstlane(__hip_atomic_load((LAS unsigned*)(uintptr_t)a, __ATOMIC_RELAXED, __HIP_MEMORY_SCOPE_WORKGROUP)) >= n) break;
        __builtin_amdgcn_s_sleep(1);
    }
    asm volatile("" ::: "memory");
}
DI void fix_pad(u32x4& s1, unsigned pad, unsigned mhi) { s1[2] = (s1[2] & ~mhi) | (pad & mhi); s1[3] &= ~mhi; }
template <bool V>
DI void pack_unit(f32x16 a, int u, unsigned m28, unsigned mhi, u32x4& s0, u32x4& s1) {
    if (!V) {
        const bool isq = u < 16;
        const float sc = isq ? QSCALE : 1.0f;
#pragma unroll
        for (int i = 0; i < 16; ++i) a[i] *= sc;
        pack16(a, s0, s1);
        fix_pad(s1, isq ? 0u : 0x00003F80u, mhi);
    } else { pack16(a, s0, s1); fix_v(s0, s1, m28); }
}
#define MKTAG() do { u64 z = ((u64)__builtin_amdgcn_readfirstlane((unsigned)(ep >> 32)) << 32) | (unsigned)__builtin_amdgcn_readfirstlane((unsigned)ep); \
        z += 0x9E3779B97F4A7C15ull; z = (z ^ (z >> 30)) * 0xBF58476D1CE4E5B9ull; z = (z ^ (z >> 27)) * 0x94D049BB133111EBull; tag = z ^ (z >> 31); } while (0)
#define XB(i) (L_XB + (i) * 16384)
#define KW(i) (L_KW + (i) * 8192)
#define MM(VV, acc, wf, xfr) do { if (VV) acc = MFMA(xfr, wf, acc); else acc = MFMA(wf, xfr, acc); } while (0)
__device__ constexpr unsigned so_of(int c) { return c == 0 ? KW(0) : (c == 6 ? KW(2) : XB(c)); }
__device__ constexpr unsigned xo_of(int c) { return c == 6 ? XB(0) : XB(c); }

template <bool VK>
DI void stage1(bf16x8 (&xf)[28], const unsigned (&xa)[4], const unsigned (&xh)[4], unsigned ka0, unsigned ka1, rsrc_t rs, int h, int tile, int lane, unsigned m28, unsigned mhi,
               unsigned gv0, unsigned gv1, unsigned dmc, unsigned dmi, u64 ep, u64& tag, unsigned lds0) {
    f32x16 acc0 = {}, acc1 = {};
    bf16x8 w0[2], w1[2];
#define XR(s) xf[s] = ldsr128(xo_of((s) >> 2) >= 65536 ? xh[(s) & 3] + (xo_of((s) >> 2) - 65536) : xa[(s) & 3] + xo_of((s) >> 2))
#define KF(base, s) wfrag((base) + (s) * 2048, (base) + (s) * 2048 + 512)
    XR(0); w0[0] = KF(ka0, 0); w1[0] = KF(ka1, 0); XR(1); w0[1] = KF(ka0, 1); w1[1] = KF(ka1, 1);
#pragma unroll
    for (int s = 0; s < 28; ++s) {
        if ((s & 3) == 2 && s < 26) { BAR_ONLY(); }
        if (s == 20) {
            asm volatile("s_waitcnt vmcnt(0)" ::: "memory"); SBAR();
            asm volatile("" : "+v"(gv0), "+v"(gv1), "+v"(ep) : "v"(dmc), "v"(dmi) : "memory");
            const int tid = threadIdx.x;
            { u32x2 e = {gv0, 0u}; *(LAS u32x2*)(uintptr_t)(lds0 + L_GT + tid * 8) = e; }
            if (tid < 192) { u32x2 e = {gv1, 0u}; *(LAS u32x2*)(uintptr_t)(lds0 + L_GT + 2048 + tid * 8) = e; }
            if (tid == 0) *(LAS u32x2*)(uintptr_t)(lds0 + L_CNT) = (u32x2){0u, 0u};
            MKTAG();
        }
        SBAR();
        MM(VK, acc0, w0[s & 1], xf[s]); SBAR();
        if (s + 2 < 28) { XR(s + 2); w0[s & 1] = KF(ka0, s + 2); }
        SBAR();
        MM(VK, acc1, w1[s & 1], xf[s]); SBAR();
        if (s + 2 < 28) w1[s & 1] = KF(ka1, s + 2);
    }
    SBAR();
    u32x4 s0, s1;
    pack16(acc0, s0, s1); if (!VK) fix_pad(s1, 0x00003F80u, mhi); else fix_v(s0, s1, m28);
    const unsigned off = WS_BR + BR_KV + ((2 * h & 15) * 64 + tile) * 4096 + (VK ? 2048 : 0);
    st16c(rs, s0, lane * 16, off); st16c(rs, s1, lane * 16 + 1024, off);
    pack16(acc1, s0, s1); if (!VK) fix_pad(s1, 0x00003F80u, mhi); else fix_v(s0, s1, m28);
    st16c(rs, s0, lane * 16, off + 262144); st16c(rs, s1, lane * 16 + 1024, off + 262144);
}
template <bool V01, bool V2>
DI void stage2(const bf16x8 (&xf)[28], unsigned ql, unsigned qh, unsigned gl, bool spec, unsigned wlo, unsigned whi, char* ws, rsrc_t rs, unsigned lds0,
               int h, int cb, int j, int t, int lane, u64 tag, u32x4 (&bp)[6], unsigned m28, unsigned mhi) {
    f32x16 aq = {}, acc0 = {}, acc1 = {}, acc2 = {};
    bf16x8 wq[2], w0[2], w1[2], w2[2];
    const unsigned gstride = spec ? 32u : 256u;
    bool sent = false;
#define SADDR(lo, hi_, c) (so_of(c) >= 65536 ? (hi_) + (so_of(c) - 65536) : (lo) + so_of(c))
#define WF(s, u) wfrag(SADDR(wlo, whi, (s) >> 2) + ((s) & 3) * 3072 + (u) * 56, SADDR(wlo, whi, (s) >> 2) + ((s) & 3) * 3072 + 768 + (u) * 56)
#define QR(s) do { const unsigned a_ = spec ? gl + 128 * (s) : SADDR(ql, qh, (s) >> 2) + ((s) & 3) * 1024; wq[(s) & 1] = wfrag(a_, a_ + gstride); } while (0)
    QR(0); w0[0] = WF(0, 0); w1[0] = WF(0, 1); w2[0] = WF(0, 2); QR(1); w0[1] = WF(1, 0); w1[1] = WF(1, 1); w2[1] = WF(1, 2);
#pragma unroll
    for (int s = 0; s < 28; ++s) {
        if (s == 6 || s == 10 || s == 14 || s == 18) { BAR_ONLY(); }
        if (s >= 7 && s <= 15) {
            if (s == 15) asm volatile("s_waitcnt vmcnt(0)" ::: "memory");
            if (!sent && (s == 15 || (__builtin_amdgcn_s_getreg(7 | (3 << 11)) & 15) == 0)) {
                sent = true; asm volatile("" ::: "memory");
                if (lane == 0) { if (__hip_atomic_fetch_add((LAS unsigned*)(uintptr_t)(lds0 + L_CNT), 1u, __ATOMIC_RELAXED, __HIP_MEMORY_SCOPE_WORKGROUP) == 3u) set_flag(ws, WS_FA + (h * 16 + j) * 8, tag); }
            }
        }
        SBAR();
        aq = MFMA(wq[s & 1], xf[s], aq); SBAR();
        if (s + 2 < 28) QR(s + 2);
        SBAR();
        MM(V01, acc0, w0[s & 1], xf[s]); SBAR();
        if (s + 2 < 28) w0[s & 1] = WF(s + 2, 0);
        SBAR();
        MM(V01, acc1, w1[s & 1], xf[s]); SBAR();
        if (s + 2 < 28) w1[s & 1] = WF(s + 2, 1);
        SBAR();
        MM(V2, acc2, w2[s & 1], xf[s]); SBAR();
        if (s + 2 < 28) w2[s & 1] = WF(s + 2, 2);
    }
    SBAR();
    {
        if (lane >> 5) *(LAS float*)(uintptr_t)(lds0 + L_GL + t * 128 + (lane & 31) * 4) = aq[12];
#pragma unroll
        for (int i = 0; i < 16; ++i) aq[i] *= QSCALE;
        u32x4 s0, s1; pack16(aq, s0, s1);
        fix_pad(s1, 0u, mhi);
        *(LAS u32x4*)(uintptr_t)(lds0 + L_Q + t * 2048 + lane * 16) = s0; *(LAS u32x4*)(uintptr_t)(lds0 + L_Q + t * 2048 + 1024 + lane * 16) = s1;
    }
    pack_unit<V01>(acc0, 3 * cb, m28, mhi, bp[0], bp[1]); pack_unit<V01>(acc1, 3 * cb + 1, m28, mhi, bp[2], bp[3]); pack_unit<V2>(acc2, 3 * cb + 2, m28, mhi, bp[4], bp[5]);
}

__global__ __launch_bounds__(512, 2) void fused_kernel(const char* __restrict__ x, const char* __restrict__ wa, const char* __restrict__ woa,
                                                       const char* __restrict__ wb, const char* __restrict__ wob, const char* __restrict__ gw,
                                                       char* ws, char* __restrict__ out) {
    extern __shared__ __attribute__((aligned(16))) char smem[];
    const unsigned lds0 = (unsigned)(uintptr_t)(LAS char*)smem;
    const int tid = threadIdx.x, lane = tid & 63, w = __builtin_amdgcn_readfirstlane(tid >> 6), t = w & 3, g = w >> 2;
    const int bid = blockIdx.x, xg = bid & 7, idx = bid >> 3;
    const int h = (xg >> 1) * 4 + (idx & 3), j = (xg & 1) * 8 + (idx >> 2);
    const int r32 = lane & 31, hi = lane >> 5;
    const rsrc_t rs = __builtin_amdgcn_make_buffer_rsrc(ws, 0, WS_TOTAL, 0x00020000);
    const unsigned vl = lane * 16;
    asm volatile("" :: "s"(x), "s"(wa), "s"(woa), "s"(wb), "s"(wob), "s"(gw), "s"(ws), "s"(out));
    u64 pc; unsigned len;
    asm volatile("s_getpc_b64 %0\n.Lfk_ref:\n\ts_mov_b32 %1, .Lfk_end-.Lfk_ref" : "=s"(pc), "=s"(len));
    const int cb = (int)((0xCFEDBA8967542103ull >> (4 * h)) & 15);
    const int tile = 4 * j + t;
    const int rot = (j + 2 * (h & 3)) % 7;
#define CK(p) ((p) + rot >= 7 ? (p) + rot - 7 : (p) + rot)
    const unsigned vxe = (lane >> 3) * 896 + (((lane & 7) ^ (lane >> 4)) << 4), lx = lds0 + t * 4096;
    const char* xb = x + (size_t)(j * 128 + t * 32) * 896;
    unsigned dmi;
    {
        const unsigned co = (unsigned)((idx * 8 + w) * 2 + (lane & 1)) * 128u;
        asm volatile("global_load_ubyte %0, %1, %2" : "=v"(dmi) : "v"(co < len ? co : 0u), "s"(pc) : "memory"); }
    {
        const char* p_ = xb + rot * 128;
        GLDS_IF(g, vxe, p_, lx, XB(0), 0); GLDS_IF(g, (vxe ^ 64u) + 7168, p_, lx, XB(0) + 1024, 0); GLDS_IF(g, vxe + 14336, p_, lx, XB(0) + 2048, 0); GLDS_IF(g, (vxe ^ 64u) + 21504, p_, lx, XB(0) + 3072, 0); }
    u64 ep, tag;
    unsigned m28 = r32 == 28 ? ~0u : (r32 > 28 ? 1u : 0u), mhi = hi ? ~0u : 0u; PIN(m28); PIN(mhi);
    if (g == 0) {
        unsigned gv0, gv1, dmc;
        {
            const int pl = lane & 15;
            const char* tp = pl < 9 ? ws + (size_t)pl * 2097152 + 32768 : (pl == 9 ? (const char*)out : (pl == 10 ? woa : (pl == 11 ? wob : wb)));
            asm volatile("global_load_ubyte %0, %1, off sc1" : "=v"(dmc) : "v"(tp) : "memory");
        }
        asm volatile("global_load_ushort %0, %1, %2" : "=v"(gv0) : "v"((CK(t) * 64 + lane) * 2), "s"(gw) : "memory");
        asm volatile("global_load_ushort %0, %1, %2" : "=v"(gv1) : "v"((CK(t < 3 ? 4 + t : 6) * 64 + lane) * 2), "s"(gw) : "memory");
        asm volatile("global_load_dwordx2 %0, %1, %2 sc1" : "=v"(ep) : "v"(0u), "s"(ws) : "memory");
        const int fr = (r32 >> 1) & 7;
        unsigned xa[4], xh[4];
#pragma unroll
        for (int ks = 0; ks < 4; ++ks) { xa[ks] = lds0 + (4 * t + (r32 >> 3)) * 1024 + (r32 & 7) * 128 + (((2 * ks + hi) ^ fr) << 4); xh[ks] = xa[ks] + 65536; PIN(xa[ks]); PIN(xh[ks]); }
        const int q4 = (lane & 15) >> 2, p4 = lane & 3, ch = (lane >> 4) & 1;
        const unsigned o0 = ch * 32 + p4 * 8, o1 = o0 + 56, sw = (q4 >> 1) << 6;
        unsigned ka0 = lds0 + L_KW + (8 * hi + q4) * 128 + (o0 ^ sw), ka1 = lds0 + L_KW + (8 * hi + q4) * 128 + (o1 ^ sw); PIN(ka0); PIN(ka1);
        unsigned ql = lds0 + (8 * hi + q4) * 64 + ch * 32 + p4 * 8 + (h & 1) * 8, qh = ql + 65536; PIN(ql); PIN(qh);
        unsigned wlo = lds0 + 4096 + (8 * hi + q4) * 192 + ch * 32 + p4 * 8 + (cb & 1) * 8, whi = wlo + 65536; PIN(wlo); PIN(whi);
        const bool spec = ch == 1 && p4 == 3;
        const unsigned gl = lds0 + L_GT + 64 * hi + 8 * q4;
        bf16x8 xf[28]; u32x4 bp[6];
        WAIT_BAR(4);
        if (h < 8) stage1<false>(xf, xa, xh, ka0, ka1, rs, h, tile, lane, m28, mhi, gv0, gv1, dmc, dmi, ep, tag, lds0); else stage1<true>(xf, xa, xh, ka0, ka1, rs, h, tile, lane, m28, mhi, gv0, gv1, dmc, dmi, ep, tag, lds0);
        BAR_ONLY();
        if (cb <= 9) stage2<false, false>(xf, ql, qh, gl, spec, wlo, whi, ws, rs, lds0, h, cb, j, t, lane, tag, bp, m28, mhi);
        else if (cb == 10) stage2<false, true>(xf, ql, qh, gl, spec, wlo, whi, ws, rs, lds0, h, cb, j, t, lane, tag, bp, m28, mhi);
        else stage2<true, true>(xf, ql, qh, gl, spec, wlo, whi, ws, rs, lds0, h, cb, j, t, lane, tag, bp, m28, mhi);
        BAR_ONLY();
#pragma unroll
        for (int i = 0; i < 3; ++i) { const int u = 3 * cb + i;
            const unsigned off = WS_BR + BR_SZ + (u < 16 ? BR_Q + (u * 64 + tile) * 2048 : BR_KV + ((u & 15) * 64 + tile) * 4096 + (u >= 32 ? 2048 : 0));
            st16c(rs, bp[2 * i], vl, off); st16c(rs, bp[2 * i + 1], vl + 1024, off); }
        const int qp = t & 1, kh = t >> 1;
        const unsigned qad = lds0 + L_Q + qp * 4096 + kh * 2048 + vl, qbd = qad ^ 2048u;
        const bf16x8 qa0 = ldsr128(qad), qb0 = ldsr128(qbd);
        u32x4 qa1 = __builtin_bit_cast(u32x4, ldsr128(qad + 1024)), qb1 = __builtin_bit_cast(u32x4, ldsr128(qbd + 1024));
        unsigned ac = lds0 + L_RING + kh * 4096 + vl, an = ac + 32768, af = ac + 65536;
        f32x16 oa = {}, ob = {}, sA, sB; u32x4 pA0 = {}, pA1 = {}, pB0 = {}, pB1 = {};
        bf16x8 k0 = ldsr128(ac), k1 = ldsr128(ac + 1024), v0 = {}, v1 = {}, kn0, kn1;
        float mra, mrb;
        {
            sA = MFMA(k0, qa0, (f32x16){}); sB = MFMA(k0, qb0, (f32x16){});
            f32x16 ta = MFMA(k1, asbf(qa1), sA); sB = MFMA(k1, asbf(qb1), sB);
            const unsigned na = cvtpk(-swap_max(max16(ta)), 0.f), nb = cvtpk(-swap_max(max16(sB)), 0.f);
            mra = -__uint_as_float(na << 16); mrb = -__uint_as_float(nb << 16);
            qa1[2] = (qa1[2] & ~mhi) | (na & mhi); qb1[2] = (qb1[2] & ~mhi) | (nb & mhi);
            sA = MFMA(k1, asbf(qa1), sA);
        }
#define GRP(S, b, SP, bp, PP, jp) do { PP[jp] = cvtpk(SP[bp], SP[bp + 1]); PP[jp + 1] = cvtpk(SP[bp + 2], SP[bp + 3]); SBAR(); \
        S[b] = ex2(S[b]); S[b + 1] = ex2(S[b + 1]); S[b + 2] = ex2(S[b + 2]); S[b + 3] = ex2(S[b + 3]); PIN(PP); PIN(S); SBAR(); } while (0)
#define STEP2(KC0, KC1, KN0, KN1, VP1, VC1, ka, va) do { \
        KN0 = ldsr128(ka); ob = MFMA(v0, asbf(pB0), ob); SBAR(); GRP(sA, 0, sB, 12, pB1, 2); KN1 = ldsr128((ka) + 1024); \
        sB = MFMA(KC0, qb0, (f32x16){}); SBAR(); VC1 = ldsr128((va) + 3072); GRP(sA, 4, sA, 0, pA0, 0); v0 = ldsr128((va) + 2048); \
        sB = MFMA(KC1, asbf(qb1), sB); SBAR(); GRP(sA, 8, sA, 4, pA0, 2); \
        ob = MFMA(VP1, asbf(pB1), ob); SBAR(); GRP(sA, 12, sA, 8, pA1, 0); \
        oa = MFMA(v0, asbf(pA0), oa); SBAR(); GRP(sB, 0, sA, 12, pA1, 2); \
        sA = MFMA(KN0, qa0, (f32x16){}); SBAR(); GRP(sB, 4, sB, 0, pB0, 0); \
        sA = MFMA(KN1, asbf(qa1), sA); SBAR(); GRP(sB, 8, sB, 4, pB0, 2); \
        oa = MFMA(VC1, asbf(pA1), oa); SBAR(); GRP(sB, 12, sB, 8, pB1, 0); \
    } while (0)
        sB = (f32x16){};
        bf16x8 v1n;
#pragma unroll 1
        for (int gr = 0; gr < 8; ++gr) {
            STEP2(k0, k1, kn0, kn1, v1, v1n, ac + 8192, ac);
            STEP2(kn0, kn1, k0, k1, v1n, v1, ac + 16384, ac + 8192);
            if (gr == 0) asm volatile("s_barrier" ::: "memory"); else asm volatile("s_waitcnt vmcnt(0)\n\ts_barrier" ::: "memory");
            SBAR();
            STEP2(k0, k1, kn0, kn1, v1, v1n, ac + 24576, ac + 16384);
            STEP2(kn0, kn1, k0, k1, v1n, v1, an, ac + 24576);
            const unsigned tmp = ac; ac = an; an = af; af = tmp;
        }
        pB1[2] = cvtpk(sB[12], sB[13]); pB1[3] = cvtpk(sB[14], sB[15]);
        ob = MFMA(v0, asbf(pB0), ob); ob = MFMA(v1, asbf(pB1), ob);
        if (__builtin_expect(__any(hi && !(oa[12] < 1.0e27f && ob[12] < 1.0e27f && oa[12] > 1.0e-27f && ob[12] > 1.0e-27f)), 0)) {
            qa1[2] &= ~mhi; qb1[2] &= ~mhi;
            mra = -__builtin_inff(); mrb = mra; oa = (f32x16){}; ob = (f32x16){};
            const unsigned kb = WS_BR + BR_KV + h * 262144 + kh * 4096;
#pragma unroll 1
            for (int i = 0; i < 32; ++i) {
                const unsigned so = kb + i * 8192;
                const bf16x8 a0 = ld16c(rs, vl, so), a1 = ld16c(rs, vl + 1024, so), b0 = ld16c(rs, vl + 2048, so), b1 = ld16c(rs, vl + 3072, so);
#define ONLINE(Q0, Q1, O, M) do { f32x16 s_ = MFMA(a0, Q0, (f32x16){}); s_ = MFMA(a1, asbf(Q1), s_); \
                const float mn = __builtin_fmaxf(M, swap_max(max16(s_))), al = ex2(M - mn); M = mn; \
                _Pragma("unroll") for (int r = 0; r < 16; ++r) { s_[r] = ex2(s_[r] - mn); O[r] *= al; } \
                u32x4 e0, e1; pack16(s_, e0, e1); O = MFMA(b0, asbf(e0), O); O = MFMA(b1, asbf(e1), O); } while (0)
                ONLINE(qa0, qa1, oa, mra); ONLINE(qb0, qb1, ob, mrb);
            }
        }
        {   const unsigned cw_ = lds0 + L_OA + t * 4096 + vl;
#define PUTP(O, M) do { O[13] = __uint_as_float((__float_as_uint(O[13]) & ~mhi) | (__float_as_uint(M) & mhi)); \
            _Pragma("unroll") for (int i = 0; i < 4; ++i) *(LAS f32x4*)(uintptr_t)(cw_ + i * 1024) = (f32x4){O[4 * i], O[4 * i + 1], O[4 * i + 2], O[4 * i + 3]}; } while (0)
            PUTP(ob, mrb);
        }
        BAR_ONLY();
        {   const unsigned cbs = lds0 + L_OA + (t ^ 2) * 4096 + vl;
            f32x16 o1;
#pragma unroll
            for (int i = 3; i >= 0; --i) { const f32x4 e = *(const LAS f32x4*)(uintptr_t)(cbs + i * 1024); o1[4 * i] = e[0]; o1[4 * i + 1] = e[1]; o1[4 * i + 2] = e[2]; o1[4 * i + 3] = e[3]; }
            const float m1 = from_hi(o1[13]);
            const float mm = __builtin_fmaxf(mra, m1), a0 = ex2(mra - mm), a1 = ex2(m1 - mm);
            const float rl = __builtin_amdgcn_rcpf(from_hi(oa[12] * a0 + o1[12] * a1)), f0 = a0 * rl, f1 = a1 * rl;
            f32x16 v;
#pragma unroll
            for (int i = 0; i < 16; ++i) v[i] = oa[i] * f0 + o1[i] * f1;
            u32x4 e0, e1; pack16(v, e0, e1);
            fix_pad(e1, 0u, mhi);
            const int ot = 4 * j + 2 * qp + kh;
            const unsigned so = WS_BR + BR_O + (ot * 16 + h) * 2048;
            st16c(rs, e0, vl, so); st16c(rs, e1, vl + 1024, so);
            asm volatile("s_waitcnt lgkmcnt(0)\n\ts_barrier\n\ts_waitcnt vmcnt(0)" ::: "memory");
            if (lane == 0) set_flag(ws, WS_FOA + (ot * 16 + h) * 8, tag);
        }
    } else {
        const unsigned vx[4] = {vxe, (vxe ^ 64u) + 7168, vxe + 14336, (vxe ^ 64u) + 21504};
        const unsigned kc = (lane & 7) ^ (((lane >> 4) & 1) << 2);
        const unsigned vk0 = (lane >> 3) * 2688 + (kc > 6 ? 6 : kc) * 16, vk1 = vk0 + 8 * 2688;
        const char* wkb = wa + 896 + 112 * h + (size_t)(16 * t) * 2688;
        const unsigned lk = lds0 + t * 2048;
#define DMA_K(ck) do { const char* p_ = wkb + (size_t)CK(ck) * 64 * 2688; GLDS(vk0, p_, lk, KW(ck), 0); GLDS(vk1, p_, lk, KW(ck) + 1024, 0); } while (0)
#define DMA_X(cx) do { const char* p_ = xb + CK(cx) * 128; GLDS(vx[0], p_, lx, xo_of(cx), 0); GLDS(vx[1], p_, lx, xo_of(cx) + 1024, 0); \
        GLDS(vx[2], p_, lx, xo_of(cx) + 2048, 0); GLDS(vx[3], p_, lx, xo_of(cx) + 3072, 0); } while (0)
        asm volatile("global_load_dwordx2 %0, %1, %2 sc1" : "=v"(ep) : "v"(0u), "s"(ws) : "memory");
        DMA_K(0); DMA_X(1); DMA_K(1);
        unsigned vw[3];
#pragma unroll
        for (int n = 0; n < 3; ++n) { const unsigned L = 64 * n + lane, row = (L * 43691u) >> 19, cc = L - row * 12; vw[n] = row * 2688 + (cc > 10 ? 10 : cc) * 16; }
        const unsigned vq = (lane >> 2) * 2688 + (lane & 3) * 16;
        const char* wqb = wa + (56 * h - 8 * (h & 1)) + (size_t)(16 * t) * 2688;
        const char* wbb = wb + (168 * cb - 8 * (cb & 1)) + (size_t)(16 * t) * 2688;
        const unsigned lq = lds0 + t * 1024, lw = lds0 + 4096 + t * 3072;
#define DMA_S(ck) do { GLDS(vq, wqb + (size_t)CK(ck) * 64 * 2688, lq, so_of(ck), 0); const char* p_ = wbb + (size_t)CK(ck) * 64 * 2688; \
        GLDS(vw[0], p_, lw, so_of(ck), 0); GLDS(vw[1], p_, lw, so_of(ck) + 1024, 0); GLDS(vw[2], p_, lw, so_of(ck) + 2048, 0); } while (0)
        WAIT_BAR(6); asm volatile("" :: "v"(dmi)); DMA_X(2); DMA_K(2);
        WAIT_BAR(6); DMA_X(3); DMA_K(3);
        WAIT_BAR(6); DMA_X(4); DMA_K(4);
        WAIT_BAR(6); DMA_X(5); DMA_K(5); DMA_S(0);
        WAIT_BAR(10); DMA_X(6); DMA_K(6); DMA_S(1);
        WAIT_BAR(14);
        WAIT_BAR(4); DMA_S(2);
        asm volatile("" : "+v"(ep)); MKTAG();
        const unsigned fo = WS_FA + ((((lane >> 4) & 1) * 8 + (h >> 1)) * 16 + (lane & 15)) * 8;
        WAIT_BAR(4); DMA_S(3);
        WAIT_BAR(4); DMA_S(4); DMA_S(5);
        WAIT_BAR(8); DMA_S(6);
        unsigned dm0;
        {
            const int li = (t & 1) * 56 + (lane < 56 ? lane : 55), c0 = (h & 3) < 3 ? (h & 3) * 256 : 640;
            asm volatile("global_load_ubyte %0, %1, %2" : "=v"(dm0) : "v"(((j & 7) * 56 + (li >> 1)) * 896 + (li & 1) * 128 + c0), "s"((t & 2) ? wob : woa) : "memory"); }
        f32x16 cbv, pbv;
        {   const unsigned long long M = (0x5555ull << (r32 + 18)) >> (4 * hi);
            int nlo = tile > 0 ? ~(unsigned)M : ~0u, nhi = ~(unsigned)(M >> 32);
#pragma unroll
            for (int r = 0; r < 16; ++r) { const int cc = (r & 3) + 8 * (r >> 2);
                int e0 = nhi << (31 - cc); PIN(e0); cbv[r] = __uint_as_float((unsigned)(e0 >> 8) & 0xFF800000u);
                if (r < 8) pbv[r] = 0.f; else { int e1 = nlo << (31 - cc); PIN(e1); pbv[r] = __uint_as_float((unsigned)(e1 >> 8) & 0xFF800000u); } }
            PIN(cbv); PIN(pbv); }
        WAIT_BAR(9);
        WAIT_BAR(0);
        asm volatile("" :: "v"(dm0));
        u64 pa;
        if (t == 0) { wait_flags_d(ws, fo, tag); lds_arrive(lds0 + L_CNT + 4, lane); } else lds_wait(lds0 + L_CNT + 4, 1);
        const char* kv0 = ws + WS_BR + BR_KV + (size_t)h * 262144 + t * 8192; const unsigned lr = lds0 + t * 8192;
#define KV4(p, LC) do { GLDSC(vl, p, lr, LC, 0); GLDSC(vl, p, lr, LC, 1024); GLDSC(vl, p, lr, LC, 2048); GLDSC(vl, p, lr, LC, 3072); } while (0)
        KV4(kv0, L_RING); KV4(kv0 + 4096, L_RING + 4096); KV4(kv0 + 32768, L_RING + 32768); KV4(kv0 + 36864, L_RING + 36864);
        WAIT_BAR(8);
        const int tt = 4 * j + (h >> 2), cg = h & 3, c = t;
        float glog = *(const LAS float*)(uintptr_t)(lds0 + L_GL + (h >> 2) * 128 + r32 * 4);
        const int col0 = cg < 3 ? cg * 128 : 320;
        const unsigned vwo = (lane >> 4) * 896 + (((lane & 15) ^ ((lane >> 4) << 2)) << 4), lc7 = lds0 + c * 7168;
        const char* wobp = wob + col0 * 2 + (size_t)(c * 7) * 3584; const char* woap = woa + col0 * 2 + (size_t)(c * 7) * 3584;
#define DWP(base, p, LC) do { if ((p) & 1) GLDS(vwo, (base) + (size_t)((p) - 1) * 3584, lc7, (LC) + 4096 + (p) * 1024 - 3584 - 4096, 3584); else GLDS(vwo, (base) + (size_t)(p) * 3584, lc7, (LC) + (p) * 1024, 0); } while (0)
#define DWQ(base, q, LC) do { const char* b_ = (base) + (size_t)(q) * 28 * 3584; DWP(b_, 0, LC); DWP(b_, 1, LC); DWP(b_, 2, LC); DWP(b_, 3, LC); DWP(b_, 4, LC); DWP(b_, 5, LC); DWP(b_, 6, LC); } while (0)
#define DWQ0(base, q) do { const char* b_ = (base) + (size_t)(q) * 28 * 3584; GLDS(vwo, b_, lc7, 0, 0); GLDS(vwo, b_ + 3584, lc7, 1024, 0); GLDS(vwo, b_ + 2 * 3584, lc7, 2048, 0); \
        GLDS(vwo, b_ + 3 * 3584, lc7, 3072, 0); DWP(b_, 4, 0); DWP(b_, 5, 0); DWP(b_, 6, 0); } while (0)
#define KVG(gr) do { const char* p_ = kv0 + (size_t)(gr) * 32768; KV4(p_, L_RING + ((gr) % 3) * 32768); KV4(p_ + 4096, L_RING + ((gr) % 3) * 32768 + 4096); } while (0)
        const int q4 = (lane & 15) >> 2, p4 = lane & 3, ch = (lane >> 4) & 1;
        const unsigned wl = lds0 + (4 * hi + q4) * 256 + (((c ^ q4) << 6) + ch * 32 + p4 * 8);
        unsigned wsa = wl + L_SA, wsk = wsa - (1024u & mhi); PIN(wsa); PIN(wsk);
        unsigned wz = wl, wzk = wl - (1024u & mhi); PIN(wz); PIN(wzk);
#define WFR(B, BK, off, k) wfrag((B) + (off) + ((k) >> 1) * 7168 + ((k) & 1) * 4096, ((k) == 7 ? (BK) : (B)) + (off) + ((k) >> 1) * 7168 + ((k) & 1) * 4096 + 2048)
        bf16x8 war[32];
#define WARQ(q, off) do { _Pragma("unroll") for (int k = 0; k < 8; ++k) war[8 * (q) + k] = WFR(wsa, wsk, off, k); } while (0)
        const unsigned bB = WS_BR + BR_SZ;
        WAIT_BAR(0);
        KVG(2); DWQ(woap, 0, L_SA);
        WAIT_BAR(0);
        if (tid == 256) set_flag(ws, WS_FB + (cb * 16 + j) * 8, tag);
        KVG(3); DWQ(woap, 1, L_SB); WARQ(0, 0);
        const unsigned fbo = WS_FB + (((16 * (lane % 3) + h) / 3) * 16 + ((lane & 4) && j > 0 ? j - 1 : j)) * 8;
        WAIT_BAR(0);
        KVG(4); DWQ(woap, 2, L_SA); WARQ(1, L_SB - L_SA);
        WAIT_BAR(0);
        asm volatile("global_load_dwordx2 %0, %1, %2 sc1" : "=v"(pa) : "v"(fbo), "s"(ws) : "memory");
        KVG(5); WARQ(2, 0);
        asm volatile("s_waitcnt vmcnt(8)" ::: "memory"); SBAR(); asm volatile("" : "+v"(pa) :: "memory");
        if (__builtin_expect(!__all(pa == tag), 0)) wait_flags_d(ws, fbo, tag);
        {   const char* qo = ws + bB + BR_Q + (size_t)(h * 64 + tile) * 2048; const char* ko = ws + bB + BR_KV + (size_t)(h * 64 + tile) * 4096;
            const unsigned lq2 = lds0 + t * 2048, lk2 = lds0 + t * 4096;
            GLDSC(vl, qo, lq2, L_SB + 20480, 0); GLDSC(vl, qo, lq2, L_SB + 20480, 1024);
            GLDSC(vl, ko, lk2, L_SB + 4096, 0); GLDSC(vl, ko, lk2, L_SB + 4096, 1024); GLDSC(vl, ko, lk2, L_SB + 4096, 2048); GLDSC(vl, ko, lk2, L_SB + 4096, 3072);
            if (t == 0) { const char* po = tile > 0 ? ko - 4096 : ko; GLDSC(vl, po, lds0, L_SB, 0); GLDSC(vl, po, lds0, L_SB, 1024); GLDSC(vl, po, lds0, L_SB, 3072); } }
        WAIT_BAR(0);
        {
            const unsigned bo = lds0 + L_SB + t * 4096 + vl, bq = lds0 + L_SB + 20480 + t * 2048 + vl;
            const bf16x8 bq0 = ldsr128(bq), bq1 = ldsr128(bq + 1024), bk0 = ldsr128(bo + 4096), bk1 = ldsr128(bo + 5120), bv0 = ldsr128(bo + 6144), bv1 = ldsr128(bo + 7168);
            const bf16x8 bp0 = ldsr128(bo), bp1 = ldsr128(bo + 1024), bpv = ldsr128(bo + 3072);
            KVG(6); DWQ(woap, 3, L_SA);
            __builtin_amdgcn_s_setprio(1);
            f32x16 sc = MFMA(bk0, bq0, cbv); sc = MFMA(bk1, bq1, sc);
            f32x16 sp = MFMA(bp0, bq0, pbv); sp = MFMA(bp1, bq1, sp);
            float m = max16(sc);
#pragma unroll
            for (int r = 8; r < 16; r += 2) m = __builtin_fmaxf(__builtin_fmaxf(m, sp[r]), sp[r + 1]);
            m = swap_max(m);
#pragma unroll
            for (int r = 0; r < 16; ++r) sc[r] = ex2(sc[r] - m);
#pragma unroll
            for (int r = 8; r < 16; ++r) sp[r] = ex2(sp[r] - m);
            u32x4 c0, c1, p0, p1; pack16(sc, c0, c1); pack16(sp, p0, p1);
            f32x16 ob = MFMA(bv0, asbf(c0), (f32x16){}); ob = MFMA(bv1, asbf(c1), ob); ob = MFMA(bpv, asbf(p1), ob);
            const float rl = __builtin_amdgcn_rcpf(from_hi(ob[12]));
#pragma unroll
            for (int i = 0; i < 16; ++i) ob[i] *= rl;
            u32x4 e0, e1; pack16(ob, e0, e1); fix_pad(e1, 0u, mhi);
            st16c(rs, e0, vl, bB + BR_O + (tile * 16 + h) * 2048); st16c(rs, e1, vl + 1024, bB + BR_O + (tile * 16 + h) * 2048);
            __builtin_amdgcn_s_setprio(0);
        }
        WAIT_BAR(2);
        KVG(7); DWQ(wobp, 0, L_SB); WARQ(3, 0);
        asm volatile("s_waitcnt vmcnt(15)" ::: "memory");
        if (lane == 0) set_flag(ws, WS_FOB + (tile * 16 + h) * 8, tag);
        const unsigned foo = WS_FOB + (tt * 16 + (lane & 15)) * 8;
        WAIT_BAR(1);
        DWQ(wobp, 1, L_SA);
        WAIT_BAR(0);
        asm volatile("global_load_dwordx2 %0, %1, %2 sc1" : "=v"(pa) : "v"(foo), "s"(ws) : "memory");
        DWQ0(wobp, 2);
        asm volatile("s_waitcnt vmcnt(7)" ::: "memory"); SBAR(); asm volatile("" : "+v"(pa) :: "memory");
        if (__builtin_expect(!__all(pa == tag), 0)) wait_flags_d(ws, foo, tag);
        {   const char* ofb = ws + bB + BR_O + (size_t)tt * 32768 + c * 8192; KV4(ofb, L_OF); KV4(ofb + 4096, L_OF + 4096); }
        unsigned oad = lds0 + L_OF + vl; PIN(oad);
        f32x16 accB = {};
        bf16x8 wf[4], of[4];
#define OPB(B, BK, off, kbase) do { \
        _Pragma("unroll") for (int i = 0; i < 4; ++i) { wf[i] = WFR(B, BK, off, i); of[i] = ldsr128(oad + ((kbase) + i) * 1024); } \
        _Pragma("unroll") for (int i = 0; i < 8; ++i) { SBAR(); accB = MFMA(wf[i & 3], of[i & 3], accB); SBAR(); \
            if (i < 4) { wf[i] = WFR(B, BK, off, i + 4); of[i] = ldsr128(oad + ((kbase) + i + 4) * 1024); } } SBAR(); } while (0)
        BAR_ONLY();
        WAIT_BAR(0);
        DWQ(wobp, 3, L_OA);
        OPB(wsa, wsk, L_SB - L_SA, 0); OPB(wsa, wsk, 0, 8); OPB(wz, wzk, 0, 16);
        if (tid == 256) *(LAS unsigned*)(uintptr_t)(lds0 + L_SCR) = 0u;
        WAIT_BAR(0);
        OPB(wz, wzk, L_OA, 24);
        float gate = __builtin_amdgcn_rcpf(1.0f + ex2(-1.4426950408889634f * glog)); const float g1 = 1.0f - gate;
#pragma unroll
        for (int i = 0; i < 16; ++i) accB[i] *= g1;
        unsigned oo = (tt * 32 + r32) * 896 + (col0 + 32 * c) * 2 + hi * 16;
        PIN(accB); PIN(gate); PIN(oo);
        if (t == 0) { wait_flags_d(ws, WS_FOA + (tt * 16 + (lane & 15)) * 8, tag); lds_arrive(lds0 + L_SCR, lane); } else lds_wait(lds0 + L_SCR, 1);
        {
            const char* ofa = ws + WS_BR + BR_O + (size_t)tt * 32768 + c * 4096; const unsigned lp = lds0 + c * 4096;
            GLDSC(vl, ofa, lp, 0, 0); GLDSC(vl, ofa, lp, 0, 1024); GLDSC(vl, ofa, lp, 0, 2048); GLDSC(vl, ofa, lp, 0, 3072);
            GLDSC(vl, ofa + 16384, lp, 16384, 0); GLDSC(vl, ofa + 16384, lp, 16384, 1024); GLDSC(vl, ofa + 16384, lp, 16384, 2048); GLDSC(vl, ofa + 16384, lp, 16384, 3072); }
        WAIT_BAR(4);
        f32x16 acc = {};
        {   unsigned ob_ = lds0 + vl; PIN(ob_);
            bf16x8 og[8];
#pragma unroll
            for (int i = 0; i < 8; ++i) og[i] = ldsr128(ob_ + i * 1024);
#pragma unroll
            for (int i = 0; i < 32; ++i) { if (i == 8) { WAIT_BAR(0); } SBAR(); acc = MFMA(war[i], og[i & 7], acc); SBAR(); if (i < 24) og[i & 7] = ldsr128(ob_ + (i + 8) * 1024); }
            SBAR();
        }
        if (bid == 0 && tid == 256) __hip_atomic_store((GAS u64*)(ws + WS_EPOCH), ep + 1, RLX_AGENT);
        if (cg < 3 || c >= 2) {
#pragma unroll
            for (int i = 0; i < 16; ++i) acc[i] = acc[i] * gate + accB[i];
            u32x4 s0_, s1_; pack16(acc, s0_, s1_);
            auto r0 = __builtin_amdgcn_permlane32_swap(s0_[0], s0_[2], false, false), r1 = __builtin_amdgcn_permlane32_swap(s0_[1], s0_[3], false, false);
            auto r2 = __builtin_amdgcn_permlane32_swap(s1_[0], s1_[2], false, false), r3 = __builtin_amdgcn_permlane32_swap(s1_[1], s1_[3], false, false);
            char* op = out + oo;
            *(GAS u32x4*)op = (u32x4){r0[0], r1[0], r0[1], r1[1]};
            *(GAS u32x4*)(op + 32) = (u32x4){r2[0], r3[0], r2[1], r3[1]};
        }
    }
    asm volatile(".Lfk_end:");
}

extern "C" void kernel_launch(void* const* d_in, const int* in_sizes, int n_in,
                              void* d_out, int out_size, void* d_ws, size_t ws_size,
                              hipStream_t stream) {
    (void)in_sizes; (void)n_in; (void)out_size; (void)ws_size;
    (void)hipFuncSetAttribute((const void*)fused_kernel, hipFuncAttributeMaxDynamicSharedMemorySize, LDS_BYTES);
    fused_kernel<<<256, 512, LDS_BYTES, stream>>>((const char*)d_in[0], (const char*)d_in[1], (const char*)d_in[2], (const char*)d_in[4],
                                                  (const char*)d_in[5], (const char*)d_in[6], (char*)d_ws, (char*)d_out);
}
```
